```python
import torch

def _softmax(x, axis=-1):
    return torch.softmax(x, dim=axis)

def _gelu(x):
    return torch.nn.functional.gelu(x, approximate='tanh')

def _silu(x):
    return torch.nn.functional.silu(x)

def _sigmoid(x):
    return torch.sigmoid(x)

def _layernorm(x, axis=-1, eps=1e-05):
    m = x.mean(dim=axis, keepdim=True)
    v = x.var(dim=axis, keepdim=True, unbiased=False)
    return (x - m) / torch.sqrt(v + eps)

def _scatter_add(tgt, idx, src, axis=0):
    return tgt.clone().index_add_(axis, idx, src)

def _segment_sum(src, seg, n):
    out = torch.zeros((n,) + src.shape[1:], dtype=src.dtype)
    return out.index_add_(0, seg, src)

def _pad(x, pw):
    flat = tuple((v for pair in reversed(pw) for v in pair))
    return torch.nn.functional.pad(x, flat)
kernel_name = 'adaLN_cond__gqa_block_cached__swiglu_ffn__decode'

def setup_inputs():

    _w = lambda a: (lambda t: t.to(torch.bfloat16) if t.is_floating_point() else t)(torch.as_tensor(a))

    def _A_setup():
        B, Sq, D = (1, 1, 640)
        Hq, Hkv, Sk, Dh = (32, 32, 16384, 128)
        assert Hq % Hkv == 0, (Hq, Hkv)
        return {'x': _w((lambda *s: torch.randn(*s, dtype=torch.bfloat16) * 0.05)(B, Sq, D)), 'k_cache': _w((lambda *s: torch.randn(*s, dtype=torch.bfloat16) * 0.8)(B, Hkv, Sk, Dh)), 'v_cache': _w((lambda *s: torch.randn(*s, dtype=torch.bfloat16) * 1.6)(B, Hkv, Sk, Dh)), 'wq': _w((lambda *s: torch.randn(*s, dtype=torch.bfloat16) * 0.8)(D, Hq * Dh) * D ** (-0.5)), 'wo': _w((lambda *s: torch.randn(*s, dtype=torch.bfloat16) * 1.6)(Hq * Dh, D) * (Hq * Dh) ** (-0.5))}
    _A_in = _A_setup()

    def _B_setup():
        B, S, D, F = (1, 1, 640, 1728)
        return {'x': _w((lambda *s: torch.randn(*s, dtype=torch.bfloat16) * 0.05)(B, S, D)), 'w_gate': _w((lambda *s: torch.randn(*s, dtype=torch.bfloat16) * 0.05)(D, F)), 'w_up': _w((lambda *s: torch.randn(*s, dtype=torch.bfloat16) * 0.05)(D, F)), 'w_down': _w((lambda *s: torch.randn(*s, dtype=torch.bfloat16) * 0.05)(F, D))}
    _B_in = _B_setup()
    return {'A_x': _A_in['x'], 'A_k_cache': _A_in['k_cache'], 'A_v_cache': _A_in['v_cache'], 'A_wq': _A_in['wq'], 'A_wo': _A_in['wo'], 'B_x': _B_in['x'], 'B_w_gate': _B_in['w_gate'], 'B_w_up': _B_in['w_up'], 'B_w_down': _B_in['w_down'], 'c': _w((lambda *s: torch.randn(*s, dtype=torch.bfloat16) * 0.2)(1, 640) * 8.0), 'w_mod': _w((lambda *s: torch.randn(*s, dtype=torch.bfloat16) * 0.2)(640, 6 * 640) * 640 ** (-0.5) * 8.0)}

def reference(A_x, A_k_cache, A_v_cache, A_wq, A_wo, B_x, B_w_gate, B_w_up, B_w_down, c, w_mod):

    def _A_ref(x, k_cache, v_cache, wq, wo):
        B, Sq, D = x.shape
        Hq, Dh = (32, 128)
        Hkv = k_cache.shape[1]
        rep = Hq // Hkv
        q = (lambda x, *d: x.permute(*d))((x @ wq).reshape(B, Sq, Hq, Dh), 0, 2, 1, 3)
        k = (lambda x, n, axis: torch.repeat_interleave(x, n, dim=axis))(k_cache, rep, axis=1)
        v = (lambda x, n, axis: torch.repeat_interleave(x, n, dim=axis))(v_cache, rep, axis=1)
        scores = torch.einsum('bhqd,bhkd->bhqk', q, k) * Dh ** (-0.5)
        p = _softmax(scores, axis=-1)
        o = torch.einsum('bhqk,bhkd->bhqd', p, v)
        return (lambda x, *d: x.permute(*d))(o, 0, 2, 1, 3).reshape(B, Sq, Hq * Dh) @ wo

    def _B_ref(x, w_gate, w_up, w_down):
        g = _silu(x @ w_gate)
        u = x @ w_up
        return g * u @ w_down
    x = A_x
    mod = (_silu(c) @ w_mod).reshape(c.shape[0], 6, 1, -1)
    sa, ca, ga, sb, cb, gb = (mod[:, 0].clone(), mod[:, 1].clone(), mod[:, 2].clone(), mod[:, 3].clone(), mod[:, 4].clone(), mod[:, 5].clone())
    h = _layernorm(x, axis=-1) * (1 + ca) + sa
    x = x + ga * _A_ref(h, A_k_cache, A_v_cache, A_wq, A_wo)
    h = _layernorm(x, axis=-1) * (1 + cb) + sb
    x = x + gb * _B_ref(h, B_w_gate, B_w_up, B_w_down)
    return x
```

```cpp
#include <hip/hip_runtime.h>
#include <hip/hip_bf16.h>
#include <stdint.h>

typedef unsigned short u16;
typedef unsigned v4u __attribute__((ext_vector_type(4)));
typedef float f32x4 __attribute__((ext_vector_type(4)));
typedef __bf16 bf16x2 __attribute__((ext_vector_type(2)));

constexpr int D = 640, H = 32, SK = 16384, DH = 128, F = 1728;
constexpr int NSPLIT = 8;

__device__ __forceinline__ float bf2f(unsigned b) { return __builtin_bit_cast(float, b << 16); }
__device__ __forceinline__ unsigned f2bf(float f) { unsigned u = __builtin_bit_cast(unsigned, f); return (u + 0x7fffu + ((u >> 16) & 1u)) >> 16; }
__device__ __forceinline__ float rbf(float f) { return bf2f(f2bf(f)); }
__device__ __forceinline__ float dot2(unsigned k, unsigned q, float acc) {
  return __builtin_amdgcn_fdot2_f32_bf16(__builtin_bit_cast(bf16x2, k), __builtin_bit_cast(bf16x2, q), acc, false);
}
__device__ __forceinline__ float wsum(float v) { for (int o = 32; o; o >>= 1) v += __shfl_xor(v, o); return v; }
__device__ __forceinline__ float wmax(float v) { for (int o = 32; o; o >>= 1) v = fmaxf(v, __shfl_xor(v, o)); return v; }

template <int K, int N>
__device__ __forceinline__ void gemv64(const float* in, const u16* W, int n0, float* red, float* out64) {
  int tid = threadIdx.x, c = tid & 7, r = tid >> 3;
  float acc[8] = {0, 0, 0, 0, 0, 0, 0, 0};
  for (int k = r; k < K; k += 32) {
    v4u w = *(const v4u*)(W + (size_t)k * N + n0 + 8 * c);
    float a = in[k];
    for (int j = 0; j < 4; ++j) { acc[2 * j] += a * bf2f(w[j] & 0xffff); acc[2 * j + 1] += a * bf2f(w[j] >> 16); }
  }
  for (int j = 0; j < 8; ++j) red[r * 64 + c * 8 + j] = acc[j];
  __syncthreads();
  if (tid < 64) { float s = 0; for (int rr = 0; rr < 32; ++rr) s += red[rr * 64 + tid]; out64[tid] = s; }
  __syncthreads();
}

__global__ void __launch_bounds__(256) k_mod(const u16* c, const u16* wmod, float* mod) {
  __shared__ float s[D], red[32 * 64], o[64];
  for (int i = threadIdx.x; i < D; i += 256) { float v = bf2f(c[i]); s[i] = rbf(v / (1.f + __expf(-v))); }
  __syncthreads();
  gemv64<D, 6 * D>(s, wmod, blockIdx.x * 64, red, o);
  if (threadIdx.x < 64) mod[blockIdx.x * 64 + threadIdx.x] = rbf(o[threadIdx.x]);
}

__device__ __forceinline__ void ln_mod(const float* x, const float* scale, const float* shift, float* h, float* red) {
  int tid = threadIdx.x;
  float s = 0; for (int i = tid; i < D; i += 256) s += x[i];
  s = wsum(s); if ((tid & 63) == 0) red[tid >> 6] = s; __syncthreads();
  float mean = (red[0] + red[1] + red[2] + red[3]) / D; __syncthreads();
  float v = 0; for (int i = tid; i < D; i += 256) { float d = x[i] - mean; v += d * d; }
  v = wsum(v); if ((tid & 63) == 0) red[tid >> 6] = v; __syncthreads();
  float var = (red[0] + red[1] + red[2] + red[3]) / D; __syncthreads();
  float mb = rbf(mean), sd = rbf(sqrtf(rbf(rbf(var) + 1e-5f)));
  for (int i = tid; i < D; i += 256) { float n = rbf(rbf(x[i] - mb) / sd); h[i] = rbf(rbf(n * rbf(1.f + scale[i])) + shift[i]); }
  __syncthreads();
}

__global__ void __launch_bounds__(256) k_q(const u16* x, const float* mod, const u16* wq, float* q) {
  __shared__ float xs[D], h[D], red[32 * 64], o[64];
  for (int i = threadIdx.x; i < D; i += 256) xs[i] = bf2f(x[i]);
  __syncthreads();
  ln_mod(xs, mod + D, mod, h, red);
  gemv64<D, H * DH>(h, wq, blockIdx.x * 64, red, o);
  if (threadIdx.x < 64) q[blockIdx.x * 64 + threadIdx.x] = rbf(o[threadIdx.x]);
}

__global__ void __launch_bounds__(512) k_attn(const float* q, const u16* K, const u16* V, float* part) {
  __shared__ float sm[8][132];
  int b = blockIdx.x, h = b / NSPLIT, s = b % NSPLIT;
  int tid = threadIdx.x, lane = tid & 63, w = tid >> 6, r = lane >> 4, c = lane & 15;
  v4u qp;
  for (int j = 0; j < 4; ++j) qp[j] = f2bf(q[h * DH + 8 * c + 2 * j]) | (f2bf(q[h * DH + 8 * c + 2 * j + 1]) << 16);
  size_t base = ((size_t)h * SK + s * 2048 + w * 256) * DH + r * DH + 8 * c;
  const v4u* kp = (const v4u*)(K + base);
  const v4u* vp = (const v4u*)(V + base);
  float sc[4];
#pragma unroll
  for (int g = 0; g < 4; ++g) {
    float x[16];
#pragma unroll
    for (int t = 0; t < 16; ++t) { v4u k = __builtin_nontemporal_load(kp + (g * 16 + t) * 64); float d = 0; for (int j = 0; j < 4; ++j) d = dot2(k[j], qp[j], d); x[t] = d; }
    float mine = 0;
#pragma unroll
    for (int t = 0; t < 16; ++t) { float d = x[t]; d += __shfl_xor(d, 1); d += __shfl_xor(d, 2); d += __shfl_xor(d, 4); d += __shfl_xor(d, 8); if (c == t) mine = d; }
    sc[g] = rbf(rbf(mine) * 0.08838834764831845f);
  }
  float m = wmax(fmaxf(fmaxf(sc[0], sc[1]), fmaxf(sc[2], sc[3])));
  float l = 0, p[4];
  for (int g = 0; g < 4; ++g) { p[g] = __expf(sc[g] - m); l += p[g]; }
  l = wsum(l);
  float acc[8] = {0, 0, 0, 0, 0, 0, 0, 0};
#pragma unroll
  for (int g = 0; g < 4; ++g)
#pragma unroll
    for (int t = 0; t < 16; ++t) {
      v4u v = __builtin_nontemporal_load(vp + (g * 16 + t) * 64);
      float pp = __shfl(p[g], (lane & 48) | t);
      for (int j = 0; j < 4; ++j) { acc[2 * j] += pp * bf2f(v[j] & 0xffff); acc[2 * j + 1] += pp * bf2f(v[j] >> 16); }
    }
  for (int j = 0; j < 8; ++j) { acc[j] += __shfl_xor(acc[j], 16); acc[j] += __shfl_xor(acc[j], 32); }
  if (r == 0) for (int j = 0; j < 8; ++j) sm[w][4 + 8 * c + j] = acc[j];
  if (lane == 0) { sm[w][0] = m; sm[w][1] = l; }
  __syncthreads();
  if (tid < 128) {
    float M = sm[0][0]; for (int i = 1; i < 8; ++i) M = fmaxf(M, sm[i][0]);
    float L = 0, o = 0;
    for (int i = 0; i < 8; ++i) { float wt = __expf(sm[i][0] - M); L += wt * sm[i][1]; o += wt * sm[i][4 + tid]; }
    part[(size_t)b * 132 + 4 + tid] = o;
    if (tid == 0) { part[(size_t)b * 132] = M; part[(size_t)b * 132 + 1] = L; }
  }
}

__global__ void __launch_bounds__(256) k_wo(const float* part, const u16* wo, const u16* x, const float* mod, float* x1) {
  __shared__ float o[H * DH], red[32 * 64], y[64];
  for (int i = threadIdx.x; i < H * DH; i += 256) {
    int h = i >> 7, d = i & 127;
    float M = -INFINITY; for (int s = 0; s < NSPLIT; ++s) M = fmaxf(M, part[(h * NSPLIT + s) * 132]);
    float L = 0, a = 0;
    for (int s = 0; s < NSPLIT; ++s) { const float* pp = part + (h * NSPLIT + s) * 132; float wt = __expf(pp[0] - M); L += wt * pp[1]; a += wt * pp[4 + d]; }
    o[i] = rbf(a / L);
  }
  __syncthreads();
  gemv64<H * DH, D>(o, wo, blockIdx.x * 64, red, y);
  if (threadIdx.x < 64) { int n = blockIdx.x * 64 + threadIdx.x; x1[n] = rbf(bf2f(x[n]) + rbf(mod[2 * D + n] * rbf(y[threadIdx.x]))); }
}

__global__ void __launch_bounds__(256) k_ffn1(const float* x1, const float* mod, const u16* wg, const u16* wu, float* act) {
  __shared__ float xs[D], h[D], red[32 * 64], g[64], u[64];
  for (int i = threadIdx.x; i < D; i += 256) xs[i] = x1[i];
  __syncthreads();
  ln_mod(xs, mod + 4 * D, mod + 3 * D, h, red);
  gemv64<D, F>(h, wg, blockIdx.x * 64, red, g);
  gemv64<D, F>(h, wu, blockIdx.x * 64, red, u);
  if (threadIdx.x < 64) { float gg = rbf(g[threadIdx.x]); float sg = rbf(gg / (1.f + __expf(-gg))); act[blockIdx.x * 64 + threadIdx.x] = rbf(sg * rbf(u[threadIdx.x])); }
}

__global__ void __launch_bounds__(256) k_ffn2(const float* act, const float* x1, const float* mod, const u16* wd, u16* out) {
  __shared__ float a[F], red[32 * 64], y[64];
  for (int i = threadIdx.x; i < F; i += 256) a[i] = act[i];
  __syncthreads();
  gemv64<F, D>(a, wd, blockIdx.x * 64, red, y);
  if (threadIdx.x < 64) { int n = blockIdx.x * 64 + threadIdx.x; out[n] = (u16)f2bf(x1[n] + rbf(mod[5 * D + n] * rbf(y[threadIdx.x]))); }
}

extern "C" void kernel_launch(void* const* d_in, const int* in_sizes, int n_in, void* d_out, int out_size, void* d_ws, size_t ws_size,
                              hipStream_t stream) {
  const u16* x = (const u16*)d_in[0]; const u16* K = (const u16*)d_in[1]; const u16* V = (const u16*)d_in[2];
  const u16* wq = (const u16*)d_in[3]; const u16* wo = (const u16*)d_in[4];
  const u16* wg = (const u16*)d_in[6]; const u16* wu = (const u16*)d_in[7]; const u16* wd = (const u16*)d_in[8];
  const u16* c = (const u16*)d_in[9]; const u16* wmod = (const u16*)d_in[10];
  float* ws = (float*)d_ws;
  float* mod = ws; float* q = ws + 4096; float* part = ws + 8192; float* x1 = ws + 8192 + 256 * 132; float* act = x1 + 1024;
  k_mod<<<60, 256, 0, stream>>>(c, wmod, mod);
  k_q<<<64, 256, 0, stream>>>(x, mod, wq, q);
  k_attn<<<H * NSPLIT, 512, 0, stream>>>(q, K, V, part);
  k_wo<<<10, 256, 0, stream>>>(part, wo, x, mod, x1);
  k_ffn1<<<27, 256, 0, stream>>>(x1, mod, wg, wu, act);
  k_ffn2<<<10, 256, 0, stream>>>(act, x1, mod, wd, (u16*)d_out);
}
```

```cpp
#define NW 4
#define WIN 16
#define AUXK 2
#define AUXV 2
#include <hip/hip_runtime.h>
#include <hip/hip_bf16.h>
#include <stdint.h>

typedef unsigned short u16;
typedef unsigned v4u __attribute__((ext_vector_type(4)));
typedef float f32x4 __attribute__((ext_vector_type(4)));
typedef __bf16 bf16x2 __attribute__((ext_vector_type(2)));

constexpr int D = 640, H = 32, SK = 16384, DH = 128, F = 1728;
constexpr int NSPLIT = 8;

__device__ __forceinline__ float bf2f(unsigned b) { return __builtin_bit_cast(float, b << 16); }
__device__ __forceinline__ unsigned f2bf(float f) { unsigned u = __builtin_bit_cast(unsigned, f); return (u + 0x7fffu + ((u >> 16) & 1u)) >> 16; }
__device__ __forceinline__ float rbf(float f) { return bf2f(f2bf(f)); }
__device__ __forceinline__ float dot2(unsigned k, unsigned q, float acc) {
  return __builtin_amdgcn_fdot2_f32_bf16(__builtin_bit_cast(bf16x2, k), __builtin_bit_cast(bf16x2, q), acc, false);
}
__device__ __forceinline__ float wsum(float v) { for (int o = 32; o; o >>= 1) v += __shfl_xor(v, o); return v; }
__device__ __forceinline__ float wmax(float v) { for (int o = 32; o; o >>= 1) v = fmaxf(v, __shfl_xor(v, o)); return v; }

template <int K, int N>
__device__ __forceinline__ void gemv64(const float* in, const u16* W, int n0, float* red, float* out64) {
  int tid = threadIdx.x, c = tid & 7, r = tid >> 3;
  float acc[8] = {0, 0, 0, 0, 0, 0, 0, 0};
  for (int k = r; k < K; k += 32) {
    v4u w = *(const v4u*)(W + (size_t)k * N + n0 + 8 * c);
    float a = in[k];
    for (int j = 0; j < 4; ++j) { acc[2 * j] += a * bf2f(w[j] & 0xffff); acc[2 * j + 1] += a * bf2f(w[j] >> 16); }
  }
  for (int j = 0; j < 8; ++j) red[r * 64 + c * 8 + j] = acc[j];
  __syncthreads();
  if (tid < 64) { float s = 0; for (int rr = 0; rr < 32; ++rr) s += red[rr * 64 + tid]; out64[tid] = s; }
  __syncthreads();
}

__global__ void __launch_bounds__(256) k_mod(const u16* c, const u16* wmod, float* mod) {
  __shared__ float s[D], red[32 * 64], o[64];
  for (int i = threadIdx.x; i < D; i += 256) { float v = bf2f(c[i]); s[i] = rbf(v / (1.f + __expf(-v))); }
  __syncthreads();
  gemv64<D, 6 * D>(s, wmod, blockIdx.x * 64, red, o);
  if (threadIdx.x < 64) mod[blockIdx.x * 64 + threadIdx.x] = rbf(o[threadIdx.x]);
}

__device__ __forceinline__ void ln_mod(const float* x, const float* scale, const float* shift, float* h, float* red) {
  int tid = threadIdx.x;
  float s = 0; for (int i = tid; i < D; i += 256) s += x[i];
  s = wsum(s); if ((tid & 63) == 0) red[tid >> 6] = s; __syncthreads();
  float mean = (red[0] + red[1] + red[2] + red[3]) / D; __syncthreads();
  float v = 0; for (int i = tid; i < D; i += 256) { float d = x[i] - mean; v += d * d; }
  v = wsum(v); if ((tid & 63) == 0) red[tid >> 6] = v; __syncthreads();
  float var = (red[0] + red[1] + red[2] + red[3]) / D; __syncthreads();
  float mb = rbf(mean), sd = rbf(sqrtf(rbf(rbf(var) + 1e-5f)));
  for (int i = tid; i < D; i += 256) { float n = rbf(rbf(x[i] - mb) / sd); h[i] = rbf(rbf(n * rbf(1.f + scale[i])) + shift[i]); }
  __syncthreads();
}

__global__ void __launch_bounds__(256) k_q(const u16* x, const float* mod, const u16* wq, float* q) {
  __shared__ float xs[D], h[D], red[32 * 64], o[64];
  for (int i = threadIdx.x; i < D; i += 256) xs[i] = bf2f(x[i]);
  __syncthreads();
  ln_mod(xs, mod + D, mod, h, red);
  gemv64<D, H * DH>(h, wq, blockIdx.x * 64, red, o);
  if (threadIdx.x < 64) q[blockIdx.x * 64 + threadIdx.x] = rbf(o[threadIdx.x]);
}

template <int CTRL> __device__ __forceinline__ float dpp(float x) {
  return __builtin_bit_cast(float, __builtin_amdgcn_mov_dpp(__builtin_bit_cast(int, x), CTRL, 0xf, 0xf, true));
}
constexpr int XOR1 = 0xB1, XOR2 = 0x4E, XOR7 = 0x141, XOR8 = 0x128, ROR4 = 0x124;
constexpr int KPW = 2048 / NW, NL = KPW / 4;
__global__ void __launch_bounds__(NW * 64, 1) k_attn(const float* q, const u16* K, const u16* V, float* part) {
  __shared__ __attribute__((aligned(16))) float sp[NW][4][NL];
  __shared__ float sm[NW][132];
  const int b = blockIdx.x, h = b / NSPLIT, s = b % NSPLIT;
  const int tid = threadIdx.x, lane = tid & 63, w = __builtin_amdgcn_readfirstlane(tid >> 6), r = lane >> 4, c = lane & 15;
  const __amdgpu_buffer_rsrc_t rk = __builtin_amdgcn_make_buffer_rsrc((void*)K, 0, 128 << 20, 0x00020000);
  const __amdgpu_buffer_rsrc_t rv = __builtin_amdgcn_make_buffer_rsrc((void*)V, 0, 128 << 20, 0x00020000);
  const __amdgpu_buffer_rsrc_t rz = __builtin_amdgcn_make_buffer_rsrc((void*)V, 0, 0, 0x00020000);
  const int sbase = (h * SK + s * 2048 + w * KPW) * 256;
  const int voff = lane * 16;
  const f32x4 q0 = *(const f32x4*)(q + h * DH + 8 * c), q1 = *(const f32x4*)(q + h * DH + 8 * c + 4);
  __builtin_amdgcn_sched_barrier(0);
  v4u a[WIN];
#pragma unroll
  for (int i = 0; i < WIN; ++i) a[i] = __builtin_amdgcn_raw_buffer_load_b128(rk, voff, sbase + i * 1024, AUXK);
  __builtin_amdgcn_sched_barrier(0);
  v4u qp;
  qp[0] = f2bf(q0[0]) | (f2bf(q0[1]) << 16); qp[1] = f2bf(q0[2]) | (f2bf(q0[3]) << 16);
  qp[2] = f2bf(q1[0]) | (f2bf(q1[1]) << 16); qp[3] = f2bf(q1[2]) | (f2bf(q1[3]) << 16);
  float mx = -INFINITY;
  constexpr int NIT = NL / WIN;
#pragma unroll 1
  for (int it = 0; it < NIT; ++it) {
    const bool lastk = it == NIT - 1;
    const int so = lastk ? sbase : sbase + (it + 1) * WIN * 1024;
    const __amdgpu_buffer_rsrc_t rr = lastk ? rv : rk;
#pragma unroll
    for (int g = 0; g < WIN / 16; ++g) {
      float x[16];
#pragma unroll
      for (int t = 0; t < 16; ++t) {
        const int j = g * 16 + t;
        float d = 0;
#pragma unroll
        for (int jj = 0; jj < 4; ++jj) d = dot2(a[j][jj], qp[jj], d);
        x[t] = d;
        __builtin_amdgcn_sched_barrier(0);
        a[j] = __builtin_amdgcn_raw_buffer_load_b128(rr, voff, so + j * 1024, AUXK);
        __builtin_amdgcn_sched_barrier(0);
      }
#define TR_STEP(HALF, CTRL, BIT) _Pragma("unroll") for (int hh = 0; hh < (HALF); ++hh) { \
      const float lo_ = x[hh], hi_ = x[hh + (HALF)]; const float t1_ = lo_ + dpp<CTRL>(lo_), t2_ = hi_ + dpp<CTRL>(hi_); x[hh] = (c & (BIT)) ? t2_ : t1_; }
      TR_STEP(8, XOR8, 8) TR_STEP(4, XOR7, 4) TR_STEP(2, XOR2, 2) TR_STEP(1, XOR1, 1)
#undef TR_STEP
      const float sc = rbf(rbf(x[0]) * 0.08838834764831845f);
      mx = fmaxf(mx, sc);
      sp[w][r][it * WIN + g * 16 + c] = sc;
    }
  }
  mx = wmax(mx);
  float l = 0;
#pragma unroll
  for (int g = 0; g < NL / 16; ++g) { float p = __expf(sp[w][r][g * 16 + c] - mx); l += p; sp[w][r][g * 16 + c] = p; }
  l = wsum(l);
  float acc[8] = {0, 0, 0, 0, 0, 0, 0, 0};
#pragma unroll 1
  for (int it = 0; it < NIT; ++it) {
    const bool lastv = it == NIT - 1;
    const __amdgpu_buffer_rsrc_t rr = lastv ? rz : rv;
    const int so = sbase + (it + 1) * WIN * 1024;
#pragma unroll
    for (int g = 0; g < WIN / 4; ++g) {
      const f32x4 p4 = *(const f32x4*)&sp[w][r][it * WIN + g * 4];
#pragma unroll
      for (int t = 0; t < 4; ++t) {
        const int j = g * 4 + t;
        const float pp = p4[t];
#pragma unroll
        for (int jj = 0; jj < 4; ++jj) { acc[2 * jj] += pp * __builtin_bit_cast(float, a[j][jj] << 16); acc[2 * jj + 1] += pp * __builtin_bit_cast(float, a[j][jj] & 0xffff0000u); }
        asm volatile("" : "+v"(acc[0]), "+v"(acc[1]), "+v"(acc[2]), "+v"(acc[3]), "+v"(acc[4]), "+v"(acc[5]), "+v"(acc[6]), "+v"(acc[7]));
        __builtin_amdgcn_sched_barrier(0);
        a[j] = __builtin_amdgcn_raw_buffer_load_b128(rr, voff, so + j * 1024, AUXV);
        __builtin_amdgcn_sched_barrier(0);
      }
    }
  }
  for (int j = 0; j < 8; ++j) { acc[j] += __shfl_xor(acc[j], 16); acc[j] += __shfl_xor(acc[j], 32); }
  if (r == 0) for (int j = 0; j < 8; ++j) sm[w][4 + 8 * c + j] = acc[j];
  if (lane == 0) { sm[w][0] = mx; sm[w][1] = l; }
  __syncthreads();
  if (tid < 128) {
    float M = sm[0][0]; for (int i = 1; i < NW; ++i) M = fmaxf(M, sm[i][0]);
    float L = 0, o = 0;
    for (int i = 0; i < NW; ++i) { float wt = __expf(sm[i][0] - M); L += wt * sm[i][1]; o += wt * sm[i][4 + tid]; }
    part[(size_t)b * 132 + 4 + tid] = o;
    if (tid == 0) { part[(size_t)b * 132] = M; part[(size_t)b * 132 + 1] = L; }
  }
}

__global__ void __launch_bounds__(256) k_wo(const float* part, const u16* wo, const u16* x, const float* mod, float* x1) {
  __shared__ float o[H * DH], red[32 * 64], y[64];
  for (int i = threadIdx.x; i < H * DH; i += 256) {
    int h = i >> 7, d = i & 127;
    float M = -INFINITY; for (int s = 0; s < NSPLIT; ++s) M = fmaxf(M, part[(h * NSPLIT + s) * 132]);
    float L = 0, a = 0;
    for (int s = 0; s < NSPLIT; ++s) { const float* pp = part + (h * NSPLIT + s) * 132; float wt = __expf(pp[0] - M); L += wt * pp[1]; a += wt * pp[4 + d]; }
    o[i] = rbf(a / L);
  }
  __syncthreads();
  gemv64<H * DH, D>(o, wo, blockIdx.x * 64, red, y);
  if (threadIdx.x < 64) { int n = blockIdx.x * 64 + threadIdx.x; x1[n] = rbf(bf2f(x[n]) + rbf(mod[2 * D + n] * rbf(y[threadIdx.x]))); }
}

__global__ void __launch_bounds__(256) k_ffn1(const float* x1, const float* mod, const u16* wg, const u16* wu, float* act) {
  __shared__ float xs[D], h[D], red[32 * 64], g[64], u[64];
  for (int i = threadIdx.x; i < D; i += 256) xs[i] = x1[i];
  __syncthreads();
  ln_mod(xs, mod + 4 * D, mod + 3 * D, h, red);
  gemv64<D, F>(h, wg, blockIdx.x * 64, red, g);
  gemv64<D, F>(h, wu, blockIdx.x * 64, red, u);
  if (threadIdx.x < 64) { float gg = rbf(g[threadIdx.x]); float sg = rbf(gg / (1.f + __expf(-gg))); act[blockIdx.x * 64 + threadIdx.x] = rbf(sg * rbf(u[threadIdx.x])); }
}

__global__ void __launch_bounds__(256) k_ffn2(const float* act, const float* x1, const float* mod, const u16* wd, u16* out) {
  __shared__ float a[F], red[32 * 64], y[64];
  for (int i = threadIdx.x; i < F; i += 256) a[i] = act[i];
  __syncthreads();
  gemv64<F, D>(a, wd, blockIdx.x * 64, red, y);
  if (threadIdx.x < 64) { int n = blockIdx.x * 64 + threadIdx.x; out[n] = (u16)f2bf(x1[n] + rbf(mod[5 * D + n] * rbf(y[threadIdx.x]))); }
}

extern "C" void kernel_launch(void* const* d_in, const int* in_sizes, int n_in, void* d_out, int out_size, void* d_ws, size_t ws_size,
                              hipStream_t stream) {
  const u16* x = (const u16*)d_in[0]; const u16* K = (const u16*)d_in[1]; const u16* V = (const u16*)d_in[2];
  const u16* wq = (const u16*)d_in[3]; const u16* wo = (const u16*)d_in[4];
  const u16* wg = (const u16*)d_in[6]; const u16* wu = (const u16*)d_in[7]; const u16* wd = (const u16*)d_in[8];
  const u16* c = (const u16*)d_in[9]; const u16* wmod = (const u16*)d_in[10];
  float* ws = (float*)d_ws;
  float* mod = ws; float* q = ws + 4096; float* part = ws + 8192; float* x1 = ws + 8192 + 256 * 132; float* act = x1 + 1024;
  k_mod<<<60, 256, 0, stream>>>(c, wmod, mod);
  k_q<<<64, 256, 0, stream>>>(x, mod, wq, q);
  hipFuncSetAttribute((const void*)k_attn, hipFuncAttributeMaxDynamicSharedMemorySize, 84 * 1024);
  k_attn<<<H * NSPLIT, NW * 64, 84 * 1024, stream>>>(q, K, V, part);
  k_wo<<<10, 256, 0, stream>>>(part, wo, x, mod, x1);
  k_ffn1<<<27, 256, 0, stream>>>(x1, mod, wg, wu, act);
  k_ffn2<<<10, 256, 0, stream>>>(act, x1, mod, wd, (u16*)d_out);
}
```

```cpp
#include <hip/hip_runtime.h>
#include <stdint.h>

typedef unsigned short u16;
typedef unsigned v4u __attribute__((ext_vector_type(4)));
typedef unsigned v2u __attribute__((ext_vector_type(2)));
typedef float f32x4 __attribute__((ext_vector_type(4)));
typedef __bf16 bf16x2 __attribute__((ext_vector_type(2)));
typedef __amdgpu_buffer_rsrc_t rsrc_t;
#define LAS __attribute__((address_space(3)))
#define DI __device__ __forceinline__

#define WIN 32
#define AUXKV 2
#define DMA_PRE 12
#define RH 4
#define RM 4
#define SPIN_CAP (1 << 16)

constexpr int D = 640, SK = 16384, F = 1728;
constexpr int OFF_H = 256;
constexpr int OFF_Q = OFF_H + RH * 2560;
constexpr int OFF_M = OFF_Q + 32 * 512;
constexpr int PSTR = 72 * 16;
constexpr int OFF_P = OFF_M + RM * 10240;
constexpr int OFF_Y = OFF_P + 256 * PSTR;
constexpr int OFF_Z = OFF_Y + 8 * 5120;
constexpr int WS_BYTES = OFF_Z + 54 * 5120;
constexpr int OFF_STAMP = 4 << 20;
constexpr int NLMAX = 160;
constexpr int L_SP = 0;
constexpr int L_RED = L_SP + 16 * NLMAX * 4;
constexpr int L_X1 = L_RED + 4096;
constexpr int L_CB = L_X1 + 2560;
constexpr int L_SB = L_CB + 2560;
constexpr int L_HW = L_SB + 2560;
constexpr int L_W = L_HW + 4 * 768;
constexpr int L_DUMMY = L_W + 120 * 1024;
constexpr int LDS_BYTES = L_DUMMY + 1024;
static_assert(L_W % 16 == 0 && LDS_BYTES <= 160 * 1024, "lds");

DI float asf(unsigned u) { return __builtin_bit_cast(float, u); }
DI unsigned asu(float f) { return __builtin_bit_cast(unsigned, f); }
DI float lo16(unsigned w) { return asf(w << 16); }
DI float hi16(unsigned w) { return asf(w & 0xffff0000u); }
DI unsigned f2bf(float f) { unsigned u = asu(f); return (u + 0x7fffu + ((u >> 16) & 1u)) >> 16; }
DI float rbf(float f) { unsigned u = asu(f); return asf((u + 0x7fffu + ((u >> 16) & 1u)) & 0xffff0000u); }
DI unsigned pack2(float a, float b) { return f2bf(a) | (f2bf(b) << 16); }
DI float dot2(unsigned k, unsigned q, float acc) {
  return __builtin_amdgcn_fdot2_f32_bf16(__builtin_bit_cast(bf16x2, k), __builtin_bit_cast(bf16x2, q), acc, false);
}
template <int CTRL> DI float dpp(float x) { return asf(__builtin_amdgcn_mov_dpp(asu(x), CTRL, 0xf, 0xf, true)); }
constexpr int XOR1 = 0xB1, XOR2 = 0x4E, XOR7 = 0x141, XOR8 = 0x128, ROR4 = 0x124, SHL4 = 0x104;
DI float tr32(float a, float b) { v2u r = __builtin_amdgcn_permlane32_swap(asu(a), asu(b), false, false); unsigned p = r[0], q = r[1]; return asf(p) + asf(q); }
DI float tr16(float a, float b) { v2u r = __builtin_amdgcn_permlane16_swap(asu(a), asu(b), false, false); unsigned p = r[0], q = r[1]; return asf(p) + asf(q); }
DI float tr8(float a, float b, bool hi) { const float t1 = a + dpp<XOR8>(a), t2 = b + dpp<XOR8>(b); return hi ? t2 : t1; }
DI float wsum(float v) {
  v += dpp<XOR1>(v); v += dpp<XOR2>(v); v += dpp<XOR7>(v); v += dpp<XOR8>(v); v = tr16(v, v); return tr32(v, v);
}
DI float mx16(float a) { v2u r = __builtin_amdgcn_permlane16_swap(asu(a), asu(a), false, false); unsigned p = r[0], q = r[1]; return fmaxf(asf(p), asf(q)); }
DI float mx32(float a) { v2u r = __builtin_amdgcn_permlane32_swap(asu(a), asu(a), false, false); unsigned p = r[0], q = r[1]; return fmaxf(asf(p), asf(q)); }
DI float wmax(float v) {
  v = fmaxf(v, dpp<XOR1>(v)); v = fmaxf(v, dpp<XOR2>(v)); v = fmaxf(v, dpp<XOR7>(v)); v = fmaxf(v, dpp<XOR8>(v)); return mx32(mx16(v));
}
DI float silu(float v) { return v * __builtin_amdgcn_rcpf(1.f + __expf(-v)); }
DI unsigned plo(unsigned a, unsigned b) { return __builtin_amdgcn_perm(b, a, 0x05040100u); }
DI unsigned phi(unsigned a, unsigned b) { return __builtin_amdgcn_perm(b, a, 0x07060302u); }
struct P8 { unsigned p[8]; };
DI P8 mkpairs(v4u a, v4u b) {
  P8 r;
#pragma unroll
  for (int i = 0; i < 4; ++i) { const unsigned x = a[i], y = b[i]; r.p[2 * i] = plo(x, y); r.p[2 * i + 1] = phi(x, y); }
  return r;
}
DI rsrc_t mkrsrc(const void* p, unsigned bytes) { return __builtin_amdgcn_make_buffer_rsrc((void*)p, 0, bytes, 0x00020000); }
DI v4u ldg(rsrc_t r, int off) { return __builtin_amdgcn_raw_buffer_load_b128(r, off, 0, 16); }
DI void stg(rsrc_t r, int off, unsigned a, unsigned b, unsigned tag) { v4u g = {a, tag, b, tag}; __builtin_amdgcn_raw_buffer_store_b128(g, r, off, 0, 16); }
DI bool okg(v4u g, unsigned tag) { const unsigned a = g[1], b = g[3]; return a == tag && b == tag; }
DI void fence_c() { asm volatile("" ::: "memory"); }
DI void dma16(rsrc_t r, unsigned voff, unsigned soff, unsigned lds) {
  unsigned keep;
  asm volatile("s_mov_b32 %0, m0\n\ts_mov_b32 m0, %1\n\ts_nop 0\n\tbuffer_load_dwordx4 %2, %3, %4 offen lds\n\ts_mov_b32 m0, %0"
               : "=&s"(keep) : "s"(lds), "v"(voff), "s"(r), "s"(soff) : "memory");
}
DI void bar() { asm volatile("s_waitcnt lgkmcnt(0)\n\ts_barrier" ::: "memory"); }
DI float red8(const float (&a)[8], int lane) {
  const float b0 = tr32(a[0], a[4]), b1 = tr32(a[1], a[5]), b2 = tr32(a[2], a[6]), b3 = tr32(a[3], a[7]);
  const float c0 = tr16(b0, b2), c1 = tr16(b1, b3);
  return tr8(c0, c1, lane & 8);
}
DI int red8col(int lane) { return ((lane >> 5) & 1) * 4 + ((lane >> 4) & 1) * 2 + ((lane >> 3) & 1); }

#define ST(i) do { } while (0)

__global__ void __launch_bounds__(256, 1)
mega(const u16* __restrict__ x, const u16* __restrict__ K, const u16* __restrict__ V, const u16* __restrict__ wq, const u16* __restrict__ wo,
     const u16* __restrict__ wg, const u16* __restrict__ wu, const u16* __restrict__ wd, const u16* __restrict__ c, const u16* __restrict__ wmod,
     u16* __restrict__ out, unsigned* ws) {
  extern __shared__ __attribute__((aligned(16))) char smem[];
  const int tid = threadIdx.x, lane = tid & 63, w = __builtin_amdgcn_readfirstlane(tid >> 6), b = blockIdx.x;
  const int xc = b & 7, head = b >> 3, slot = (head + 4 * xc) & 31;
  ST(0);
  const rsrc_t rw = mkrsrc(ws, WS_BYTES);
  const unsigned ldsb = (unsigned)(size_t)(LAS char*)smem;
  const unsigned ew = __builtin_amdgcn_raw_buffer_load_b32(rw, 0, 0, 16);
  const bool isQ = slot < 8, isM1 = slot >= 8 && slot < 13, isF = slot >= 13 && slot < 20 && 7 * xc + slot - 13 < 54, isW = slot >= 20 && slot < 30;
  unsigned tag = 0;
  unsigned sw[5];

  if (isM1) {
    const int mi = 5 * xc + slot - 8, wh = w & 1, rho = lane >> 1, cq = lane & 1;
    const rsrc_t rm = mkrsrc(wmod, D * 6 * D * 2);
    const unsigned* cw = (const unsigned*)c + 160 * wh + 5 * rho;
    unsigned cv[5], xv[5];
#pragma unroll
    for (int i = 0; i < 5; ++i) cv[i] = cw[i];
#pragma unroll
    for (int i = 0; i < 5; ++i) xv[i] = ((const unsigned*)x)[5 * lane + i];
    const unsigned xo = x[16 * mi + (lane & 15)];
    const int vo = (320 * wh + 10 * rho) * 7680 + ((w >> 1) * D + 16 * mi + 8 * cq) * 2;
    v4u wv[10];
#pragma unroll
    for (int i = 0; i < 10; ++i) wv[i] = __builtin_amdgcn_raw_buffer_load_b128(rm, vo, i * 7680, 0);
    __builtin_amdgcn_sched_barrier(0);
#pragma unroll
    for (int i = 0; i < 5; ++i) sw[i] = pack2(silu(lo16(cv[i])), silu(hi16(cv[i])));
    float s = 0;
#pragma unroll
    for (int i = 0; i < 5; ++i) s += lo16(xv[i]) + hi16(xv[i]);
    const float mean = wsum(s) * (1.f / D);
    float v = 0;
#pragma unroll
    for (int i = 0; i < 5; ++i) { const float d0 = lo16(xv[i]) - mean, d1 = hi16(xv[i]) - mean; v += d0 * d0 + d1 * d1; }
    const float var = wsum(v) * (1.f / D);
    const float rs = __builtin_amdgcn_rcpf(rbf(__builtin_sqrtf(rbf(rbf(var) + 1e-5f))));
    const float nrm = rbf(rbf(lo16(xo) - rbf(mean)) * rs);
    float acc[8] = {0, 0, 0, 0, 0, 0, 0, 0};
#pragma unroll
    for (int i = 0; i < 5; ++i) {
      const P8 p = mkpairs(wv[2 * i], wv[2 * i + 1]);
#pragma unroll
      for (int j = 0; j < 8; ++j) acc[j] = dot2(p.p[j], sw[i], acc[j]);
    }
    float r = red8(acc, lane);
    r += dpp<SHL4>(r); r += dpp<XOR2>(r);
    float* red = (float*)(smem + L_RED);
    if ((lane & 6) == 0) red[w * 16 + cq * 8 + red8col(lane)] = r;
    bar();
    if (w == 0) {
      const int col = lane & 15;
      const float sa = rbf(red[col] + red[16 + col]), ca = rbf(red[32 + col] + red[48 + col]);
      const float hv = rbf(rbf(nrm * rbf(1.f + ca)) + sa);
      const unsigned hb = asu(hv) >> 16;
      const unsigned hw = hb | (asu(dpp<XOR1>(hv)) & 0xffff0000u);
      const unsigned hw2 = asu(dpp<XOR2>(asf(hw)));
      tag = __builtin_amdgcn_readfirstlane(ew) + 1;
      if ((lane & 3) == 0 && (lane >> 4) < RH) stg(rw, OFF_H + (lane >> 4) * 2560 + (4 * mi + ((lane & 15) >> 2)) * 16, hw, hw2, tag);
    }
    ST(1);
  } else if (isQ) {
    const int qi = 8 * xc + slot, rho = lane >> 3, cq = lane & 7;
    const rsrc_t rq = mkrsrc(wq, D * 4096 * 2);
    const int vo = (160 * w + 20 * rho) * 8192 + (64 * qi + 8 * cq) * 2;
    v4u wv[20];
#pragma unroll
    for (int i = 0; i < 20; ++i) wv[i] = __builtin_amdgcn_raw_buffer_load_b128(rq, vo, i * 8192, 0);
    __builtin_amdgcn_sched_barrier(0);
    unsigned pr[80];
#pragma unroll
    for (int i = 0; i < 10; ++i) {
      const P8 p = mkpairs(wv[2 * i], wv[2 * i + 1]);
#pragma unroll
      for (int j = 0; j < 8; ++j) pr[8 * i + j] = p.p[j];
    }
    tag = __builtin_amdgcn_readfirstlane(ew) + 1;
    ST(1);
    v4u g[5];
    const int go = OFF_H + (xc % RH) * 2560 + (40 * w + 5 * rho) * 16;
    for (int spin = 0; spin < SPIN_CAP; ++spin) {
      fence_c();
      bool ok = true;
#pragma unroll
      for (int i = 0; i < 5; ++i) { g[i] = ldg(rw, go + i * 16); }
#pragma unroll
      for (int i = 0; i < 5; ++i) ok &= okg(g[i], tag);
      if (__all(ok)) break;
      __builtin_amdgcn_s_sleep(1);
    }
    ST(2);
    float acc[8] = {0, 0, 0, 0, 0, 0, 0, 0};
#pragma unroll
    for (int i = 0; i < 10; ++i) {
      const unsigned hw = g[i >> 1][(i & 1) * 2];
#pragma unroll
      for (int j = 0; j < 8; ++j) acc[j] = dot2(pr[8 * i + j], hw, acc[j]);
    }
    const float r = red8(acc, lane);
    float* red = (float*)(smem + L_RED);
    red[w * 64 + cq * 8 + red8col(lane)] = r;
    bar();
    if (w == 0 && lane < 16) {
      const f32x4 a0 = *(const f32x4*)(red + 4 * lane), a1 = *(const f32x4*)(red + 64 + 4 * lane), a2 = *(const f32x4*)(red + 128 + 4 * lane), a3 = *(const f32x4*)(red + 192 + 4 * lane);
      const f32x4 s = (a0 + a1) + (a2 + a3);
      stg(rw, OFF_Q + (16 * qi + lane) * 16, pack2(s[0], s[1]), pack2(s[2], s[3]), tag);
    }
    ST(3);
  }
  __builtin_amdgcn_sched_barrier(0);

  const int nkeys = 2048, key0 = (b & 7) * 2048;
  const int NLD = nkeys >> 4;
  const rsrc_t rk = mkrsrc(K + ((size_t)head * SK + key0) * 128, nkeys * 256);
  const rsrc_t rv = mkrsrc(V + ((size_t)head * SK + key0) * 128, nkeys * 256);
  const rsrc_t rz = mkrsrc(V, 0);
  unsigned cvm[5];
  if (isW) {
#pragma unroll
    for (int i = 0; i < 5; ++i) cvm[i] = ((const unsigned*)c)[80 * w + 5 * (lane >> 2) + i];
  }
  __builtin_amdgcn_sched_barrier(0);
  const int voff = lane * 16 + w * 4096;
  v4u a[WIN];
#pragma unroll
  for (int i = 0; i < WIN; ++i) a[i] = __builtin_amdgcn_raw_buffer_load_b128(rk, voff + (i & 3) * 1024, (i >> 2) * 16384, AUXKV);
  __builtin_amdgcn_sched_barrier(0);

  auto role_dma = [&](int i0, int i1) {
    if (isW) {
      const int C = slot - 20, m2 = 10 * xc + C;
      const rsrc_t ro = mkrsrc(wo, 4096 * D * 2), rm = mkrsrc(wmod, D * 6 * D * 2);
      const unsigned vo = ((512 * xc + 128 * w + 16 * (lane >> 3)) * D + 64 * C + 8 * (lane & 7)) * 2;
      const unsigned vm = (160 * w + 10 * (lane >> 2)) * 7680 + (2 * D + 32 * m2 + 8 * (lane & 3)) * 2;
#pragma unroll
      for (int i = 0; i < 26; ++i) if (i >= i0 && i < i1) {
        if (i < 10) dma16(rm, vm, i * 7680, ldsb + L_W + 65536 + (w * 10 + i) * 1024);
        else dma16(ro, vo, (i - 10) * 1280, ldsb + L_W + (w * 16 + i - 10) * 1024);
      }
    } else if (isF) {
      const int fj = 7 * xc + slot - 13;
      const rsrc_t rg = mkrsrc(w < 2 ? wg : wu, D * F * 2), rd = mkrsrc(wd, F * D * 2);
      const unsigned vg = ((320 * (w & 1) + 20 * (lane >> 2)) * F + 32 * fj + 8 * (lane & 3)) * 2;
      const unsigned vd = (32 * fj * D) * 2 + w * 10240 + lane * 16;
#pragma unroll
      for (int i = 0; i < 30; ++i) if (i >= i0 && i < i1) {
        if (i < 20) dma16(rg, vg, i * (F * 2), ldsb + L_W + (w * 20 + i) * 1024);
        else dma16(rd, vd, (i - 20) * 1024, ldsb + L_W + 81920 + (w * 10 + i - 20) * 1024);
      }
    }
  };
  role_dma(0, DMA_PRE);
  if (isW) {
#pragma unroll
    for (int i = 0; i < 5; ++i) sw[i] = pack2(silu(lo16(cvm[i])), silu(hi16(cvm[i])));
  }
  tag = __builtin_amdgcn_readfirstlane(ew) + 1;
  const int r4 = lane >> 4, cc = lane & 15;
  v4u qp;
  {
    v4u g0, g1;
    for (int spin = 0; spin < SPIN_CAP; ++spin) {
      fence_c();
      g0 = ldg(rw, OFF_Q + head * 512 + cc * 32); g1 = ldg(rw, OFF_Q + head * 512 + cc * 32 + 16);
      if (__all(okg(g0, tag) && okg(g1, tag))) break;
      __builtin_amdgcn_s_sleep(1);
    }
    qp[0] = g0[0]; qp[1] = g0[2]; qp[2] = g1[0]; qp[3] = g1[2];
  }
  ST(4);
  role_dma(DMA_PRE, 30);
  __builtin_amdgcn_sched_barrier(0);

  float* sp = (float*)(smem + L_SP) + (w * 4 + r4) * NLMAX;
  float mx = -INFINITY;
  const int NIT = (NLD + WIN - 1) / WIN;
#pragma unroll 1
  for (int it = 0; it < NIT; ++it) {
    const bool lastk = it == NIT - 1;
    const int so = lastk ? 0 : (it + 1) * (WIN / 4) * 16384;
    const rsrc_t rr = lastk ? rv : rk;
#pragma unroll
    for (int g = 0; g < WIN / 16; ++g) {
      float xs[16];
#pragma unroll
      for (int t = 0; t < 16; ++t) {
        const int j = g * 16 + t;
        float d = 0;
#pragma unroll
        for (int jj = 0; jj < 4; ++jj) d = dot2(a[j][jj], qp[jj], d);
        xs[t] = d;
        __builtin_amdgcn_sched_barrier(0);
        a[j] = __builtin_amdgcn_raw_buffer_load_b128(rr, voff + (j & 3) * 1024, so + (j >> 2) * 16384, AUXKV);
        __builtin_amdgcn_sched_barrier(0);
      }
#define TR_STEP(HALF, CTRL, BIT) _Pragma("unroll") for (int hh = 0; hh < (HALF); ++hh) { \
      const float lo_ = xs[hh], hi_ = xs[hh + (HALF)]; const float t1_ = lo_ + dpp<CTRL>(lo_), t2_ = hi_ + dpp<CTRL>(hi_); xs[hh] = (cc & (BIT)) ? t2_ : t1_; }
      TR_STEP(8, XOR8, 8) TR_STEP(4, XOR7, 4) TR_STEP(2, XOR2, 2) TR_STEP(1, XOR1, 1)
#undef TR_STEP
      const int li = it * WIN + g * 16 + cc;
      const float sc = li < NLD ? rbf(rbf(xs[0]) * 0.08838834764831845f) : -INFINITY;
      mx = fmaxf(mx, sc);
      sp[li] = sc;
    }
  }
  ST(5);
  mx = wmax(mx);
  float l = 0;
  for (int g = 0; g < NIT * (WIN / 16); ++g) { const float p = __expf(sp[g * 16 + cc] - mx); l += p; sp[g * 16 + cc] = p; }
  l = wsum(l);

  if (isW) {
    const int m2 = 10 * xc + slot - 20, cq = lane & 3;
    const v4u* wl = (const v4u*)(smem + L_W + 65536 + w * 10240) + lane;
    float acc[8] = {0, 0, 0, 0, 0, 0, 0, 0};
#pragma unroll
    for (int i = 0; i < 5; ++i) {
      const P8 p = mkpairs(wl[(2 * i) * 64], wl[(2 * i + 1) * 64]);
#pragma unroll
      for (int j = 0; j < 8; ++j) acc[j] = dot2(p.p[j], sw[i], acc[j]);
    }
    float r = red8(acc, lane);
    r += dpp<SHL4>(r);
    float* red = (float*)(smem + L_RED);
    if ((lane & 4) == 0) red[w * 32 + cq * 8 + red8col(lane)] = r;
    bar();
    if (w == 0) {
      const int g = lane & 7, rep = lane >> 3;
      const f32x4 a0 = *(const f32x4*)(red + 4 * g), a1 = *(const f32x4*)(red + 32 + 4 * g), a2 = *(const f32x4*)(red + 64 + 4 * g), a3 = *(const f32x4*)(red + 96 + 4 * g);
      const f32x4 s = (a0 + a1) + (a2 + a3);
      if (rep < RM) stg(rw, OFF_M + rep * 10240 + (8 * m2 + g) * 16, pack2(s[0], s[1]), pack2(s[2], s[3]), tag);
    }
    bar();
  }

  float acc[8] = {0, 0, 0, 0, 0, 0, 0, 0};
#pragma unroll 1
  for (int it = 0; it < NIT; ++it) {
    const bool lastv = it == NIT - 1;
    const rsrc_t rr = lastv ? rz : rv;
    const int so = (it + 1) * (WIN / 4) * 16384;
#pragma unroll
    for (int g = 0; g < WIN / 4; ++g) {
      const f32x4 p4 = *(const f32x4*)&sp[it * WIN + g * 4];
#pragma unroll
      for (int t = 0; t < 4; ++t) {
        const int j = g * 4 + t;
        const float pp = p4[t];
#pragma unroll
        for (int jj = 0; jj < 4; ++jj) { const unsigned wv = a[j][jj]; acc[2 * jj] += pp * lo16(wv); acc[2 * jj + 1] += pp * hi16(wv); }
        asm volatile("" : "+v"(acc[0]), "+v"(acc[1]), "+v"(acc[2]), "+v"(acc[3]), "+v"(acc[4]), "+v"(acc[5]), "+v"(acc[6]), "+v"(acc[7]));
        __builtin_amdgcn_sched_barrier(0);
        a[j] = __builtin_amdgcn_raw_buffer_load_b128(rr, voff + (j & 3) * 1024, so + (j >> 2) * 16384, AUXKV);
        __builtin_amdgcn_sched_barrier(0);
      }
    }
  }
  ST(6);
  {
    float* sm = (float*)(smem + L_RED);
#pragma unroll
    for (int j = 0; j < 8; ++j) { acc[j] = tr16(acc[j], acc[j]); acc[j] = tr32(acc[j], acc[j]); }
    if (r4 == 0) { *(f32x4*)(sm + w * 132 + 4 + 8 * cc) = f32x4{acc[0], acc[1], acc[2], acc[3]}; *(f32x4*)(sm + w * 132 + 8 + 8 * cc) = f32x4{acc[4], acc[5], acc[6], acc[7]}; }
    if (lane == 0) { sm[w * 132] = mx; sm[w * 132 + 1] = l; }
    asm volatile("s_waitcnt vmcnt(0)" ::: "memory");
    bar();
    if (w < 2) {
      const float m0 = sm[0], m1 = sm[132], m2 = sm[264], m3 = sm[396];
      const float M = fmaxf(fmaxf(m0, m1), fmaxf(m2, m3));
      const float e0 = __expf(m0 - M), e1 = __expf(m1 - M), e2 = __expf(m2 - M), e3 = __expf(m3 - M);
      const int po = OFF_P + b * PSTR;
      if (w == 0) {
        const float* s0 = sm + 4 + 2 * lane;
        const float o0 = e0 * s0[0] + e1 * s0[132] + e2 * s0[264] + e3 * s0[396], o1 = e0 * s0[1] + e1 * s0[133] + e2 * s0[265] + e3 * s0[397];
        stg(rw, po + lane * 16, asu(o0), asu(o1), tag);
      } else if (lane == 0) {
        stg(rw, po + 64 * 16, asu(M), asu(e0 * sm[1] + e1 * sm[133] + e2 * sm[265] + e3 * sm[397]), tag);
      }
    }
    bar();
  }
  ST(7);

  if (isW) {
    const int C = slot - 20, hh = 4 * xc + w, cq = lane & 7;
    const v4u* wl = (const v4u*)(smem + L_W + w * 16384) + lane;
    unsigned pr[64];
#pragma unroll
    for (int i = 0; i < 8; ++i) {
      const P8 p = mkpairs(wl[(2 * i) * 64], wl[(2 * i + 1) * 64]);
#pragma unroll
      for (int j = 0; j < 8; ++j) pr[8 * i + j] = p.p[j];
    }
    const int po = OFF_P + hh * 8 * PSTR;
    v4u g[8], ml;
    bool have[9] = {false, false, false, false, false, false, false, false, false};
    for (int spin = 0; spin < SPIN_CAP; ++spin) {
      fence_c();
#pragma unroll
      for (int s = 0; s < 8; ++s) if (!have[s]) g[s] = ldg(rw, po + s * PSTR + lane * 16);
      if (!have[8]) ml = ldg(rw, po + (lane & 7) * PSTR + 64 * 16);
      bool all = true;
#pragma unroll
      for (int s = 0; s < 8; ++s) { have[s] = __all(okg(g[s], tag)); all &= have[s]; }
      have[8] = __all(okg(ml, tag)); all &= have[8];
      if (all) break;
      __builtin_amdgcn_s_sleep(1);
    }
    ST(8);
    const unsigned mu = ml[0], lu = ml[2];
    const float ms = asf(mu);
    float M = fmaxf(ms, dpp<XOR1>(ms)); M = fmaxf(M, dpp<XOR2>(M)); M = fmaxf(M, dpp<XOR7>(M));
    const float e = __expf(ms - M);
    float L = e * asf(lu); L += dpp<XOR1>(L); L += dpp<XOR2>(L); L += dpp<XOR7>(L);
    const float f = e * __builtin_amdgcn_rcpf(L);
    float o0 = 0, o1 = 0;
#pragma unroll
    for (int s = 0; s < 8; ++s) {
      const float fs = asf(__builtin_amdgcn_readlane(asu(f), s));
      const unsigned u0 = g[s][0], u1 = g[s][2];
      o0 += fs * asf(u0); o1 += fs * asf(u1);
    }
    unsigned* hw = (unsigned*)(smem + L_HW) + w * 192;
    hw[lane] = pack2(o0, o1);
    fence_c();
    const v4u h0 = *(const v4u*)(hw + 8 * (lane >> 3)), h1 = *(const v4u*)(hw + 8 * (lane >> 3) + 4);
    float y[8] = {0, 0, 0, 0, 0, 0, 0, 0};
#pragma unroll
    for (int i = 0; i < 8; ++i) {
      const unsigned hv = i < 4 ? h0[i & 3] : h1[i & 3];
#pragma unroll
      for (int j = 0; j < 8; ++j) y[j] = dot2(pr[8 * i + j], hv, y[j]);
    }
    const float r = red8(y, lane);
    float* red = (float*)(smem + L_RED);
    red[w * 64 + cq * 8 + red8col(lane)] = r;
    bar();
    if (w == 0 && lane < 32) {
      const float* rp = red + 2 * lane;
      const float y0 = (rp[0] + rp[64]) + (rp[128] + rp[192]), y1 = (rp[1] + rp[65]) + (rp[129] + rp[193]);
      stg(rw, OFF_Y + (xc * 320 + 32 * C + lane) * 16, asu(y0), asu(y1), tag);
    }
    ST(9);
  } else if (isF) {
    const int fj = 7 * xc + slot - 13, wh = w & 1, rho = lane >> 2, cq = lane & 3;
    unsigned pr[80], pd[64];
    {
      const v4u* wl = (const v4u*)(smem + L_W + w * 20480) + lane;
#pragma unroll
      for (int i = 0; i < 10; ++i) {
        const P8 p = mkpairs(wl[(2 * i) * 64], wl[(2 * i + 1) * 64]);
#pragma unroll
        for (int j = 0; j < 8; ++j) pr[8 * i + j] = p.p[j];
      }
      const int tc = tid < 160 ? tid : 0;
      const v2u* dl = (const v2u*)(smem + L_W + 81920) + tc;
#pragma unroll
      for (int i = 0; i < 16; ++i) {
        const v2u r0 = dl[(2 * i) * 160], r1 = dl[(2 * i + 1) * 160];
        const unsigned a0 = r0[0], a1 = r0[1], b0 = r1[0], b1 = r1[1];
        pd[4 * i] = plo(a0, b0); pd[4 * i + 1] = phi(a0, b0); pd[4 * i + 2] = plo(a1, b1); pd[4 * i + 3] = phi(a1, b1);
      }
    }
    float* x1s = (float*)(smem + L_X1); float* cbs = (float*)(smem + L_CB); float* sbs = (float*)(smem + L_SB);
    if (tid < 160) {
      const int mo = OFF_M + (xc % RM) * 10240 + tid * 16;
      v4u ga, sb, cb;
      for (int spin = 0; spin < SPIN_CAP; ++spin) {
        fence_c();
        ga = ldg(rw, mo); sb = ldg(rw, mo + 2560); cb = ldg(rw, mo + 5120);
        if (__all(okg(ga, tag) && okg(sb, tag) && okg(cb, tag))) break;
        __builtin_amdgcn_s_sleep(1);
      }
      const v2u xw = *(const v2u*)(x + 4 * tid);
      { const unsigned c0 = cb[0], c1 = cb[2], s0 = sb[0], s1 = sb[2];
        *(f32x4*)(cbs + 4 * tid) = f32x4{rbf(1.f + lo16(c0)), rbf(1.f + hi16(c0)), rbf(1.f + lo16(c1)), rbf(1.f + hi16(c1))};
        *(f32x4*)(sbs + 4 * tid) = f32x4{lo16(s0), hi16(s0), lo16(s1), hi16(s1)}; }
      v4u g[16];
      bool have[8] = {false, false, false, false, false, false, false, false};
      for (int spin = 0; spin < SPIN_CAP; ++spin) {
        fence_c();
#pragma unroll
        for (int G = 0; G < 8; ++G) if (!have[G]) { g[2 * G] = ldg(rw, OFF_Y + (G * 320 + 2 * tid) * 16); g[2 * G + 1] = ldg(rw, OFF_Y + (G * 320 + 2 * tid + 1) * 16); }
        bool all = true;
#pragma unroll
        for (int G = 0; G < 8; ++G) { have[G] = __all(okg(g[2 * G], tag) && okg(g[2 * G + 1], tag)); all &= have[G]; }
        if (all) break;
        __builtin_amdgcn_s_sleep(1);
      }
      float y[4] = {0, 0, 0, 0};
#pragma unroll
      for (int G = 0; G < 8; ++G) { const unsigned u0 = g[2 * G][0], u1 = g[2 * G][2], u2 = g[2 * G + 1][0], u3 = g[2 * G + 1][2]; y[0] += asf(u0); y[1] += asf(u1); y[2] += asf(u2); y[3] += asf(u3); }
      const unsigned g0 = ga[0], g1 = ga[2], x0 = xw[0], x1w = xw[1];
      *(f32x4*)(x1s + 4 * tid) = f32x4{rbf(lo16(x0) + rbf(lo16(g0) * rbf(y[0]))), rbf(hi16(x0) + rbf(hi16(g0) * rbf(y[1]))),
                                       rbf(lo16(x1w) + rbf(lo16(g1) * rbf(y[2]))), rbf(hi16(x1w) + rbf(hi16(g1) * rbf(y[3])))};
    }
    ST(8);
    bar();
    float xa[5], xb[5];
#pragma unroll
    for (int i = 0; i < 5; ++i) { xa[i] = x1s[5 * lane + i]; xb[i] = x1s[320 + 5 * lane + i]; }
    float s = 0;
#pragma unroll
    for (int i = 0; i < 5; ++i) s += xa[i] + xb[i];
    const float mean = wsum(s) * (1.f / D);
    float v = 0;
#pragma unroll
    for (int i = 0; i < 5; ++i) { const float d0 = xa[i] - mean, d1 = xb[i] - mean; v += d0 * d0 + d1 * d1; }
    const float var = wsum(v) * (1.f / D);
    const float rs = __builtin_amdgcn_rcpf(rbf(__builtin_sqrtf(rbf(rbf(var) + 1e-5f)))), mb = rbf(mean);
    u16* hs = (u16*)((unsigned*)(smem + L_HW) + w * 192);
#pragma unroll
    for (int i = 0; i < 5; ++i) {
      const int col = 320 * wh + 5 * lane + i;
      const float xx = wh ? xb[i] : xa[i];
      hs[5 * lane + i] = (u16)f2bf(rbf(rbf(rbf(xx - mb) * rs) * cbs[col]) + sbs[col]);
    }
    fence_c();
    const v2u* hp = (const v2u*)((const unsigned*)hs + 10 * rho);
    float ga8[8] = {0, 0, 0, 0, 0, 0, 0, 0};
#pragma unroll
    for (int i = 0; i < 5; ++i) {
      const v2u hv = hp[i];
      const unsigned h0 = hv[0], h1 = hv[1];
#pragma unroll
      for (int j = 0; j < 8; ++j) ga8[j] = dot2(pr[16 * i + j], h0, ga8[j]);
#pragma unroll
      for (int j = 0; j < 8; ++j) ga8[j] = dot2(pr[16 * i + 8 + j], h1, ga8[j]);
    }
    float r = red8(ga8, lane);
    r += dpp<SHL4>(r);
    float* red = (float*)(smem + L_RED);
    if ((lane & 4) == 0) red[w * 32 + cq * 8 + red8col(lane)] = r;
    bar();
    const int col = lane & 31;
    const float gg = rbf(red[col] + red[32 + col]), uu = rbf(red[64 + col] + red[96 + col]);
    const float av = rbf(rbf(silu(gg)) * uu);
    const unsigned aw = (asu(av) >> 16) | (asu(dpp<XOR1>(av)) & 0xffff0000u);
    float z[4] = {0, 0, 0, 0};
#pragma unroll
    for (int i = 0; i < 16; ++i) {
      const unsigned as = __builtin_amdgcn_readlane(aw, 2 * i);
#pragma unroll
      for (int j = 0; j < 4; ++j) z[j] = dot2(pd[4 * i + j], as, z[j]);
    }
    if (tid < 160) {
      stg(rw, OFF_Z + (fj * 320 + 2 * tid) * 16, asu(z[0]), asu(z[1]), tag);
      stg(rw, OFF_Z + (fj * 320 + 2 * tid + 1) * 16, asu(z[2]), asu(z[3]), tag);
    }
    ST(9);
  } else if (isM1) {
    const int fi = 5 * xc + slot - 8, g8 = lane & 7;
    float x1a = 0, x1b = 0, gb0 = 0, gb1 = 0;
    if (w == 0) {
      const int mo = OFF_M + (xc % RM) * 10240 + (4 * fi + (g8 >> 1)) * 16;
      v4u ga, gb, yy;
      for (int spin = 0; spin < SPIN_CAP; ++spin) {
        fence_c();
        ga = ldg(rw, mo); gb = ldg(rw, mo + 7680); yy = ldg(rw, OFF_Y + ((lane >> 3) * 320 + 8 * fi + g8) * 16);
        if (__all(okg(ga, tag) && okg(gb, tag) && okg(yy, tag))) break;
        __builtin_amdgcn_s_sleep(1);
      }
      const unsigned xw = ((const unsigned*)x)[8 * fi + g8];
      const unsigned gaw = (g8 & 1) ? ga[2] : ga[0], gbw = (g8 & 1) ? gb[2] : gb[0], u0 = yy[0], u1 = yy[2];
      float y0 = 0, y1 = 0;
#pragma unroll
      for (int G = 0; G < 8; ++G) { y0 += __shfl(asf(u0), G * 8 + g8); y1 += __shfl(asf(u1), G * 8 + g8); }
      x1a = rbf(lo16(xw) + rbf(lo16(gaw) * rbf(y0))); x1b = rbf(hi16(xw) + rbf(hi16(gaw) * rbf(y1)));
      gb0 = lo16(gbw); gb1 = hi16(gbw);
    }
    ST(8);
    const int p0 = tid >> 3;
    v4u z0, z1 = {0, 0, 0, 0};
    bool h0 = false, h1 = false;
    for (int spin = 0; spin < SPIN_CAP; ++spin) {
      fence_c();
      if (!h0) z0 = ldg(rw, OFF_Z + (p0 * 320 + 8 * fi + g8) * 16);
      if (!h1 && p0 < 22) z1 = ldg(rw, OFF_Z + ((p0 + 32) * 320 + 8 * fi + g8) * 16);
      h0 = __all(okg(z0, tag)); h1 = __all(p0 >= 22 || okg(z1, tag));
      if (h0 && h1) break;
      __builtin_amdgcn_s_sleep(1);
    }
    ST(9);
    const unsigned u0 = z0[0], u1 = z0[2], u2 = z1[0], u3 = z1[2];
    float s0 = asf(u0) + asf(u2), s1 = asf(u1) + asf(u3);
    s0 += dpp<XOR8>(s0); s1 += dpp<XOR8>(s1);
    s0 = tr16(s0, s0); s1 = tr16(s1, s1); s0 = tr32(s0, s0); s1 = tr32(s1, s1);
    float* red = (float*)(smem + L_RED);
    if (lane < 8) { red[w * 16 + 2 * lane] = s0; red[w * 16 + 2 * lane + 1] = s1; }
    bar();
    if (w == 0 && lane < 8) {
      const float* rp = red + 2 * lane;
      const float y0 = (rp[0] + rp[16]) + (rp[32] + rp[48]), y1 = (rp[1] + rp[17]) + (rp[33] + rp[49]);
      ((unsigned*)out)[8 * fi + lane] = pack2(x1a + rbf(gb0 * rbf(y0)), x1b + rbf(gb1 * rbf(y1)));
      if (fi == 0 && lane == 0) __builtin_amdgcn_raw_buffer_store_b32(tag, rw, 0, 0, 16);
    }
    ST(10);
  }
}

extern "C" void kernel_launch(void* const* d_in, const int* in_sizes, int n_in, void* d_out, int out_size, void* d_ws, size_t ws_size,
                              hipStream_t stream) {
  hipFuncSetAttribute((const void*)mega, hipFuncAttributeMaxDynamicSharedMemorySize, LDS_BYTES);
  mega<<<256, 256, LDS_BYTES, stream>>>((const u16*)d_in[0], (const u16*)d_in[1], (const u16*)d_in[2], (const u16*)d_in[3], (const u16*)d_in[4],
                                        (const u16*)d_in[6], (const u16*)d_in[7], (const u16*)d_in[8], (const u16*)d_in[9], (const u16*)d_in[10],
                                        (u16*)d_out, (unsigned*)d_ws);
}
```

```cpp
#define NW 8
#define WIN 16
#define AUXK 2
#define AUXV 2
#include <hip/hip_runtime.h>
#include <hip/hip_bf16.h>
#include <stdint.h>

typedef unsigned short u16;
typedef unsigned v4u __attribute__((ext_vector_type(4)));
typedef float f32x4 __attribute__((ext_vector_type(4)));
typedef __bf16 bf16x2 __attribute__((ext_vector_type(2)));

constexpr int D = 640, H = 32, SK = 16384, DH = 128, F = 1728;
constexpr int NSPLIT = 8;

__device__ __forceinline__ float bf2f(unsigned b) { return __builtin_bit_cast(float, b << 16); }
__device__ __forceinline__ unsigned f2bf(float f) { unsigned u = __builtin_bit_cast(unsigned, f); return (u + 0x7fffu + ((u >> 16) & 1u)) >> 16; }
__device__ __forceinline__ float rbf(float f) { return bf2f(f2bf(f)); }
__device__ __forceinline__ float dot2(unsigned k, unsigned q, float acc) {
  return __builtin_amdgcn_fdot2_f32_bf16(__builtin_bit_cast(bf16x2, k), __builtin_bit_cast(bf16x2, q), acc, false);
}
__device__ __forceinline__ float wsum(float v) { for (int o = 32; o; o >>= 1) v += __shfl_xor(v, o); return v; }
__device__ __forceinline__ float wmax(float v) { for (int o = 32; o; o >>= 1) v = fmaxf(v, __shfl_xor(v, o)); return v; }

template <int K, int N>
__device__ __forceinline__ void gemv64(const float* in, const u16* W, int n0, float* red, float* out64) {
  int tid = threadIdx.x, c = tid & 7, r = tid >> 3;
  float acc[8] = {0, 0, 0, 0, 0, 0, 0, 0};
  for (int k = r; k < K; k += 32) {
    v4u w = *(const v4u*)(W + (size_t)k * N + n0 + 8 * c);
    float a = in[k];
    for (int j = 0; j < 4; ++j) { acc[2 * j] += a * bf2f(w[j] & 0xffff); acc[2 * j + 1] += a * bf2f(w[j] >> 16); }
  }
  for (int j = 0; j < 8; ++j) red[r * 64 + c * 8 + j] = acc[j];
  __syncthreads();
  if (tid < 64) { float s = 0; for (int rr = 0; rr < 32; ++rr) s += red[rr * 64 + tid]; out64[tid] = s; }
  __syncthreads();
}

__global__ void __launch_bounds__(256) k_mod(const u16* c, const u16* wmod, float* mod) {
  __shared__ float s[D], red[32 * 64], o[64];
  for (int i = threadIdx.x; i < D; i += 256) { float v = bf2f(c[i]); s[i] = rbf(v / (1.f + __expf(-v))); }
  __syncthreads();
  gemv64<D, 6 * D>(s, wmod, blockIdx.x * 64, red, o);
  if (threadIdx.x < 64) mod[blockIdx.x * 64 + threadIdx.x] = rbf(o[threadIdx.x]);
}

__device__ __forceinline__ void ln_mod(const float* x, const float* scale, const float* shift, float* h, float* red) {
  int tid = threadIdx.x;
  float s = 0; for (int i = tid; i < D; i += 256) s += x[i];
  s = wsum(s); if ((tid & 63) == 0) red[tid >> 6] = s; __syncthreads();
  float mean = (red[0] + red[1] + red[2] + red[3]) / D; __syncthreads();
  float v = 0; for (int i = tid; i < D; i += 256) { float d = x[i] - mean; v += d * d; }
  v = wsum(v); if ((tid & 63) == 0) red[tid >> 6] = v; __syncthreads();
  float var = (red[0] + red[1] + red[2] + red[3]) / D; __syncthreads();
  float mb = rbf(mean), sd = rbf(sqrtf(rbf(rbf(var) + 1e-5f)));
  for (int i = tid; i < D; i += 256) { float n = rbf(rbf(x[i] - mb) / sd); h[i] = rbf(rbf(n * rbf(1.f + scale[i])) + shift[i]); }
  __syncthreads();
}

__global__ void __launch_bounds__(256) k_q(const u16* x, const float* mod, const u16* wq, float* q) {
  __shared__ float xs[D], h[D], red[32 * 64], o[64];
  for (int i = threadIdx.x; i < D; i += 256) xs[i] = bf2f(x[i]);
  __syncthreads();
  ln_mod(xs, mod + D, mod, h, red);
  gemv64<D, H * DH>(h, wq, blockIdx.x * 64, red, o);
  if (threadIdx.x < 64) q[blockIdx.x * 64 + threadIdx.x] = rbf(o[threadIdx.x]);
}

template <int CTRL> __device__ __forceinline__ float dpp(float x) {
  return __builtin_bit_cast(float, __builtin_amdgcn_mov_dpp(__builtin_bit_cast(int, x), CTRL, 0xf, 0xf, true));
}
constexpr int XOR1 = 0xB1, XOR2 = 0x4E, XOR7 = 0x141, XOR8 = 0x128, ROR4 = 0x124;
constexpr int KPW = 2048 / NW, NL = KPW / 4;
__global__ void __launch_bounds__(NW * 64, 1) k_attn(const float* q, const u16* K, const u16* V, float* part) {
  __shared__ __attribute__((aligned(16))) float sp[NW][4][NL];
  __shared__ float sm[NW][132];
  const int b = blockIdx.x, h = b / NSPLIT, s = b % NSPLIT;
  const int tid = threadIdx.x, lane = tid & 63, w = __builtin_amdgcn_readfirstlane(tid >> 6), r = lane >> 4, c = lane & 15;
  const __amdgpu_buffer_rsrc_t rk = __builtin_amdgcn_make_buffer_rsrc((void*)K, 0, 128 << 20, 0x00020000);
  const __amdgpu_buffer_rsrc_t rv = __builtin_amdgcn_make_buffer_rsrc((void*)V, 0, 128 << 20, 0x00020000);
  const __amdgpu_buffer_rsrc_t rz = __builtin_amdgcn_make_buffer_rsrc((void*)V, 0, 0, 0x00020000);
  const int sbase = (h * SK + s * 2048 + w * KPW) * 256;
  const int voff = lane * 16;
  const f32x4 q0 = *(const f32x4*)(q + h * DH + 8 * c), q1 = *(const f32x4*)(q + h * DH + 8 * c + 4);
  __builtin_amdgcn_sched_barrier(0);
  v4u a[WIN];
#pragma unroll
  for (int i = 0; i < WIN; ++i) a[i] = __builtin_amdgcn_raw_buffer_load_b128(rk, voff, sbase + i * 1024, AUXK);
  __builtin_amdgcn_sched_barrier(0);
  v4u qp;
  qp[0] = f2bf(q0[0]) | (f2bf(q0[1]) << 16); qp[1] = f2bf(q0[2]) | (f2bf(q0[3]) << 16);
  qp[2] = f2bf(q1[0]) | (f2bf(q1[1]) << 16); qp[3] = f2bf(q1[2]) | (f2bf(q1[3]) << 16);
  float mx = -INFINITY;
  constexpr int NIT = NL / WIN;
#pragma unroll 1
  for (int it = 0; it < NIT; ++it) {
    const bool lastk = it == NIT - 1;
    const int so = lastk ? sbase : sbase + (it + 1) * WIN * 1024;
    const __amdgpu_buffer_rsrc_t rr = lastk ? rv : rk;
#pragma unroll
    for (int g = 0; g < WIN / 16; ++g) {
      float x[16];
#pragma unroll
      for (int t = 0; t < 16; ++t) {
        const int j = g * 16 + t;
        float d = 0;
#pragma unroll
        for (int jj = 0; jj < 4; ++jj) d = dot2(a[j][jj], qp[jj], d);
        x[t] = d;
        __builtin_amdgcn_sched_barrier(0);
        a[j] = __builtin_amdgcn_raw_buffer_load_b128(rr, voff, so + j * 1024, AUXK);
        __builtin_amdgcn_sched_barrier(0);
      }
#define TR_STEP(HALF, CTRL, BIT) _Pragma("unroll") for (int hh = 0; hh < (HALF); ++hh) { \
      const float lo_ = x[hh], hi_ = x[hh + (HALF)]; const float t1_ = lo_ + dpp<CTRL>(lo_), t2_ = hi_ + dpp<CTRL>(hi_); x[hh] = (c & (BIT)) ? t2_ : t1_; }
      TR_STEP(8, XOR8, 8) TR_STEP(4, XOR7, 4) TR_STEP(2, XOR2, 2) TR_STEP(1, XOR1, 1)
#undef TR_STEP
      const float sc = rbf(rbf(x[0]) * 0.08838834764831845f);
      mx = fmaxf(mx, sc);
      sp[w][r][it * WIN + g * 16 + c] = sc;
    }
  }
  mx = wmax(mx);
  float l = 0;
#pragma unroll
  for (int g = 0; g < NL / 16; ++g) { float p = __expf(sp[w][r][g * 16 + c] - mx); l += p; sp[w][r][g * 16 + c] = p; }
  l = wsum(l);
  float acc[8] = {0, 0, 0, 0, 0, 0, 0, 0};
#pragma unroll 1
  for (int it = 0; it < NIT; ++it) {
    const bool lastv = it == NIT - 1;
    const __amdgpu_buffer_rsrc_t rr = lastv ? rz : rv;
    const int so = sbase + (it + 1) * WIN * 1024;
#pragma unroll
    for (int g = 0; g < WIN / 4; ++g) {
      const f32x4 p4 = *(const f32x4*)&sp[w][r][it * WIN + g * 4];
#pragma unroll
      for (int t = 0; t < 4; ++t) {
        const int j = g * 4 + t;
        const float pp = p4[t];
#pragma unroll
        for (int jj = 0; jj < 4; ++jj) { acc[2 * jj] += pp * __builtin_bit_cast(float, a[j][jj] << 16); acc[2 * jj + 1] += pp * __builtin_bit_cast(float, a[j][jj] & 0xffff0000u); }
        asm volatile("" : "+v"(acc[0]), "+v"(acc[1]), "+v"(acc[2]), "+v"(acc[3]), "+v"(acc[4]), "+v"(acc[5]), "+v"(acc[6]), "+v"(acc[7]));
        __builtin_amdgcn_sched_barrier(0);
        a[j] = __builtin_amdgcn_raw_buffer_load_b128(rr, voff, so + j * 1024, AUXV);
        __builtin_amdgcn_sched_barrier(0);
      }
    }
  }
  for (int j = 0; j < 8; ++j) { acc[j] += __shfl_xor(acc[j], 16); acc[j] += __shfl_xor(acc[j], 32); }
  if (r == 0) for (int j = 0; j < 8; ++j) sm[w][4 + 8 * c + j] = acc[j];
  if (lane == 0) { sm[w][0] = mx; sm[w][1] = l; }
  __syncthreads();
  if (tid < 128) {
    float M = sm[0][0]; for (int i = 1; i < NW; ++i) M = fmaxf(M, sm[i][0]);
    float L = 0, o = 0;
    for (int i = 0; i < NW; ++i) { float wt = __expf(sm[i][0] - M); L += wt * sm[i][1]; o += wt * sm[i][4 + tid]; }
    part[(size_t)b * 132 + 4 + tid] = o;
    if (tid == 0) { part[(size_t)b * 132] = M; part[(size_t)b * 132 + 1] = L; }
  }
}

__global__ void __launch_bounds__(256) k_wo(const float* part, const u16* wo, const u16* x, const float* mod, float* x1) {
  __shared__ float o[H * DH], red[32 * 64], y[64];
  for (int i = threadIdx.x; i < H * DH; i += 256) {
    int h = i >> 7, d = i & 127;
    float M = -INFINITY; for (int s = 0; s < NSPLIT; ++s) M = fmaxf(M, part[(h * NSPLIT + s) * 132]);
    float L = 0, a = 0;
    for (int s = 0; s < NSPLIT; ++s) { const float* pp = part + (h * NSPLIT + s) * 132; float wt = __expf(pp[0] - M); L += wt * pp[1]; a += wt * pp[4 + d]; }
    o[i] = rbf(a / L);
  }
  __syncthreads();
  gemv64<H * DH, D>(o, wo, blockIdx.x * 64, red, y);
  if (threadIdx.x < 64) { int n = blockIdx.x * 64 + threadIdx.x; x1[n] = rbf(bf2f(x[n]) + rbf(mod[2 * D + n] * rbf(y[threadIdx.x]))); }
}

__global__ void __launch_bounds__(256) k_ffn1(const float* x1, const float* mod, const u16* wg, const u16* wu, float* act) {
  __shared__ float xs[D], h[D], red[32 * 64], g[64], u[64];
  for (int i = threadIdx.x; i < D; i += 256) xs[i] = x1[i];
  __syncthreads();
  ln_mod(xs, mod + 4 * D, mod + 3 * D, h, red);
  gemv64<D, F>(h, wg, blockIdx.x * 64, red, g);
  gemv64<D, F>(h, wu, blockIdx.x * 64, red, u);
  if (threadIdx.x < 64) { float gg = rbf(g[threadIdx.x]); float sg = rbf(gg / (1.f + __expf(-gg))); act[blockIdx.x * 64 + threadIdx.x] = rbf(sg * rbf(u[threadIdx.x])); }
}

__global__ void __launch_bounds__(256) k_ffn2(const float* act, const float* x1, const float* mod, const u16* wd, u16* out) {
  __shared__ float a[F], red[32 * 64], y[64];
  for (int i = threadIdx.x; i < F; i += 256) a[i] = act[i];
  __syncthreads();
  gemv64<F, D>(a, wd, blockIdx.x * 64, red, y);
  if (threadIdx.x < 64) { int n = blockIdx.x * 64 + threadIdx.x; out[n] = (u16)f2bf(x1[n] + rbf(mod[5 * D + n] * rbf(y[threadIdx.x]))); }
}

extern "C" void kernel_launch(void* const* d_in, const int* in_sizes, int n_in, void* d_out, int out_size, void* d_ws, size_t ws_size,
                              hipStream_t stream) {
  const u16* x = (const u16*)d_in[0]; const u16* K = (const u16*)d_in[1]; const u16* V = (const u16*)d_in[2];
  const u16* wq = (const u16*)d_in[3]; const u16* wo = (const u16*)d_in[4];
  const u16* wg = (const u16*)d_in[6]; const u16* wu = (const u16*)d_in[7]; const u16* wd = (const u16*)d_in[8];
  const u16* c = (const u16*)d_in[9]; const u16* wmod = (const u16*)d_in[10];
  float* ws = (float*)d_ws;
  float* mod = ws; float* q = ws + 4096; float* part = ws + 8192; float* x1 = ws + 8192 + 256 * 132; float* act = x1 + 1024;
  k_mod<<<60, 256, 0, stream>>>(c, wmod, mod);
  k_q<<<64, 256, 0, stream>>>(x, mod, wq, q);
  hipFuncSetAttribute((const void*)k_attn, hipFuncAttributeMaxDynamicSharedMemorySize, 84 * 1024);
  k_attn<<<H * NSPLIT, NW * 64, 84 * 1024, stream>>>(q, K, V, part);
  k_wo<<<10, 256, 0, stream>>>(part, wo, x, mod, x1);
  k_ffn1<<<27, 256, 0, stream>>>(x1, mod, wg, wu, act);
  k_ffn2<<<10, 256, 0, stream>>>(act, x1, mod, wd, (u16*)d_out);
}
```

```cpp
#define PROBE 0
#include <hip/hip_runtime.h>
#include <stdint.h>

typedef unsigned short u16;
typedef unsigned v4u __attribute__((ext_vector_type(4)));
typedef unsigned v2u __attribute__((ext_vector_type(2)));
typedef float f32x4 __attribute__((ext_vector_type(4)));
typedef __bf16 bf16x2 __attribute__((ext_vector_type(2)));
typedef __amdgpu_buffer_rsrc_t rsrc_t;
#define LAS __attribute__((address_space(3)))
#define DI __device__ __forceinline__

#define WIN 32
#define AUXKV 2
#define DMA_PRE 12
#define RH 4
#define RM 4
#define SPIN_CAP (1 << 16)

constexpr int D = 640, SK = 16384, F = 1728;
constexpr int OFF_H = 256;
constexpr int OFF_Q = OFF_H + RH * 2560;
constexpr int OFF_M = OFF_Q + 32 * 512;
constexpr int PSTR = 72 * 16;
constexpr int OFF_P = OFF_M + RM * 10240;
constexpr int OFF_Y = OFF_P + 256 * PSTR;
constexpr int OFF_Z = OFF_Y + 8 * 5120;
constexpr int WS_BYTES = OFF_Z + 54 * 5120;
constexpr int OFF_STAMP = 4 << 20;
constexpr int NLMAX = 160;
constexpr int L_SP = 0;
constexpr int L_RED = L_SP + 16 * NLMAX * 4;
constexpr int L_X1 = L_RED + 4096;
constexpr int L_CB = L_X1 + 2560;
constexpr int L_SB = L_CB + 2560;
constexpr int L_HW = L_SB + 2560;
constexpr int L_W = L_HW + 4 * 768;
constexpr int L_DUMMY = L_W + 120 * 1024;
constexpr int LDS_BYTES = L_DUMMY + 1024;
static_assert(L_W % 16 == 0 && LDS_BYTES <= 160 * 1024, "lds");

DI float asf(unsigned u) { return __builtin_bit_cast(float, u); }
DI unsigned asu(float f) { return __builtin_bit_cast(unsigned, f); }
DI float lo16(unsigned w) { return asf(w << 16); }
DI float hi16(unsigned w) { return asf(w & 0xffff0000u); }
DI unsigned f2bf(float f) { unsigned u = asu(f); return (u + 0x7fffu + ((u >> 16) & 1u)) >> 16; }
DI float rbf(float f) { unsigned u = asu(f); return asf((u + 0x7fffu + ((u >> 16) & 1u)) & 0xffff0000u); }
DI unsigned pack2(float a, float b) { return f2bf(a) | (f2bf(b) << 16); }
DI float dot2(unsigned k, unsigned q, float acc) {
  return __builtin_amdgcn_fdot2_f32_bf16(__builtin_bit_cast(bf16x2, k), __builtin_bit_cast(bf16x2, q), acc, false);
}
template <int CTRL> DI float dpp(float x) { return asf(__builtin_amdgcn_mov_dpp(asu(x), CTRL, 0xf, 0xf, true)); }
constexpr int XOR1 = 0xB1, XOR2 = 0x4E, XOR7 = 0x141, XOR8 = 0x128, ROR4 = 0x124, SHL4 = 0x104;
DI float tr32(float a, float b) { v2u r = __builtin_amdgcn_permlane32_swap(asu(a), asu(b), false, false); unsigned p = r[0], q = r[1]; return asf(p) + asf(q); }
DI float tr16(float a, float b) { v2u r = __builtin_amdgcn_permlane16_swap(asu(a), asu(b), false, false); unsigned p = r[0], q = r[1]; return asf(p) + asf(q); }
DI float tr8(float a, float b, bool hi) { const float t1 = a + dpp<XOR8>(a), t2 = b + dpp<XOR8>(b); return hi ? t2 : t1; }
DI float wsum(float v) {
  v += dpp<XOR1>(v); v += dpp<XOR2>(v); v += dpp<XOR7>(v); v += dpp<XOR8>(v); v = tr16(v, v); return tr32(v, v);
}
DI float mx16(float a) { v2u r = __builtin_amdgcn_permlane16_swap(asu(a), asu(a), false, false); unsigned p = r[0], q = r[1]; return fmaxf(asf(p), asf(q)); }
DI float mx32(float a) { v2u r = __builtin_amdgcn_permlane32_swap(asu(a), asu(a), false, false); unsigned p = r[0], q = r[1]; return fmaxf(asf(p), asf(q)); }
DI float wmax(float v) {
  v = fmaxf(v, dpp<XOR1>(v)); v = fmaxf(v, dpp<XOR2>(v)); v = fmaxf(v, dpp<XOR7>(v)); v = fmaxf(v, dpp<XOR8>(v)); return mx32(mx16(v));
}
DI float silu(float v) { return v * __builtin_amdgcn_rcpf(1.f + __expf(-v)); }
DI unsigned plo(unsigned a, unsigned b) { return __builtin_amdgcn_perm(b, a, 0x05040100u); }
DI unsigned phi(unsigned a, unsigned b) { return __builtin_amdgcn_perm(b, a, 0x07060302u); }
struct P8 { unsigned p[8]; };
DI P8 mkpairs(v4u a, v4u b) {
  P8 r;
#pragma unroll
  for (int i = 0; i < 4; ++i) { const unsigned x = a[i], y = b[i]; r.p[2 * i] = plo(x, y); r.p[2 * i + 1] = phi(x, y); }
  return r;
}
DI rsrc_t mkrsrc(const void* p, unsigned bytes) { return __builtin_amdgcn_make_buffer_rsrc((void*)p, 0, bytes, 0x00020000); }
DI v4u ldg(rsrc_t r, int off) { return __builtin_amdgcn_raw_buffer_load_b128(r, off, 0, 16); }
DI void stg(rsrc_t r, int off, unsigned a, unsigned b, unsigned tag) { v4u g = {a, tag, b, tag}; __builtin_amdgcn_raw_buffer_store_b128(g, r, off, 0, 16); }
DI bool okg(v4u g, unsigned tag) { const unsigned a = g[1], b = g[3]; return a == tag && b == tag; }
DI void fence_c() { asm volatile("" ::: "memory"); }
DI void dma16(rsrc_t r, unsigned voff, unsigned soff, unsigned lds) {
  unsigned keep;
  asm volatile("s_mov_b32 %0, m0\n\ts_mov_b32 m0, %1\n\ts_nop 0\n\tbuffer_load_dwordx4 %2, %3, %4 offen lds\n\ts_mov_b32 m0, %0"
               : "=&s"(keep) : "s"(lds), "v"(voff), "s"(r), "s"(soff) : "memory");
}
DI void bar() { asm volatile("s_waitcnt lgkmcnt(0)\n\ts_barrier" ::: "memory"); }
DI float red8(const float (&a)[8], int lane) {
  const float b0 = tr32(a[0], a[4]), b1 = tr32(a[1], a[5]), b2 = tr32(a[2], a[6]), b3 = tr32(a[3], a[7]);
  const float c0 = tr16(b0, b2), c1 = tr16(b1, b3);
  return tr8(c0, c1, lane & 8);
}
DI int red8col(int lane) { return ((lane >> 5) & 1) * 4 + ((lane >> 4) & 1) * 2 + ((lane >> 3) & 1); }

#define ST(i) do { if ((i) == 0) pt0 = __builtin_amdgcn_s_memrealtime(); if ((i) == PROBE) pt1 = __builtin_amdgcn_s_memrealtime(); } while (0)

__global__ void __launch_bounds__(256, 1)
mega(const u16* __restrict__ x, const u16* __restrict__ K, const u16* __restrict__ V, const u16* __restrict__ wq, const u16* __restrict__ wo,
     const u16* __restrict__ wg, const u16* __restrict__ wu, const u16* __restrict__ wd, const u16* __restrict__ c, const u16* __restrict__ wmod,
     u16* __restrict__ out, unsigned* ws) {
  extern __shared__ __attribute__((aligned(16))) char smem[];
  const int tid = threadIdx.x, lane = tid & 63, w = __builtin_amdgcn_readfirstlane(tid >> 6), b = blockIdx.x;
  const int xc = b & 7, head = b >> 3, slot = (head + 4 * xc) & 31;
  unsigned long long pt0 = 0, pt1 = 0;
  ST(0);
  const rsrc_t rw = mkrsrc(ws, WS_BYTES);
  const unsigned ldsb = (unsigned)(size_t)(LAS char*)smem;
  const unsigned ew = __builtin_amdgcn_raw_buffer_load_b32(rw, 0, 0, 16);
  const bool isQ = slot < 8, isM1 = slot >= 8 && slot < 13, isF = slot >= 13 && slot < 20 && 7 * xc + slot - 13 < 54, isW = slot >= 20 && slot < 30;
  unsigned tag = 0;
  unsigned sw[5];

  if (isM1) {
    const int mi = 5 * xc + slot - 8, wh = w & 1, rho = lane >> 1, cq = lane & 1;
    const rsrc_t rm = mkrsrc(wmod, D * 6 * D * 2);
    const unsigned* cw = (const unsigned*)c + 160 * wh + 5 * rho;
    unsigned cv[5], xv[5];
#pragma unroll
    for (int i = 0; i < 5; ++i) cv[i] = cw[i];
#pragma unroll
    for (int i = 0; i < 5; ++i) xv[i] = ((const unsigned*)x)[5 * lane + i];
    const unsigned xo = x[16 * mi + (lane & 15)];
    const int vo = (320 * wh + 10 * rho) * 7680 + ((w >> 1) * D + 16 * mi + 8 * cq) * 2;
    v4u wv[10];
#pragma unroll
    for (int i = 0; i < 10; ++i) wv[i] = __builtin_amdgcn_raw_buffer_load_b128(rm, vo, i * 7680, 0);
    __builtin_amdgcn_sched_barrier(0);
#pragma unroll
    for (int i = 0; i < 5; ++i) sw[i] = pack2(silu(lo16(cv[i])), silu(hi16(cv[i])));
    float s = 0;
#pragma unroll
    for (int i = 0; i < 5; ++i) s += lo16(xv[i]) + hi16(xv[i]);
    const float mean = wsum(s) * (1.f / D);
    float v = 0;
#pragma unroll
    for (int i = 0; i < 5; ++i) { const float d0 = lo16(xv[i]) - mean, d1 = hi16(xv[i]) - mean; v += d0 * d0 + d1 * d1; }
    const float var = wsum(v) * (1.f / D);
    const float rs = __builtin_amdgcn_rcpf(rbf(__builtin_sqrtf(rbf(rbf(var) + 1e-5f))));
    const float nrm = rbf(rbf(lo16(xo) - rbf(mean)) * rs);
    float acc[8] = {0, 0, 0, 0, 0, 0, 0, 0};
#pragma unroll
    for (int i = 0; i < 5; ++i) {
      const P8 p = mkpairs(wv[2 * i], wv[2 * i + 1]);
#pragma unroll
      for (int j = 0; j < 8; ++j) acc[j] = dot2(p.p[j], sw[i], acc[j]);
    }
    float r = red8(acc, lane);
    r += dpp<SHL4>(r); r += dpp<XOR2>(r);
    float* red = (float*)(smem + L_RED);
    if ((lane & 6) == 0) red[w * 16 + cq * 8 + red8col(lane)] = r;
    bar();
    if (w == 0) {
      const int col = lane & 15;
      const float sa = rbf(red[col] + red[16 + col]), ca = rbf(red[32 + col] + red[48 + col]);
      const float hv = rbf(rbf(nrm * rbf(1.f + ca)) + sa);
      const unsigned hb = asu(hv) >> 16;
      const unsigned hw = hb | (asu(dpp<XOR1>(hv)) & 0xffff0000u);
      const unsigned hw2 = asu(dpp<XOR2>(asf(hw)));
      tag = __builtin_amdgcn_readfirstlane(ew) + 1;
      if ((lane & 3) == 0 && (lane >> 4) < RH) stg(rw, OFF_H + (lane >> 4) * 2560 + (4 * mi + ((lane & 15) >> 2)) * 16, hw, hw2, tag);
    }
    ST(1);
  } else if (isQ) {
    const int qi = 8 * xc + slot, rho = lane >> 3, cq = lane & 7;
    const rsrc_t rq = mkrsrc(wq, D * 4096 * 2);
    const int vo = (160 * w + 20 * rho) * 8192 + (64 * qi + 8 * cq) * 2;
    v4u wv[20];
#pragma unroll
    for (int i = 0; i < 20; ++i) wv[i] = __builtin_amdgcn_raw_buffer_load_b128(rq, vo, i * 8192, 0);
    __builtin_amdgcn_sched_barrier(0);
    unsigned pr[80];
#pragma unroll
    for (int i = 0; i < 10; ++i) {
      const P8 p = mkpairs(wv[2 * i], wv[2 * i + 1]);
#pragma unroll
      for (int j = 0; j < 8; ++j) pr[8 * i + j] = p.p[j];
    }
    tag = __builtin_amdgcn_readfirstlane(ew) + 1;
    ST(1);
    v4u g[5];
    const int go = OFF_H + (xc % RH) * 2560 + (40 * w + 5 * rho) * 16;
    for (int spin = 0; spin < SPIN_CAP; ++spin) {
      fence_c();
      bool ok = true;
#pragma unroll
      for (int i = 0; i < 5; ++i) { g[i] = ldg(rw, go + i * 16); }
#pragma unroll
      for (int i = 0; i < 5; ++i) ok &= okg(g[i], tag);
      if (__all(ok)) break;
      __builtin_amdgcn_s_sleep(1);
    }
    ST(2);
    float acc[8] = {0, 0, 0, 0, 0, 0, 0, 0};
#pragma unroll
    for (int i = 0; i < 10; ++i) {
      const unsigned hw = g[i >> 1][(i & 1) * 2];
#pragma unroll
      for (int j = 0; j < 8; ++j) acc[j] = dot2(pr[8 * i + j], hw, acc[j]);
    }
    const float r = red8(acc, lane);
    float* red = (float*)(smem + L_RED);
    red[w * 64 + cq * 8 + red8col(lane)] = r;
    bar();
    if (w == 0 && lane < 16) {
      const f32x4 a0 = *(const f32x4*)(red + 4 * lane), a1 = *(const f32x4*)(red + 64 + 4 * lane), a2 = *(const f32x4*)(red + 128 + 4 * lane), a3 = *(const f32x4*)(red + 192 + 4 * lane);
      const f32x4 s = (a0 + a1) + (a2 + a3);
      stg(rw, OFF_Q + (16 * qi + lane) * 16, pack2(s[0], s[1]), pack2(s[2], s[3]), tag);
    }
    ST(3);
  }
  __builtin_amdgcn_sched_barrier(0);

  const int nkeys = 2048, key0 = (b & 7) * 2048;
  const int NLD = nkeys >> 4;
  const rsrc_t rk = mkrsrc(K + ((size_t)head * SK + key0) * 128, nkeys * 256);
  const rsrc_t rv = mkrsrc(V + ((size_t)head * SK + key0) * 128, nkeys * 256);
  const rsrc_t rz = mkrsrc(V, 0);
  unsigned cvm[5];
  if (isW) {
#pragma unroll
    for (int i = 0; i < 5; ++i) cvm[i] = ((const unsigned*)c)[80 * w + 5 * (lane >> 2) + i];
  }
  __builtin_amdgcn_sched_barrier(0);
  const int voff = lane * 16 + w * 4096;
  v4u a[WIN];
#pragma unroll
  for (int i = 0; i < WIN; ++i) a[i] = __builtin_amdgcn_raw_buffer_load_b128(rk, voff + (i & 3) * 1024, (i >> 2) * 16384, AUXKV);
  __builtin_amdgcn_sched_barrier(0);

  auto role_dma = [&](int i0, int i1) {
    if (isW) {
      const int C = slot - 20, m2 = 10 * xc + C;
      const rsrc_t ro = mkrsrc(wo, 4096 * D * 2), rm = mkrsrc(wmod, D * 6 * D * 2);
      const unsigned vo = ((512 * xc + 128 * w + 16 * (lane >> 3)) * D + 64 * C + 8 * (lane & 7)) * 2;
      const unsigned vm = (160 * w + 10 * (lane >> 2)) * 7680 + (2 * D + 32 * m2 + 8 * (lane & 3)) * 2;
#pragma unroll
      for (int i = 0; i < 26; ++i) if (i >= i0 && i < i1) {
        if (i < 10) dma16(rm, vm, i * 7680, ldsb + L_W + 65536 + (w * 10 + i) * 1024);
        else dma16(ro, vo, (i - 10) * 1280, ldsb + L_W + (w * 16 + i - 10) * 1024);
      }
    } else if (isF) {
      const int fj = 7 * xc + slot - 13;
      const rsrc_t rg = mkrsrc(w < 2 ? wg : wu, D * F * 2), rd = mkrsrc(wd, F * D * 2);
      const unsigned vg = ((320 * (w & 1) + 20 * (lane >> 2)) * F + 32 * fj + 8 * (lane & 3)) * 2;
      const unsigned vd = (32 * fj * D) * 2 + w * 10240 + lane * 16;
#pragma unroll
      for (int i = 0; i < 30; ++i) if (i >= i0 && i < i1) {
        if (i < 20) dma16(rg, vg, i * (F * 2), ldsb + L_W + (w * 20 + i) * 1024);
        else dma16(rd, vd, (i - 20) * 1024, ldsb + L_W + 81920 + (w * 10 + i - 20) * 1024);
      }
    }
  };
  role_dma(0, DMA_PRE);
  if (isW) {
#pragma unroll
    for (int i = 0; i < 5; ++i) sw[i] = pack2(silu(lo16(cvm[i])), silu(hi16(cvm[i])));
  }
  tag = __builtin_amdgcn_readfirstlane(ew) + 1;
  const int r4 = lane >> 4, cc = lane & 15;
  v4u qp;
  {
    v4u g0, g1;
    for (int spin = 0; spin < SPIN_CAP; ++spin) {
      fence_c();
      g0 = ldg(rw, OFF_Q + head * 512 + cc * 32); g1 = ldg(rw, OFF_Q + head * 512 + cc * 32 + 16);
      if (__all(okg(g0, tag) && okg(g1, tag))) break;
      __builtin_amdgcn_s_sleep(1);
    }
    qp[0] = g0[0]; qp[1] = g0[2]; qp[2] = g1[0]; qp[3] = g1[2];
  }
  ST(4);
  role_dma(DMA_PRE, 30);
  __builtin_amdgcn_sched_barrier(0);

  float* sp = (float*)(smem + L_SP) + (w * 4 + r4) * NLMAX;
  float mx = -INFINITY;
  const int NIT = (NLD + WIN - 1) / WIN;
#pragma unroll 1
  for (int it = 0; it < NIT; ++it) {
    const bool lastk = it == NIT - 1;
    const int so = lastk ? 0 : (it + 1) * (WIN / 4) * 16384;
    const rsrc_t rr = lastk ? rv : rk;
#pragma unroll
    for (int g = 0; g < WIN / 16; ++g) {
      float xs[16];
#pragma unroll
      for (int t = 0; t < 16; ++t) {
        const int j = g * 16 + t;
        float d = 0;
#pragma unroll
        for (int jj = 0; jj < 4; ++jj) d = dot2(a[j][jj], qp[jj], d);
        xs[t] = d;
        __builtin_amdgcn_sched_barrier(0);
        a[j] = __builtin_amdgcn_raw_buffer_load_b128(rr, voff + (j & 3) * 1024, so + (j >> 2) * 16384, AUXKV);
        __builtin_amdgcn_sched_barrier(0);
      }
#define TR_STEP(HALF, CTRL, BIT) _Pragma("unroll") for (int hh = 0; hh < (HALF); ++hh) { \
      const float lo_ = xs[hh], hi_ = xs[hh + (HALF)]; const float t1_ = lo_ + dpp<CTRL>(lo_), t2_ = hi_ + dpp<CTRL>(hi_); xs[hh] = (cc & (BIT)) ? t2_ : t1_; }
      TR_STEP(8, XOR8, 8) TR_STEP(4, XOR7, 4) TR_STEP(2, XOR2, 2) TR_STEP(1, XOR1, 1)
#undef TR_STEP
      const int li = it * WIN + g * 16 + cc;
      const float sc = li < NLD ? rbf(rbf(xs[0]) * 0.08838834764831845f) : -INFINITY;
      mx = fmaxf(mx, sc);
      sp[li] = sc;
    }
  }
  ST(5);
  mx = wmax(mx);
  float l = 0;
  for (int g = 0; g < NIT * (WIN / 16); ++g) { const float p = __expf(sp[g * 16 + cc] - mx); l += p; sp[g * 16 + cc] = p; }
  l = wsum(l);

  if (isW) {
    const int m2 = 10 * xc + slot - 20, cq = lane & 3;
    const v4u* wl = (const v4u*)(smem + L_W + 65536 + w * 10240) + lane;
    float acc[8] = {0, 0, 0, 0, 0, 0, 0, 0};
#pragma unroll
    for (int i = 0; i < 5; ++i) {
      const P8 p = mkpairs(wl[(2 * i) * 64], wl[(2 * i + 1) * 64]);
#pragma unroll
      for (int j = 0; j < 8; ++j) acc[j] = dot2(p.p[j], sw[i], acc[j]);
    }
    float r = red8(acc, lane);
    r += dpp<SHL4>(r);
    float* red = (float*)(smem + L_RED);
    if ((lane & 4) == 0) red[w * 32 + cq * 8 + red8col(lane)] = r;
    bar();
    if (w == 0) {
      const int g = lane & 7, rep = lane >> 3;
      const f32x4 a0 = *(const f32x4*)(red + 4 * g), a1 = *(const f32x4*)(red + 32 + 4 * g), a2 = *(const f32x4*)(red + 64 + 4 * g), a3 = *(const f32x4*)(red + 96 + 4 * g);
      const f32x4 s = (a0 + a1) + (a2 + a3);
      if (rep < RM) stg(rw, OFF_M + rep * 10240 + (8 * m2 + g) * 16, pack2(s[0], s[1]), pack2(s[2], s[3]), tag);
    }
    bar();
  }

  float acc[8] = {0, 0, 0, 0, 0, 0, 0, 0};
#pragma unroll 1
  for (int it = 0; it < NIT; ++it) {
    const bool lastv = it == NIT - 1;
    const rsrc_t rr = lastv ? rz : rv;
    const int so = (it + 1) * (WIN / 4) * 16384;
#pragma unroll
    for (int g = 0; g < WIN / 4; ++g) {
      const f32x4 p4 = *(const f32x4*)&sp[it * WIN + g * 4];
#pragma unroll
      for (int t = 0; t < 4; ++t) {
        const int j = g * 4 + t;
        const float pp = p4[t];
#pragma unroll
        for (int jj = 0; jj < 4; ++jj) { const unsigned wv = a[j][jj]; acc[2 * jj] += pp * lo16(wv); acc[2 * jj + 1] += pp * hi16(wv); }
        asm volatile("" : "+v"(acc[0]), "+v"(acc[1]), "+v"(acc[2]), "+v"(acc[3]), "+v"(acc[4]), "+v"(acc[5]), "+v"(acc[6]), "+v"(acc[7]));
        __builtin_amdgcn_sched_barrier(0);
        a[j] = __builtin_amdgcn_raw_buffer_load_b128(rr, voff + (j & 3) * 1024, so + (j >> 2) * 16384, AUXKV);
        __builtin_amdgcn_sched_barrier(0);
      }
    }
  }
  ST(6);
  {
    float* sm = (float*)(smem + L_RED);
#pragma unroll
    for (int j = 0; j < 8; ++j) { acc[j] = tr16(acc[j], acc[j]); acc[j] = tr32(acc[j], acc[j]); }
    if (r4 == 0) { *(f32x4*)(sm + w * 132 + 4 + 8 * cc) = f32x4{acc[0], acc[1], acc[2], acc[3]}; *(f32x4*)(sm + w * 132 + 8 + 8 * cc) = f32x4{acc[4], acc[5], acc[6], acc[7]}; }
    if (lane == 0) { sm[w * 132] = mx; sm[w * 132 + 1] = l; }
    asm volatile("s_waitcnt vmcnt(0)" ::: "memory");
    bar();
    if (w < 2) {
      const float m0 = sm[0], m1 = sm[132], m2 = sm[264], m3 = sm[396];
      const float M = fmaxf(fmaxf(m0, m1), fmaxf(m2, m3));
      const float e0 = __expf(m0 - M), e1 = __expf(m1 - M), e2 = __expf(m2 - M), e3 = __expf(m3 - M);
      const int po = OFF_P + b * PSTR;
      if (w == 0) {
        const float* s0 = sm + 4 + 2 * lane;
        const float o0 = e0 * s0[0] + e1 * s0[132] + e2 * s0[264] + e3 * s0[396], o1 = e0 * s0[1] + e1 * s0[133] + e2 * s0[265] + e3 * s0[397];
        stg(rw, po + lane * 16, asu(o0), asu(o1), tag);
      } else if (lane == 0) {
        stg(rw, po + 64 * 16, asu(M), asu(e0 * sm[1] + e1 * sm[133] + e2 * sm[265] + e3 * sm[397]), tag);
      }
    }
    bar();
  }
  ST(7);

  if (isW) {
    const int C = slot - 20, hh = 4 * xc + w, cq = lane & 7;
    const v4u* wl = (const v4u*)(smem + L_W + w * 16384) + lane;
    unsigned pr[64];
#pragma unroll
    for (int i = 0; i < 8; ++i) {
      const P8 p = mkpairs(wl[(2 * i) * 64], wl[(2 * i + 1) * 64]);
#pragma unroll
      for (int j = 0; j < 8; ++j) pr[8 * i + j] = p.p[j];
    }
    const int po = OFF_P + hh * 8 * PSTR;
    v4u g[8], ml;
    bool have[9] = {false, false, false, false, false, false, false, false, false};
    for (int spin = 0; spin < SPIN_CAP; ++spin) {
      fence_c();
#pragma unroll
      for (int s = 0; s < 8; ++s) if (!have[s]) g[s] = ldg(rw, po + s * PSTR + lane * 16);
      if (!have[8]) ml = ldg(rw, po + (lane & 7) * PSTR + 64 * 16);
      bool all = true;
#pragma unroll
      for (int s = 0; s < 8; ++s) { have[s] = __all(okg(g[s], tag)); all &= have[s]; }
      have[8] = __all(okg(ml, tag)); all &= have[8];
      if (all) break;
      __builtin_amdgcn_s_sleep(1);
    }
    ST(8);
    const unsigned mu = ml[0], lu = ml[2];
    const float ms = asf(mu);
    float M = fmaxf(ms, dpp<XOR1>(ms)); M = fmaxf(M, dpp<XOR2>(M)); M = fmaxf(M, dpp<XOR7>(M));
    const float e = __expf(ms - M);
    float L = e * asf(lu); L += dpp<XOR1>(L); L += dpp<XOR2>(L); L += dpp<XOR7>(L);
    const float f = e * __builtin_amdgcn_rcpf(L);
    float o0 = 0, o1 = 0;
#pragma unroll
    for (int s = 0; s < 8; ++s) {
      const float fs = asf(__builtin_amdgcn_readlane(asu(f), s));
      const unsigned u0 = g[s][0], u1 = g[s][2];
      o0 += fs * asf(u0); o1 += fs * asf(u1);
    }
    unsigned* hw = (unsigned*)(smem + L_HW) + w * 192;
    hw[lane] = pack2(o0, o1);
    fence_c();
    const v4u h0 = *(const v4u*)(hw + 8 * (lane >> 3)), h1 = *(const v4u*)(hw + 8 * (lane >> 3) + 4);
    float y[8] = {0, 0, 0, 0, 0, 0, 0, 0};
#pragma unroll
    for (int i = 0; i < 8; ++i) {
      const unsigned hv = i < 4 ? h0[i & 3] : h1[i & 3];
#pragma unroll
      for (int j = 0; j < 8; ++j) y[j] = dot2(pr[8 * i + j], hv, y[j]);
    }
    const float r = red8(y, lane);
    float* red = (float*)(smem + L_RED);
    red[w * 64 + cq * 8 + red8col(lane)] = r;
    bar();
    if (w == 0 && lane < 32) {
      const float* rp = red + 2 * lane;
      const float y0 = (rp[0] + rp[64]) + (rp[128] + rp[192]), y1 = (rp[1] + rp[65]) + (rp[129] + rp[193]);
      stg(rw, OFF_Y + (xc * 320 + 32 * C + lane) * 16, asu(y0), asu(y1), tag);
    }
    ST(9);
  } else if (isF) {
    const int fj = 7 * xc + slot - 13, wh = w & 1, rho = lane >> 2, cq = lane & 3;
    unsigned pr[80], pd[64];
    {
      const v4u* wl = (const v4u*)(smem + L_W + w * 20480) + lane;
#pragma unroll
      for (int i = 0; i < 10; ++i) {
        const P8 p = mkpairs(wl[(2 * i) * 64], wl[(2 * i + 1) * 64]);
#pragma unroll
        for (int j = 0; j < 8; ++j) pr[8 * i + j] = p.p[j];
      }
      const int tc = tid < 160 ? tid : 0;
      const v2u* dl = (const v2u*)(smem + L_W + 81920) + tc;
#pragma unroll
      for (int i = 0; i < 16; ++i) {
        const v2u r0 = dl[(2 * i) * 160], r1 = dl[(2 * i + 1) * 160];
        const unsigned a0 = r0[0], a1 = r0[1], b0 = r1[0], b1 = r1[1];
        pd[4 * i] = plo(a0, b0); pd[4 * i + 1] = phi(a0, b0); pd[4 * i + 2] = plo(a1, b1); pd[4 * i + 3] = phi(a1, b1);
      }
    }
    float* x1s = (float*)(smem + L_X1); float* cbs = (float*)(smem + L_CB); float* sbs = (float*)(smem + L_SB);
    if (tid < 160) {
      const int mo = OFF_M + (xc % RM) * 10240 + tid * 16;
      v4u ga, sb, cb;
      for (int spin = 0; spin < SPIN_CAP; ++spin) {
        fence_c();
        ga = ldg(rw, mo); sb = ldg(rw, mo + 2560); cb = ldg(rw, mo + 5120);
        if (__all(okg(ga, tag) && okg(sb, tag) && okg(cb, tag))) break;
        __builtin_amdgcn_s_sleep(1);
      }
      const v2u xw = *(const v2u*)(x + 4 * tid);
      { const unsigned c0 = cb[0], c1 = cb[2], s0 = sb[0], s1 = sb[2];
        *(f32x4*)(cbs + 4 * tid) = f32x4{rbf(1.f + lo16(c0)), rbf(1.f + hi16(c0)), rbf(1.f + lo16(c1)), rbf(1.f + hi16(c1))};
        *(f32x4*)(sbs + 4 * tid) = f32x4{lo16(s0), hi16(s0), lo16(s1), hi16(s1)}; }
      v4u g[16];
      bool have[8] = {false, false, false, false, false, false, false, false};
      for (int spin = 0; spin < SPIN_CAP; ++spin) {
        fence_c();
#pragma unroll
        for (int G = 0; G < 8; ++G) if (!have[G]) { g[2 * G] = ldg(rw, OFF_Y + (G * 320 + 2 * tid) * 16); g[2 * G + 1] = ldg(rw, OFF_Y + (G * 320 + 2 * tid + 1) * 16); }
        bool all = true;
#pragma unroll
        for (int G = 0; G < 8; ++G) { have[G] = __all(okg(g[2 * G], tag) && okg(g[2 * G + 1], tag)); all &= have[G]; }
        if (all) break;
        __builtin_amdgcn_s_sleep(1);
      }
      float y[4] = {0, 0, 0, 0};
#pragma unroll
      for (int G = 0; G < 8; ++G) { const unsigned u0 = g[2 * G][0], u1 = g[2 * G][2], u2 = g[2 * G + 1][0], u3 = g[2 * G + 1][2]; y[0] += asf(u0); y[1] += asf(u1); y[2] += asf(u2); y[3] += asf(u3); }
      const unsigned g0 = ga[0], g1 = ga[2], x0 = xw[0], x1w = xw[1];
      *(f32x4*)(x1s + 4 * tid) = f32x4{rbf(lo16(x0) + rbf(lo16(g0) * rbf(y[0]))), rbf(hi16(x0) + rbf(hi16(g0) * rbf(y[1]))),
                                       rbf(lo16(x1w) + rbf(lo16(g1) * rbf(y[2]))), rbf(hi16(x1w) + rbf(hi16(g1) * rbf(y[3])))};
    }
    ST(8);
    bar();
    float xa[5], xb[5];
#pragma unroll
    for (int i = 0; i < 5; ++i) { xa[i] = x1s[5 * lane + i]; xb[i] = x1s[320 + 5 * lane + i]; }
    float s = 0;
#pragma unroll
    for (int i = 0; i < 5; ++i) s += xa[i] + xb[i];
    const float mean = wsum(s) * (1.f / D);
    float v = 0;
#pragma unroll
    for (int i = 0; i < 5; ++i) { const float d0 = xa[i] - mean, d1 = xb[i] - mean; v += d0 * d0 + d1 * d1; }
    const float var = wsum(v) * (1.f / D);
    const float rs = __builtin_amdgcn_rcpf(rbf(__builtin_sqrtf(rbf(rbf(var) + 1e-5f)))), mb = rbf(mean);
    u16* hs = (u16*)((unsigned*)(smem + L_HW) + w * 192);
#pragma unroll
    for (int i = 0; i < 5; ++i) {
      const int col = 320 * wh + 5 * lane + i;
      const float xx = wh ? xb[i] : xa[i];
      hs[5 * lane + i] = (u16)f2bf(rbf(rbf(rbf(xx - mb) * rs) * cbs[col]) + sbs[col]);
    }
    fence_c();
    const v2u* hp = (const v2u*)((const unsigned*)hs + 10 * rho);
    float ga8[8] = {0, 0, 0, 0, 0, 0, 0, 0};
#pragma unroll
    for (int i = 0; i < 5; ++i) {
      const v2u hv = hp[i];
      const unsigned h0 = hv[0], h1 = hv[1];
#pragma unroll
      for (int j = 0; j < 8; ++j) ga8[j] = dot2(pr[16 * i + j], h0, ga8[j]);
#pragma unroll
      for (int j = 0; j < 8; ++j) ga8[j] = dot2(pr[16 * i + 8 + j], h1, ga8[j]);
    }
    float r = red8(ga8, lane);
    r += dpp<SHL4>(r);
    float* red = (float*)(smem + L_RED);
    if ((lane & 4) == 0) red[w * 32 + cq * 8 + red8col(lane)] = r;
    bar();
    const int col = lane & 31;
    const float gg = rbf(red[col] + red[32 + col]), uu = rbf(red[64 + col] + red[96 + col]);
    const float av = rbf(rbf(silu(gg)) * uu);
    const unsigned aw = (asu(av) >> 16) | (asu(dpp<XOR1>(av)) & 0xffff0000u);
    float z[4] = {0, 0, 0, 0};
#pragma unroll
    for (int i = 0; i < 16; ++i) {
      const unsigned as = __builtin_amdgcn_readlane(aw, 2 * i);
#pragma unroll
      for (int j = 0; j < 4; ++j) z[j] = dot2(pd[4 * i + j], as, z[j]);
    }
    if (tid < 160) {
      stg(rw, OFF_Z + (fj * 320 + 2 * tid) * 16, asu(z[0]), asu(z[1]), tag);
      stg(rw, OFF_Z + (fj * 320 + 2 * tid + 1) * 16, asu(z[2]), asu(z[3]), tag);
    }
    ST(9);
  } else if (isM1) {
    const int fi = 5 * xc + slot - 8, g8 = lane & 7;
    float x1a = 0, x1b = 0, gb0 = 0, gb1 = 0;
    if (w == 0) {
      const int mo = OFF_M + (xc % RM) * 10240 + (4 * fi + (g8 >> 1)) * 16;
      v4u ga, gb, yy;
      for (int spin = 0; spin < SPIN_CAP; ++spin) {
        fence_c();
        ga = ldg(rw, mo); gb = ldg(rw, mo + 7680); yy = ldg(rw, OFF_Y + ((lane >> 3) * 320 + 8 * fi + g8) * 16);
        if (__all(okg(ga, tag) && okg(gb, tag) && okg(yy, tag))) break;
        __builtin_amdgcn_s_sleep(1);
      }
      const unsigned xw = ((const unsigned*)x)[8 * fi + g8];
      const unsigned gaw = (g8 & 1) ? ga[2] : ga[0], gbw = (g8 & 1) ? gb[2] : gb[0], u0 = yy[0], u1 = yy[2];
      float y0 = 0, y1 = 0;
#pragma unroll
      for (int G = 0; G < 8; ++G) { y0 += __shfl(asf(u0), G * 8 + g8); y1 += __shfl(asf(u1), G * 8 + g8); }
      x1a = rbf(lo16(xw) + rbf(lo16(gaw) * rbf(y0))); x1b = rbf(hi16(xw) + rbf(hi16(gaw) * rbf(y1)));
      gb0 = lo16(gbw); gb1 = hi16(gbw);
    }
    ST(8);
    const int p0 = tid >> 3;
    v4u z0, z1 = {0, 0, 0, 0};
    bool h0 = false, h1 = false;
    for (int spin = 0; spin < SPIN_CAP; ++spin) {
      fence_c();
      if (!h0) z0 = ldg(rw, OFF_Z + (p0 * 320 + 8 * fi + g8) * 16);
      if (!h1 && p0 < 22) z1 = ldg(rw, OFF_Z + ((p0 + 32) * 320 + 8 * fi + g8) * 16);
      h0 = __all(okg(z0, tag)); h1 = __all(p0 >= 22 || okg(z1, tag));
      if (h0 && h1) break;
      __builtin_amdgcn_s_sleep(1);
    }
    ST(9);
    const unsigned u0 = z0[0], u1 = z0[2], u2 = z1[0], u3 = z1[2];
    float s0 = asf(u0) + asf(u2), s1 = asf(u1) + asf(u3);
    s0 += dpp<XOR8>(s0); s1 += dpp<XOR8>(s1);
    s0 = tr16(s0, s0); s1 = tr16(s1, s1); s0 = tr32(s0, s0); s1 = tr32(s1, s1);
    float* red = (float*)(smem + L_RED);
    if (lane < 8) { red[w * 16 + 2 * lane] = s0; red[w * 16 + 2 * lane + 1] = s1; }
    bar();
    if (w == 0 && lane < 8) {
      const float* rp = red + 2 * lane;
      const float y0 = (rp[0] + rp[16]) + (rp[32] + rp[48]), y1 = (rp[1] + rp[17]) + (rp[33] + rp[49]);
      ((unsigned*)out)[8 * fi + lane] = pack2(x1a + rbf(gb0 * rbf(y0)), x1b + rbf(gb1 * rbf(y1)));
      if (fi == 0 && lane == 0) __builtin_amdgcn_raw_buffer_store_b32(tag, rw, 0, 0, 16);
    }
    ST(10);
    if (fi == 0 && w == 0) {
      const unsigned long long te = __builtin_amdgcn_s_memrealtime(), d = PROBE == 0 ? 0 : pt1 - pt0;
      for (int i = 0; i < (1 << 20); ++i) { if (__builtin_amdgcn_s_memrealtime() - te >= d) break; __builtin_amdgcn_s_sleep(1); }
    }
  }
}

extern "C" void kernel_launch(void* const* d_in, const int* in_sizes, int n_in, void* d_out, int out_size, void* d_ws, size_t ws_size,
                              hipStream_t stream) {
  hipFuncSetAttribute((const void*)mega, hipFuncAttributeMaxDynamicSharedMemorySize, LDS_BYTES);
  mega<<<256, 256, LDS_BYTES, stream>>>((const u16*)d_in[0], (const u16*)d_in[1], (const u16*)d_in[2], (const u16*)d_in[3], (const u16*)d_in[4],
                                        (const u16*)d_in[6], (const u16*)d_in[7], (const u16*)d_in[8], (const u16*)d_in[9], (const u16*)d_in[10],
                                        (u16*)d_out, (unsigned*)d_ws);
}
```

```cpp
#define PROBE 0
#define WBSTART 2
#include <hip/hip_runtime.h>
#include <stdint.h>

typedef unsigned short u16;
typedef unsigned v4u __attribute__((ext_vector_type(4)));
typedef unsigned v2u __attribute__((ext_vector_type(2)));
typedef float f32x4 __attribute__((ext_vector_type(4)));
typedef __bf16 bf16x2 __attribute__((ext_vector_type(2)));
typedef __amdgpu_buffer_rsrc_t rsrc_t;
#define LAS __attribute__((address_space(3)))
#define DI __device__ __forceinline__

#define WIN 32
#define AUXKV 2
#define DMA_PRE 12
#define RH 4
#define RM 4
#define SPIN_CAP (1 << 16)

constexpr int D = 640, SK = 16384, F = 1728;
constexpr int OFF_H = 256;
constexpr int OFF_Q = OFF_H + RH * 2560;
constexpr int OFF_M = OFF_Q + 32 * 512;
constexpr int PSTR = 72 * 16;
constexpr int OFF_P = OFF_M + RM * 10240;
constexpr int OFF_Y = OFF_P + 256 * PSTR;
constexpr int OFF_Z = OFF_Y + 8 * 5120;
constexpr int WS_BYTES = OFF_Z + 54 * 5120;
constexpr int OFF_STAMP = 4 << 20;
constexpr int NLMAX = 160;
constexpr int L_SP = 0;
constexpr int L_RED = L_SP + 16 * NLMAX * 4;
constexpr int L_X1 = L_RED + 4096;
constexpr int L_CB = L_X1 + 2560;
constexpr int L_SB = L_CB + 2560;
constexpr int L_HW = L_SB + 2560;
constexpr int L_W = L_HW + 4 * 768;
constexpr int L_DUMMY = L_W + 120 * 1024;
constexpr int LDS_BYTES = L_DUMMY + 1024;
static_assert(L_W % 16 == 0 && LDS_BYTES <= 160 * 1024, "lds");

DI float asf(unsigned u) { return __builtin_bit_cast(float, u); }
DI unsigned asu(float f) { return __builtin_bit_cast(unsigned, f); }
DI float lo16(unsigned w) { return asf(w << 16); }
DI float hi16(unsigned w) { return asf(w & 0xffff0000u); }
DI unsigned f2bf(float f) { unsigned u = asu(f); return (u + 0x7fffu + ((u >> 16) & 1u)) >> 16; }
DI float rbf(float f) { unsigned u = asu(f); return asf((u + 0x7fffu + ((u >> 16) & 1u)) & 0xffff0000u); }
DI unsigned pack2(float a, float b) { return f2bf(a) | (f2bf(b) << 16); }
DI float dot2(unsigned k, unsigned q, float acc) {
  return __builtin_amdgcn_fdot2_f32_bf16(__builtin_bit_cast(bf16x2, k), __builtin_bit_cast(bf16x2, q), acc, false);
}
template <int CTRL> DI float dpp(float x) { return asf(__builtin_amdgcn_mov_dpp(asu(x), CTRL, 0xf, 0xf, true)); }
constexpr int XOR1 = 0xB1, XOR2 = 0x4E, XOR7 = 0x141, XOR8 = 0x128, ROR4 = 0x124, SHL4 = 0x104;
DI float tr32(float a, float b) { v2u r = __builtin_amdgcn_permlane32_swap(asu(a), asu(b), false, false); unsigned p = r[0], q = r[1]; return asf(p) + asf(q); }
DI float tr16(float a, float b) { v2u r = __builtin_amdgcn_permlane16_swap(asu(a), asu(b), false, false); unsigned p = r[0], q = r[1]; return asf(p) + asf(q); }
DI float tr8(float a, float b, bool hi) { const float t1 = a + dpp<XOR8>(a), t2 = b + dpp<XOR8>(b); return hi ? t2 : t1; }
DI float wsum(float v) {
  v += dpp<XOR1>(v); v += dpp<XOR2>(v); v += dpp<XOR7>(v); v += dpp<XOR8>(v); v = tr16(v, v); return tr32(v, v);
}
DI float mx16(float a) { v2u r = __builtin_amdgcn_permlane16_swap(asu(a), asu(a), false, false); unsigned p = r[0], q = r[1]; return fmaxf(asf(p), asf(q)); }
DI float mx32(float a) { v2u r = __builtin_amdgcn_permlane32_swap(asu(a), asu(a), false, false); unsigned p = r[0], q = r[1]; return fmaxf(asf(p), asf(q)); }
DI float wmax(float v) {
  v = fmaxf(v, dpp<XOR1>(v)); v = fmaxf(v, dpp<XOR2>(v)); v = fmaxf(v, dpp<XOR7>(v)); v = fmaxf(v, dpp<XOR8>(v)); return mx32(mx16(v));
}
DI float silu(float v) { return v * __builtin_amdgcn_rcpf(1.f + __expf(-v)); }
DI unsigned plo(unsigned a, unsigned b) { return __builtin_amdgcn_perm(b, a, 0x05040100u); }
DI unsigned phi(unsigned a, unsigned b) { return __builtin_amdgcn_perm(b, a, 0x07060302u); }
struct P8 { unsigned p[8]; };
DI P8 mkpairs(v4u a, v4u b) {
  P8 r;
#pragma unroll
  for (int i = 0; i < 4; ++i) { const unsigned x = a[i], y = b[i]; r.p[2 * i] = plo(x, y); r.p[2 * i + 1] = phi(x, y); }
  return r;
}
DI rsrc_t mkrsrc(const void* p, unsigned bytes) { return __builtin_amdgcn_make_buffer_rsrc((void*)p, 0, bytes, 0x00020000); }
DI v4u ldg(rsrc_t r, int off) { return __builtin_amdgcn_raw_buffer_load_b128(r, off, 0, 16); }
DI void stg(rsrc_t r, int off, unsigned a, unsigned b, unsigned tag) { v4u g = {a, tag, b, tag}; __builtin_amdgcn_raw_buffer_store_b128(g, r, off, 0, 16); }
DI bool okg(v4u g, unsigned tag) { const unsigned a = g[1], b = g[3]; return a == tag && b == tag; }
DI void fence_c() { asm volatile("" ::: "memory"); }
DI void dma16(rsrc_t r, unsigned voff, unsigned soff, unsigned lds) {
  unsigned keep;
  asm volatile("s_mov_b32 %0, m0\n\ts_mov_b32 m0, %1\n\ts_nop 0\n\tbuffer_load_dwordx4 %2, %3, %4 offen lds\n\ts_mov_b32 m0, %0"
               : "=&s"(keep) : "s"(lds), "v"(voff), "s"(r), "s"(soff) : "memory");
}
DI void bar() { asm volatile("s_waitcnt lgkmcnt(0)\n\ts_barrier" ::: "memory"); }
DI float red8(const float (&a)[8], int lane) {
  const float b0 = tr32(a[0], a[4]), b1 = tr32(a[1], a[5]), b2 = tr32(a[2], a[6]), b3 = tr32(a[3], a[7]);
  const float c0 = tr16(b0, b2), c1 = tr16(b1, b3);
  return tr8(c0, c1, lane & 8);
}
DI int red8col(int lane) { return ((lane >> 5) & 1) * 4 + ((lane >> 4) & 1) * 2 + ((lane >> 3) & 1); }

#define ST(i) do { if ((i) == 0) pt0 = __builtin_amdgcn_s_memrealtime(); if ((i) == PROBE) pt1 = __builtin_amdgcn_s_memrealtime(); } while (0)

__global__ void __launch_bounds__(256, 1)
mega(const u16* __restrict__ x, const u16* __restrict__ K, const u16* __restrict__ V, const u16* __restrict__ wq, const u16* __restrict__ wo,
     const u16* __restrict__ wg, const u16* __restrict__ wu, const u16* __restrict__ wd, const u16* __restrict__ c, const u16* __restrict__ wmod,
     u16* __restrict__ out, unsigned* ws) {
  extern __shared__ __attribute__((aligned(16))) char smem[];
  const int tid = threadIdx.x, lane = tid & 63, w = __builtin_amdgcn_readfirstlane(tid >> 6), b = blockIdx.x;
  const int xc = b & 7, head = b >> 3, slot = (head + 4 * xc) & 31;
  unsigned long long pt0 = 0, pt1 = 0;
  ST(0);
  if ((WBSTART == 1 && (b >> 3) == 31 && w == 3) || (WBSTART == 2 && (b >> 3) >= 28 && w == 3)) { __builtin_amdgcn_fence(__ATOMIC_RELEASE, "agent"); }
  const rsrc_t rw = mkrsrc(ws, WS_BYTES);
  const unsigned ldsb = (unsigned)(size_t)(LAS char*)smem;
  const unsigned ew = __builtin_amdgcn_raw_buffer_load_b32(rw, 0, 0, 16);
  const bool isQ = slot < 8, isM1 = slot >= 8 && slot < 13, isF = slot >= 13 && slot < 20 && 7 * xc + slot - 13 < 54, isW = slot >= 20 && slot < 30;
  unsigned tag = 0;
  unsigned sw[5];

  if (isM1) {
    const int mi = 5 * xc + slot - 8, wh = w & 1, rho = lane >> 1, cq = lane & 1;
    const rsrc_t rm = mkrsrc(wmod, D * 6 * D * 2);
    const unsigned* cw = (const unsigned*)c + 160 * wh + 5 * rho;
    unsigned cv[5], xv[5];
#pragma unroll
    for (int i = 0; i < 5; ++i) cv[i] = cw[i];
#pragma unroll
    for (int i = 0; i < 5; ++i) xv[i] = ((const unsigned*)x)[5 * lane + i];
    const unsigned xo = x[16 * mi + (lane & 15)];
    const int vo = (320 * wh + 10 * rho) * 7680 + ((w >> 1) * D + 16 * mi + 8 * cq) * 2;
    v4u wv[10];
#pragma unroll
    for (int i = 0; i < 10; ++i) wv[i] = __builtin_amdgcn_raw_buffer_load_b128(rm, vo, i * 7680, 0);
    __builtin_amdgcn_sched_barrier(0);
#pragma unroll
    for (int i = 0; i < 5; ++i) sw[i] = pack2(silu(lo16(cv[i])), silu(hi16(cv[i])));
    float s = 0;
#pragma unroll
    for (int i = 0; i < 5; ++i) s += lo16(xv[i]) + hi16(xv[i]);
    const float mean = wsum(s) * (1.f / D);
    float v = 0;
#pragma unroll
    for (int i = 0; i < 5; ++i) { const float d0 = lo16(xv[i]) - mean, d1 = hi16(xv[i]) - mean; v += d0 * d0 + d1 * d1; }
    const float var = wsum(v) * (1.f / D);
    const float rs = __builtin_amdgcn_rcpf(rbf(__builtin_sqrtf(rbf(rbf(var) + 1e-5f))));
    const float nrm = rbf(rbf(lo16(xo) - rbf(mean)) * rs);
    float acc[8] = {0, 0, 0, 0, 0, 0, 0, 0};
#pragma unroll
    for (int i = 0; i < 5; ++i) {
      const P8 p = mkpairs(wv[2 * i], wv[2 * i + 1]);
#pragma unroll
      for (int j = 0; j < 8; ++j) acc[j] = dot2(p.p[j], sw[i], acc[j]);
    }
    float r = red8(acc, lane);
    r += dpp<SHL4>(r); r += dpp<XOR2>(r);
    float* red = (float*)(smem + L_RED);
    if ((lane & 6) == 0) red[w * 16 + cq * 8 + red8col(lane)] = r;
    bar();
    if (w == 0) {
      const int col = lane & 15;
      const float sa = rbf(red[col] + red[16 + col]), ca = rbf(red[32 + col] + red[48 + col]);
      const float hv = rbf(rbf(nrm * rbf(1.f + ca)) + sa);
      const unsigned hb = asu(hv) >> 16;
      const unsigned hw = hb | (asu(dpp<XOR1>(hv)) & 0xffff0000u);
      const unsigned hw2 = asu(dpp<XOR2>(asf(hw)));
      tag = __builtin_amdgcn_readfirstlane(ew) + 1;
      if ((lane & 3) == 0 && (lane >> 4) < RH) stg(rw, OFF_H + (lane >> 4) * 2560 + (4 * mi + ((lane & 15) >> 2)) * 16, hw, hw2, tag);
    }
    ST(1);
  } else if (isQ) {
    const int qi = 8 * xc + slot, rho = lane >> 3, cq = lane & 7;
    const rsrc_t rq = mkrsrc(wq, D * 4096 * 2);
    const int vo = (160 * w + 20 * rho) * 8192 + (64 * qi + 8 * cq) * 2;
    v4u wv[20];
#pragma unroll
    for (int i = 0; i < 20; ++i) wv[i] = __builtin_amdgcn_raw_buffer_load_b128(rq, vo, i * 8192, 0);
    __builtin_amdgcn_sched_barrier(0);
    unsigned pr[80];
#pragma unroll
    for (int i = 0; i < 10; ++i) {
      const P8 p = mkpairs(wv[2 * i], wv[2 * i + 1]);
#pragma unroll
      for (int j = 0; j < 8; ++j) pr[8 * i + j] = p.p[j];
    }
    tag = __builtin_amdgcn_readfirstlane(ew) + 1;
    ST(1);
    v4u g[5];
    const int go = OFF_H + (xc % RH) * 2560 + (40 * w + 5 * rho) * 16;
    for (int spin = 0; spin < SPIN_CAP; ++spin) {
      fence_c();
      bool ok = true;
#pragma unroll
      for (int i = 0; i < 5; ++i) { g[i] = ldg(rw, go + i * 16); }
#pragma unroll
      for (int i = 0; i < 5; ++i) ok &= okg(g[i], tag);
      if (__all(ok)) break;
      __builtin_amdgcn_s_sleep(1);
    }
    ST(2);
    float acc[8] = {0, 0, 0, 0, 0, 0, 0, 0};
#pragma unroll
    for (int i = 0; i < 10; ++i) {
      const unsigned hw = g[i >> 1][(i & 1) * 2];
#pragma unroll
      for (int j = 0; j < 8; ++j) acc[j] = dot2(pr[8 * i + j], hw, acc[j]);
    }
    const float r = red8(acc, lane);
    float* red = (float*)(smem + L_RED);
    red[w * 64 + cq * 8 + red8col(lane)] = r;
    bar();
    if (w == 0 && lane < 16) {
      const f32x4 a0 = *(const f32x4*)(red + 4 * lane), a1 = *(const f32x4*)(red + 64 + 4 * lane), a2 = *(const f32x4*)(red + 128 + 4 * lane), a3 = *(const f32x4*)(red + 192 + 4 * lane);
      const f32x4 s = (a0 + a1) + (a2 + a3);
      stg(rw, OFF_Q + (16 * qi + lane) * 16, pack2(s[0], s[1]), pack2(s[2], s[3]), tag);
    }
    ST(3);
  }
  __builtin_amdgcn_sched_barrier(0);

  const int nkeys = 2048, key0 = (b & 7) * 2048;
  const int NLD = nkeys >> 4;
  const rsrc_t rk = mkrsrc(K + ((size_t)head * SK + key0) * 128, nkeys * 256);
  const rsrc_t rv = mkrsrc(V + ((size_t)head * SK + key0) * 128, nkeys * 256);
  const rsrc_t rz = mkrsrc(V, 0);
  unsigned cvm[5];
  if (isW) {
#pragma unroll
    for (int i = 0; i < 5; ++i) cvm[i] = ((const unsigned*)c)[80 * w + 5 * (lane >> 2) + i];
  }
  __builtin_amdgcn_sched_barrier(0);
  const int voff = lane * 16 + w * 4096;
  v4u a[WIN];
#pragma unroll
  for (int i = 0; i < WIN; ++i) a[i] = __builtin_amdgcn_raw_buffer_load_b128(rk, voff + (i & 3) * 1024, (i >> 2) * 16384, AUXKV);
  __builtin_amdgcn_sched_barrier(0);

  auto role_dma = [&](int i0, int i1) {
    if (isW) {
      const int C = slot - 20, m2 = 10 * xc + C;
      const rsrc_t ro = mkrsrc(wo, 4096 * D * 2), rm = mkrsrc(wmod, D * 6 * D * 2);
      const unsigned vo = ((512 * xc + 128 * w + 16 * (lane >> 3)) * D + 64 * C + 8 * (lane & 7)) * 2;
      const unsigned vm = (160 * w + 10 * (lane >> 2)) * 7680 + (2 * D + 32 * m2 + 8 * (lane & 3)) * 2;
#pragma unroll
      for (int i = 0; i < 26; ++i) if (i >= i0 && i < i1) {
        if (i < 10) dma16(rm, vm, i * 7680, ldsb + L_W + 65536 + (w * 10 + i) * 1024);
        else dma16(ro, vo, (i - 10) * 1280, ldsb + L_W + (w * 16 + i - 10) * 1024);
      }
    } else if (isF) {
      const int fj = 7 * xc + slot - 13;
      const rsrc_t rg = mkrsrc(w < 2 ? wg : wu, D * F * 2), rd = mkrsrc(wd, F * D * 2);
      const unsigned vg = ((320 * (w & 1) + 20 * (lane >> 2)) * F + 32 * fj + 8 * (lane & 3)) * 2;
      const unsigned vd = (32 * fj * D) * 2 + w * 10240 + lane * 16;
#pragma unroll
      for (int i = 0; i < 30; ++i) if (i >= i0 && i < i1) {
        if (i < 20) dma16(rg, vg, i * (F * 2), ldsb + L_W + (w * 20 + i) * 1024);
        else dma16(rd, vd, (i - 20) * 1024, ldsb + L_W + 81920 + (w * 10 + i - 20) * 1024);
      }
    }
  };
  role_dma(0, DMA_PRE);
  if (isW) {
#pragma unroll
    for (int i = 0; i < 5; ++i) sw[i] = pack2(silu(lo16(cvm[i])), silu(hi16(cvm[i])));
  }
  tag = __builtin_amdgcn_readfirstlane(ew) + 1;
  const int r4 = lane >> 4, cc = lane & 15;
  v4u qp;
  {
    v4u g0, g1;
    for (int spin = 0; spin < SPIN_CAP; ++spin) {
      fence_c();
      g0 = ldg(rw, OFF_Q + head * 512 + cc * 32); g1 = ldg(rw, OFF_Q + head * 512 + cc * 32 + 16);
      if (__all(okg(g0, tag) && okg(g1, tag))) break;
      __builtin_amdgcn_s_sleep(1);
    }
    qp[0] = g0[0]; qp[1] = g0[2]; qp[2] = g1[0]; qp[3] = g1[2];
  }
  ST(4);
  role_dma(DMA_PRE, 30);
  __builtin_amdgcn_sched_barrier(0);

  float* sp = (float*)(smem + L_SP) + (w * 4 + r4) * NLMAX;
  float mx = -INFINITY;
  const int NIT = (NLD + WIN - 1) / WIN;
#pragma unroll 1
  for (int it = 0; it < NIT; ++it) {
    const bool lastk = it == NIT - 1;
    const int so = lastk ? 0 : (it + 1) * (WIN / 4) * 16384;
    const rsrc_t rr = lastk ? rv : rk;
#pragma unroll
    for (int g = 0; g < WIN / 16; ++g) {
      float xs[16];
#pragma unroll
      for (int t = 0; t < 16; ++t) {
        const int j = g * 16 + t;
        float d = 0;
#pragma unroll
        for (int jj = 0; jj < 4; ++jj) d = dot2(a[j][jj], qp[jj], d);
        xs[t] = d;
        __builtin_amdgcn_sched_barrier(0);
        a[j] = __builtin_amdgcn_raw_buffer_load_b128(rr, voff + (j & 3) * 1024, so + (j >> 2) * 16384, AUXKV);
        __builtin_amdgcn_sched_barrier(0);
      }
#define TR_STEP(HALF, CTRL, BIT) _Pragma("unroll") for (int hh = 0; hh < (HALF); ++hh) { \
      const float lo_ = xs[hh], hi_ = xs[hh + (HALF)]; const float t1_ = lo_ + dpp<CTRL>(lo_), t2_ = hi_ + dpp<CTRL>(hi_); xs[hh] = (cc & (BIT)) ? t2_ : t1_; }
      TR_STEP(8, XOR8, 8) TR_STEP(4, XOR7, 4) TR_STEP(2, XOR2, 2) TR_STEP(1, XOR1, 1)
#undef TR_STEP
      const int li = it * WIN + g * 16 + cc;
      const float sc = li < NLD ? rbf(rbf(xs[0]) * 0.08838834764831845f) : -INFINITY;
      mx = fmaxf(mx, sc);
      sp[li] = sc;
    }
  }
  ST(5);
  mx = wmax(mx);
  float l = 0;
  for (int g = 0; g < NIT * (WIN / 16); ++g) { const float p = __expf(sp[g * 16 + cc] - mx); l += p; sp[g * 16 + cc] = p; }
  l = wsum(l);

  if (isW) {
    const int m2 = 10 * xc + slot - 20, cq = lane & 3;
    const v4u* wl = (const v4u*)(smem + L_W + 65536 + w * 10240) + lane;
    float acc[8] = {0, 0, 0, 0, 0, 0, 0, 0};
#pragma unroll
    for (int i = 0; i < 5; ++i) {
      const P8 p = mkpairs(wl[(2 * i) * 64], wl[(2 * i + 1) * 64]);
#pragma unroll
      for (int j = 0; j < 8; ++j) acc[j] = dot2(p.p[j], sw[i], acc[j]);
    }
    float r = red8(acc, lane);
    r += dpp<SHL4>(r);
    float* red = (float*)(smem + L_RED);
    if ((lane & 4) == 0) red[w * 32 + cq * 8 + red8col(lane)] = r;
    bar();
    if (w == 0) {
      const int g = lane & 7, rep = lane >> 3;
      const f32x4 a0 = *(const f32x4*)(red + 4 * g), a1 = *(const f32x4*)(red + 32 + 4 * g), a2 = *(const f32x4*)(red + 64 + 4 * g), a3 = *(const f32x4*)(red + 96 + 4 * g);
      const f32x4 s = (a0 + a1) + (a2 + a3);
      if (rep < RM) stg(rw, OFF_M + rep * 10240 + (8 * m2 + g) * 16, pack2(s[0], s[1]), pack2(s[2], s[3]), tag);
    }
    bar();
  }

  float acc[8] = {0, 0, 0, 0, 0, 0, 0, 0};
#pragma unroll 1
  for (int it = 0; it < NIT; ++it) {
    const bool lastv = it == NIT - 1;
    const rsrc_t rr = lastv ? rz : rv;
    const int so = (it + 1) * (WIN / 4) * 16384;
#pragma unroll
    for (int g = 0; g < WIN / 4; ++g) {
      const f32x4 p4 = *(const f32x4*)&sp[it * WIN + g * 4];
#pragma unroll
      for (int t = 0; t < 4; ++t) {
        const int j = g * 4 + t;
        const float pp = p4[t];
#pragma unroll
        for (int jj = 0; jj < 4; ++jj) { const unsigned wv = a[j][jj]; acc[2 * jj] += pp * lo16(wv); acc[2 * jj + 1] += pp * hi16(wv); }
        asm volatile("" : "+v"(acc[0]), "+v"(acc[1]), "+v"(acc[2]), "+v"(acc[3]), "+v"(acc[4]), "+v"(acc[5]), "+v"(acc[6]), "+v"(acc[7]));
        __builtin_amdgcn_sched_barrier(0);
        a[j] = __builtin_amdgcn_raw_buffer_load_b128(rr, voff + (j & 3) * 1024, so + (j >> 2) * 16384, AUXKV);
        __builtin_amdgcn_sched_barrier(0);
      }
    }
  }
  ST(6);
  {
    float* sm = (float*)(smem + L_RED);
#pragma unroll
    for (int j = 0; j < 8; ++j) { acc[j] = tr16(acc[j], acc[j]); acc[j] = tr32(acc[j], acc[j]); }
    if (r4 == 0) { *(f32x4*)(sm + w * 132 + 4 + 8 * cc) = f32x4{acc[0], acc[1], acc[2], acc[3]}; *(f32x4*)(sm + w * 132 + 8 + 8 * cc) = f32x4{acc[4], acc[5], acc[6], acc[7]}; }
    if (lane == 0) { sm[w * 132] = mx; sm[w * 132 + 1] = l; }
    asm volatile("s_waitcnt vmcnt(0)" ::: "memory");
    bar();
    if (w < 2) {
      const float m0 = sm[0], m1 = sm[132], m2 = sm[264], m3 = sm[396];
      const float M = fmaxf(fmaxf(m0, m1), fmaxf(m2, m3));
      const float e0 = __expf(m0 - M), e1 = __expf(m1 - M), e2 = __expf(m2 - M), e3 = __expf(m3 - M);
      const int po = OFF_P + b * PSTR;
      if (w == 0) {
        const float* s0 = sm + 4 + 2 * lane;
        const float o0 = e0 * s0[0] + e1 * s0[132] + e2 * s0[264] + e3 * s0[396], o1 = e0 * s0[1] + e1 * s0[133] + e2 * s0[265] + e3 * s0[397];
        stg(rw, po + lane * 16, asu(o0), asu(o1), tag);
      } else if (lane == 0) {
        stg(rw, po + 64 * 16, asu(M), asu(e0 * sm[1] + e1 * sm[133] + e2 * sm[265] + e3 * sm[397]), tag);
      }
    }
    bar();
  }
  ST(7);

  if (isW) {
    const int C = slot - 20, hh = 4 * xc + w, cq = lane & 7;
    const v4u* wl = (const v4u*)(smem + L_W + w * 16384) + lane;
    unsigned pr[64];
#pragma unroll
    for (int i = 0; i < 8; ++i) {
      const P8 p = mkpairs(wl[(2 * i) * 64], wl[(2 * i + 1) * 64]);
#pragma unroll
      for (int j = 0; j < 8; ++j) pr[8 * i + j] = p.p[j];
    }
    const int po = OFF_P + hh * 8 * PSTR;
    v4u g[8], ml;
    bool have[9] = {false, false, false, false, false, false, false, false, false};
    for (int spin = 0; spin < SPIN_CAP; ++spin) {
      fence_c();
#pragma unroll
      for (int s = 0; s < 8; ++s) if (!have[s]) g[s] = ldg(rw, po + s * PSTR + lane * 16);
      if (!have[8]) ml = ldg(rw, po + (lane & 7) * PSTR + 64 * 16);
      bool all = true;
#pragma unroll
      for (int s = 0; s < 8; ++s) { have[s] = __all(okg(g[s], tag)); all &= have[s]; }
      have[8] = __all(okg(ml, tag)); all &= have[8];
      if (all) break;
      __builtin_amdgcn_s_sleep(1);
    }
    ST(8);
    const unsigned mu = ml[0], lu = ml[2];
    const float ms = asf(mu);
    float M = fmaxf(ms, dpp<XOR1>(ms)); M = fmaxf(M, dpp<XOR2>(M)); M = fmaxf(M, dpp<XOR7>(M));
    const float e = __expf(ms - M);
    float L = e * asf(lu); L += dpp<XOR1>(L); L += dpp<XOR2>(L); L += dpp<XOR7>(L);
    const float f = e * __builtin_amdgcn_rcpf(L);
    float o0 = 0, o1 = 0;
#pragma unroll
    for (int s = 0; s < 8; ++s) {
      const float fs = asf(__builtin_amdgcn_readlane(asu(f), s));
      const unsigned u0 = g[s][0], u1 = g[s][2];
      o0 += fs * asf(u0); o1 += fs * asf(u1);
    }
    unsigned* hw = (unsigned*)(smem + L_HW) + w * 192;
    hw[lane] = pack2(o0, o1);
    fence_c();
    const v4u h0 = *(const v4u*)(hw + 8 * (lane >> 3)), h1 = *(const v4u*)(hw + 8 * (lane >> 3) + 4);
    float y[8] = {0, 0, 0, 0, 0, 0, 0, 0};
#pragma unroll
    for (int i = 0; i < 8; ++i) {
      const unsigned hv = i < 4 ? h0[i & 3] : h1[i & 3];
#pragma unroll
      for (int j = 0; j < 8; ++j) y[j] = dot2(pr[8 * i + j], hv, y[j]);
    }
    const float r = red8(y, lane);
    float* red = (float*)(smem + L_RED);
    red[w * 64 + cq * 8 + red8col(lane)] = r;
    bar();
    if (w == 0 && lane < 32) {
      const float* rp = red + 2 * lane;
      const float y0 = (rp[0] + rp[64]) + (rp[128] + rp[192]), y1 = (rp[1] + rp[65]) + (rp[129] + rp[193]);
      stg(rw, OFF_Y + (xc * 320 + 32 * C + lane) * 16, asu(y0), asu(y1), tag);
    }
    ST(9);
  } else if (isF) {
    const int fj = 7 * xc + slot - 13, wh = w & 1, rho = lane >> 2, cq = lane & 3;
    unsigned pr[80], pd[64];
    {
      const v4u* wl = (const v4u*)(smem + L_W + w * 20480) + lane;
#pragma unroll
      for (int i = 0; i < 10; ++i) {
        const P8 p = mkpairs(wl[(2 * i) * 64], wl[(2 * i + 1) * 64]);
#pragma unroll
        for (int j = 0; j < 8; ++j) pr[8 * i + j] = p.p[j];
      }
      const int tc = tid < 160 ? tid : 0;
      const v2u* dl = (const v2u*)(smem + L_W + 81920) + tc;
#pragma unroll
      for (int i = 0; i < 16; ++i) {
        const v2u r0 = dl[(2 * i) * 160], r1 = dl[(2 * i + 1) * 160];
        const unsigned a0 = r0[0], a1 = r0[1], b0 = r1[0], b1 = r1[1];
        pd[4 * i] = plo(a0, b0); pd[4 * i + 1] = phi(a0, b0); pd[4 * i + 2] = plo(a1, b1); pd[4 * i + 3] = phi(a1, b1);
      }
    }
    float* x1s = (float*)(smem + L_X1); float* cbs = (float*)(smem + L_CB); float* sbs = (float*)(smem + L_SB);
    if (tid < 160) {
      const int mo = OFF_M + (xc % RM) * 10240 + tid * 16;
      v4u ga, sb, cb;
      for (int spin = 0; spin < SPIN_CAP; ++spin) {
        fence_c();
        ga = ldg(rw, mo); sb = ldg(rw, mo + 2560); cb = ldg(rw, mo + 5120);
        if (__all(okg(ga, tag) && okg(sb, tag) && okg(cb, tag))) break;
        __builtin_amdgcn_s_sleep(1);
      }
      const v2u xw = *(const v2u*)(x + 4 * tid);
      { const unsigned c0 = cb[0], c1 = cb[2], s0 = sb[0], s1 = sb[2];
        *(f32x4*)(cbs + 4 * tid) = f32x4{rbf(1.f + lo16(c0)), rbf(1.f + hi16(c0)), rbf(1.f + lo16(c1)), rbf(1.f + hi16(c1))};
        *(f32x4*)(sbs + 4 * tid) = f32x4{lo16(s0), hi16(s0), lo16(s1), hi16(s1)}; }
      v4u g[16];
      bool have[8] = {false, false, false, false, false, false, false, false};
      for (int spin = 0; spin < SPIN_CAP; ++spin) {
        fence_c();
#pragma unroll
        for (int G = 0; G < 8; ++G) if (!have[G]) { g[2 * G] = ldg(rw, OFF_Y + (G * 320 + 2 * tid) * 16); g[2 * G + 1] = ldg(rw, OFF_Y + (G * 320 + 2 * tid + 1) * 16); }
        bool all = true;
#pragma unroll
        for (int G = 0; G < 8; ++G) { have[G] = __all(okg(g[2 * G], tag) && okg(g[2 * G + 1], tag)); all &= have[G]; }
        if (all) break;
        __builtin_amdgcn_s_sleep(1);
      }
      float y[4] = {0, 0, 0, 0};
#pragma unroll
      for (int G = 0; G < 8; ++G) { const unsigned u0 = g[2 * G][0], u1 = g[2 * G][2], u2 = g[2 * G + 1][0], u3 = g[2 * G + 1][2]; y[0] += asf(u0); y[1] += asf(u1); y[2] += asf(u2); y[3] += asf(u3); }
      const unsigned g0 = ga[0], g1 = ga[2], x0 = xw[0], x1w = xw[1];
      *(f32x4*)(x1s + 4 * tid) = f32x4{rbf(lo16(x0) + rbf(lo16(g0) * rbf(y[0]))), rbf(hi16(x0) + rbf(hi16(g0) * rbf(y[1]))),
                                       rbf(lo16(x1w) + rbf(lo16(g1) * rbf(y[2]))), rbf(hi16(x1w) + rbf(hi16(g1) * rbf(y[3])))};
    }
    ST(8);
    bar();
    float xa[5], xb[5];
#pragma unroll
    for (int i = 0; i < 5; ++i) { xa[i] = x1s[5 * lane + i]; xb[i] = x1s[320 + 5 * lane + i]; }
    float s = 0;
#pragma unroll
    for (int i = 0; i < 5; ++i) s += xa[i] + xb[i];
    const float mean = wsum(s) * (1.f / D);
    float v = 0;
#pragma unroll
    for (int i = 0; i < 5; ++i) { const float d0 = xa[i] - mean, d1 = xb[i] - mean; v += d0 * d0 + d1 * d1; }
    const float var = wsum(v) * (1.f / D);
    const float rs = __builtin_amdgcn_rcpf(rbf(__builtin_sqrtf(rbf(rbf(var) + 1e-5f)))), mb = rbf(mean);
    u16* hs = (u16*)((unsigned*)(smem + L_HW) + w * 192);
#pragma unroll
    for (int i = 0; i < 5; ++i) {
      const int col = 320 * wh + 5 * lane + i;
      const float xx = wh ? xb[i] : xa[i];
      hs[5 * lane + i] = (u16)f2bf(rbf(rbf(rbf(xx - mb) * rs) * cbs[col]) + sbs[col]);
    }
    fence_c();
    const v2u* hp = (const v2u*)((const unsigned*)hs + 10 * rho);
    float ga8[8] = {0, 0, 0, 0, 0, 0, 0, 0};
#pragma unroll
    for (int i = 0; i < 5; ++i) {
      const v2u hv = hp[i];
      const unsigned h0 = hv[0], h1 = hv[1];
#pragma unroll
      for (int j = 0; j < 8; ++j) ga8[j] = dot2(pr[16 * i + j], h0, ga8[j]);
#pragma unroll
      for (int j = 0; j < 8; ++j) ga8[j] = dot2(pr[16 * i + 8 + j], h1, ga8[j]);
    }
    float r = red8(ga8, lane);
    r += dpp<SHL4>(r);
    float* red = (float*)(smem + L_RED);
    if ((lane & 4) == 0) red[w * 32 + cq * 8 + red8col(lane)] = r;
    bar();
    const int col = lane & 31;
    const float gg = rbf(red[col] + red[32 + col]), uu = rbf(red[64 + col] + red[96 + col]);
    const float av = rbf(rbf(silu(gg)) * uu);
    const unsigned aw = (asu(av) >> 16) | (asu(dpp<XOR1>(av)) & 0xffff0000u);
    float z[4] = {0, 0, 0, 0};
#pragma unroll
    for (int i = 0; i < 16; ++i) {
      const unsigned as = __builtin_amdgcn_readlane(aw, 2 * i);
#pragma unroll
      for (int j = 0; j < 4; ++j) z[j] = dot2(pd[4 * i + j], as, z[j]);
    }
    if (tid < 160) {
      stg(rw, OFF_Z + (fj * 320 + 2 * tid) * 16, asu(z[0]), asu(z[1]), tag);
      stg(rw, OFF_Z + (fj * 320 + 2 * tid + 1) * 16, asu(z[2]), asu(z[3]), tag);
    }
    ST(9);
  } else if (isM1) {
    const int fi = 5 * xc + slot - 8, g8 = lane & 7;
    float x1a = 0, x1b = 0, gb0 = 0, gb1 = 0;
    if (w == 0) {
      const int mo = OFF_M + (xc % RM) * 10240 + (4 * fi + (g8 >> 1)) * 16;
      v4u ga, gb, yy;
      for (int spin = 0; spin < SPIN_CAP; ++spin) {
        fence_c();
        ga = ldg(rw, mo); gb = ldg(rw, mo + 7680); yy = ldg(rw, OFF_Y + ((lane >> 3) * 320 + 8 * fi + g8) * 16);
        if (__all(okg(ga, tag) && okg(gb, tag) && okg(yy, tag))) break;
        __builtin_amdgcn_s_sleep(1);
      }
      const unsigned xw = ((const unsigned*)x)[8 * fi + g8];
      const unsigned gaw = (g8 & 1) ? ga[2] : ga[0], gbw = (g8 & 1) ? gb[2] : gb[0], u0 = yy[0], u1 = yy[2];
      float y0 = 0, y1 = 0;
#pragma unroll
      for (int G = 0; G < 8; ++G) { y0 += __shfl(asf(u0), G * 8 + g8); y1 += __shfl(asf(u1), G * 8 + g8); }
      x1a = rbf(lo16(xw) + rbf(lo16(gaw) * rbf(y0))); x1b = rbf(hi16(xw) + rbf(hi16(gaw) * rbf(y1)));
      gb0 = lo16(gbw); gb1 = hi16(gbw);
    }
    ST(8);
    const int p0 = tid >> 3;
    v4u z0, z1 = {0, 0, 0, 0};
    bool h0 = false, h1 = false;
    for (int spin = 0; spin < SPIN_CAP; ++spin) {
      fence_c();
      if (!h0) z0 = ldg(rw, OFF_Z + (p0 * 320 + 8 * fi + g8) * 16);
      if (!h1 && p0 < 22) z1 = ldg(rw, OFF_Z + ((p0 + 32) * 320 + 8 * fi + g8) * 16);
      h0 = __all(okg(z0, tag)); h1 = __all(p0 >= 22 || okg(z1, tag));
      if (h0 && h1) break;
      __builtin_amdgcn_s_sleep(1);
    }
    ST(9);
    const unsigned u0 = z0[0], u1 = z0[2], u2 = z1[0], u3 = z1[2];
    float s0 = asf(u0) + asf(u2), s1 = asf(u1) + asf(u3);
    s0 += dpp<XOR8>(s0); s1 += dpp<XOR8>(s1);
    s0 = tr16(s0, s0); s1 = tr16(s1, s1); s0 = tr32(s0, s0); s1 = tr32(s1, s1);
    float* red = (float*)(smem + L_RED);
    if (lane < 8) { red[w * 16 + 2 * lane] = s0; red[w * 16 + 2 * lane + 1] = s1; }
    bar();
    if (w == 0 && lane < 8) {
      const float* rp = red + 2 * lane;
      const float y0 = (rp[0] + rp[16]) + (rp[32] + rp[48]), y1 = (rp[1] + rp[17]) + (rp[33] + rp[49]);
      ((unsigned*)out)[8 * fi + lane] = pack2(x1a + rbf(gb0 * rbf(y0)), x1b + rbf(gb1 * rbf(y1)));
      if (fi == 0 && lane == 0) __builtin_amdgcn_raw_buffer_store_b32(tag, rw, 0, 0, 16);
    }
    ST(10);
    if (fi == 0 && w == 0) {
      const unsigned long long te = __builtin_amdgcn_s_memrealtime(),
        d = PROBE == 0 ? 0 : pt1 - pt0;
      for (int i = 0; i < (1 << 20); ++i) { if (__builtin_amdgcn_s_memrealtime() - te >= d) break; __builtin_amdgcn_s_sleep(1); }
    }
  }
}

extern "C" void kernel_launch(void* const* d_in, const int* in_sizes, int n_in, void* d_out, int out_size, void* d_ws, size_t ws_size,
                              hipStream_t stream) {
  hipFuncSetAttribute((const void*)mega, hipFuncAttributeMaxDynamicSharedMemorySize, LDS_BYTES);
  mega<<<256, 256, LDS_BYTES, stream>>>((const u16*)d_in[0], (const u16*)d_in[1], (const u16*)d_in[2], (const u16*)d_in[3], (const u16*)d_in[4],
                                        (const u16*)d_in[6], (const u16*)d_in[7], (const u16*)d_in[8], (const u16*)d_in[9], (const u16*)d_in[10],
                                        (u16*)d_out, (unsigned*)d_ws);
}
```

```cpp
#define PROBE 0
#include <hip/hip_runtime.h>
#include <stdint.h>

typedef unsigned short u16;
typedef unsigned v4u __attribute__((ext_vector_type(4)));
typedef unsigned v2u __attribute__((ext_vector_type(2)));
typedef float f32x4 __attribute__((ext_vector_type(4)));
typedef __bf16 bf16x2 __attribute__((ext_vector_type(2)));
typedef __amdgpu_buffer_rsrc_t rsrc_t;
#define LAS __attribute__((address_space(3)))
#define DI __device__ __forceinline__

#define WIN 32
#define AUXKV 2
#define DMA_PRE 12
#define RH 4
#define RM 4
#define SPIN_CAP (1 << 16)

constexpr int D = 640, SK = 16384, F = 1728;
constexpr int OFF_H = 256;
constexpr int OFF_Q = OFF_H + RH * 2560;
constexpr int OFF_M = OFF_Q + 32 * 512;
constexpr int PSTR = 72 * 16;
constexpr int OFF_P = OFF_M + RM * 10240;
constexpr int OFF_Y = OFF_P + 256 * PSTR;
constexpr int OFF_Z = OFF_Y + 8 * 5120;
constexpr int WS_BYTES = OFF_Z + 54 * 5120;
constexpr int OFF_STAMP = 4 << 20;
constexpr int NLMAX = 160;
constexpr int L_SP = 0;
constexpr int L_RED = L_SP + 16 * NLMAX * 4;
constexpr int L_X1 = L_RED + 4096;
constexpr int L_CB = L_X1 + 2560;
constexpr int L_SB = L_CB + 2560;
constexpr int L_HW = L_SB + 2560;
constexpr int L_W = L_HW + 4 * 768;
constexpr int L_DUMMY = L_W + 120 * 1024;
constexpr int LDS_BYTES = L_DUMMY + 1024;
static_assert(L_W % 16 == 0 && LDS_BYTES <= 160 * 1024, "lds");

DI float asf(unsigned u) { return __builtin_bit_cast(float, u); }
DI unsigned asu(float f) { return __builtin_bit_cast(unsigned, f); }
DI float lo16(unsigned w) { return asf(w << 16); }
DI float hi16(unsigned w) { return asf(w & 0xffff0000u); }
DI unsigned f2bf(float f) { unsigned u = asu(f); return (u + 0x7fffu + ((u >> 16) & 1u)) >> 16; }
DI float rbf(float f) { unsigned u = asu(f); return asf((u + 0x7fffu + ((u >> 16) & 1u)) & 0xffff0000u); }
DI unsigned pack2(float a, float b) { return f2bf(a) | (f2bf(b) << 16); }
DI float dot2(unsigned k, unsigned q, float acc) {
  return __builtin_amdgcn_fdot2_f32_bf16(__builtin_bit_cast(bf16x2, k), __builtin_bit_cast(bf16x2, q), acc, false);
}
template <int CTRL> DI float dpp(float x) { return asf(__builtin_amdgcn_mov_dpp(asu(x), CTRL, 0xf, 0xf, true)); }
constexpr int XOR1 = 0xB1, XOR2 = 0x4E, XOR7 = 0x141, XOR8 = 0x128, ROR4 = 0x124, SHL4 = 0x104;
DI float tr32(float a, float b) { v2u r = __builtin_amdgcn_permlane32_swap(asu(a), asu(b), false, false); unsigned p = r[0], q = r[1]; return asf(p) + asf(q); }
DI float tr16(float a, float b) { v2u r = __builtin_amdgcn_permlane16_swap(asu(a), asu(b), false, false); unsigned p = r[0], q = r[1]; return asf(p) + asf(q); }
DI float tr8(float a, float b, bool hi) { const float t1 = a + dpp<XOR8>(a), t2 = b + dpp<XOR8>(b); return hi ? t2 : t1; }
DI float wsum(float v) {
  v += dpp<XOR1>(v); v += dpp<XOR2>(v); v += dpp<XOR7>(v); v += dpp<XOR8>(v); v = tr16(v, v); return tr32(v, v);
}
DI float mx16(float a) { v2u r = __builtin_amdgcn_permlane16_swap(asu(a), asu(a), false, false); unsigned p = r[0], q = r[1]; return fmaxf(asf(p), asf(q)); }
DI float mx32(float a) { v2u r = __builtin_amdgcn_permlane32_swap(asu(a), asu(a), false, false); unsigned p = r[0], q = r[1]; return fmaxf(asf(p), asf(q)); }
DI float wmax(float v) {
  v = fmaxf(v, dpp<XOR1>(v)); v = fmaxf(v, dpp<XOR2>(v)); v = fmaxf(v, dpp<XOR7>(v)); v = fmaxf(v, dpp<XOR8>(v)); return mx32(mx16(v));
}
DI float silu(float v) { return v * __builtin_amdgcn_rcpf(1.f + __expf(-v)); }
DI unsigned plo(unsigned a, unsigned b) { return __builtin_amdgcn_perm(b, a, 0x05040100u); }
DI unsigned phi(unsigned a, unsigned b) { return __builtin_amdgcn_perm(b, a, 0x07060302u); }
struct P8 { unsigned p[8]; };
DI P8 mkpairs(v4u a, v4u b) {
  P8 r;
#pragma unroll
  for (int i = 0; i < 4; ++i) { const unsigned x = a[i], y = b[i]; r.p[2 * i] = plo(x, y); r.p[2 * i + 1] = phi(x, y); }
  return r;
}
DI rsrc_t mkrsrc(const void* p, unsigned bytes) { return __builtin_amdgcn_make_buffer_rsrc((void*)p, 0, bytes, 0x00020000); }
DI v4u ldg(rsrc_t r, int off) { return __builtin_amdgcn_raw_buffer_load_b128(r, off, 0, 16); }
DI void stg(rsrc_t r, int off, unsigned a, unsigned b, unsigned tag) { v4u g = {a, tag, b, tag}; __builtin_amdgcn_raw_buffer_store_b128(g, r, off, 0, 16); }
DI bool okg(v4u g, unsigned tag) { const unsigned a = g[1], b = g[3]; return a == tag && b == tag; }
DI void fence_c() { asm volatile("" ::: "memory"); }
DI void dma16(rsrc_t r, unsigned voff, unsigned soff, unsigned lds) {
  unsigned keep;
  asm volatile("s_mov_b32 %0, m0\n\ts_mov_b32 m0, %1\n\ts_nop 0\n\tbuffer_load_dwordx4 %2, %3, %4 offen lds\n\ts_mov_b32 m0, %0"
               : "=&s"(keep) : "s"(lds), "v"(voff), "s"(r), "s"(soff) : "memory");
}
DI void bar() { asm volatile("s_waitcnt lgkmcnt(0)\n\ts_barrier" ::: "memory"); }
DI float red8(const float (&a)[8], int lane) {
  const float b0 = tr32(a[0], a[4]), b1 = tr32(a[1], a[5]), b2 = tr32(a[2], a[6]), b3 = tr32(a[3], a[7]);
  const float c0 = tr16(b0, b2), c1 = tr16(b1, b3);
  return tr8(c0, c1, lane & 8);
}
DI int red8col(int lane) { return ((lane >> 5) & 1) * 4 + ((lane >> 4) & 1) * 2 + ((lane >> 3) & 1); }

#define ST(i) do { if ((i) == 0) pt0 = __builtin_amdgcn_s_memrealtime(); if ((i) == PROBE) pt1 = __builtin_amdgcn_s_memrealtime(); } while (0)

__global__ void __launch_bounds__(256, 1)
mega(const u16* __restrict__ x, const u16* __restrict__ K, const u16* __restrict__ V, const u16* __restrict__ wq, const u16* __restrict__ wo,
     const u16* __restrict__ wg, const u16* __restrict__ wu, const u16* __restrict__ wd, const u16* __restrict__ c, const u16* __restrict__ wmod,
     u16* __restrict__ out, unsigned* ws) {
  extern __shared__ __attribute__((aligned(16))) char smem[];
  const int tid = threadIdx.x, lane = tid & 63, w = __builtin_amdgcn_readfirstlane(tid >> 6), b = blockIdx.x;
  const int xc = b & 7, head = b >> 3, slot = (head + 4 * xc) & 31;
  unsigned long long pt0 = 0, pt1 = 0;
  ST(0);
  const rsrc_t rw = mkrsrc(ws, WS_BYTES);
  const unsigned ldsb = (unsigned)(size_t)(LAS char*)smem;
  const unsigned ew = __builtin_amdgcn_raw_buffer_load_b32(rw, 0, 0, 16);
  const bool isQ = slot < 8, isM1 = slot >= 8 && slot < 13, isF = slot >= 13 && slot < 20 && 7 * xc + slot - 13 < 54, isW = slot >= 20 && slot < 30;
  unsigned tag = 0;
  unsigned sw[5];

  if (isM1) {
    const int mi = 5 * xc + slot - 8, wh = w & 1, rho = lane >> 1, cq = lane & 1;
    const rsrc_t rm = mkrsrc(wmod, D * 6 * D * 2);
    const unsigned* cw = (const unsigned*)c + 160 * wh + 5 * rho;
    unsigned cv[5], xv[5];
#pragma unroll
    for (int i = 0; i < 5; ++i) cv[i] = cw[i];
#pragma unroll
    for (int i = 0; i < 5; ++i) xv[i] = ((const unsigned*)x)[5 * lane + i];
    const unsigned xo = x[16 * mi + (lane & 15)];
    const int vo = (320 * wh + 10 * rho) * 7680 + ((w >> 1) * D + 16 * mi + 8 * cq) * 2;
    v4u wv[10];
#pragma unroll
    for (int i = 0; i < 10; ++i) wv[i] = __builtin_amdgcn_raw_buffer_load_b128(rm, vo, i * 7680, 0);
    __builtin_amdgcn_sched_barrier(0);
#pragma unroll
    for (int i = 0; i < 5; ++i) sw[i] = pack2(silu(lo16(cv[i])), silu(hi16(cv[i])));
    float s = 0;
#pragma unroll
    for (int i = 0; i < 5; ++i) s += lo16(xv[i]) + hi16(xv[i]);
    const float mean = wsum(s) * (1.f / D);
    float v = 0;
#pragma unroll
    for (int i = 0; i < 5; ++i) { const float d0 = lo16(xv[i]) - mean, d1 = hi16(xv[i]) - mean; v += d0 * d0 + d1 * d1; }
    const float var = wsum(v) * (1.f / D);
    const float rs = __builtin_amdgcn_rcpf(rbf(__builtin_sqrtf(rbf(rbf(var) + 1e-5f))));
    const float nrm = rbf(rbf(lo16(xo) - rbf(mean)) * rs);
    float acc[8] = {0, 0, 0, 0, 0, 0, 0, 0};
#pragma unroll
    for (int i = 0; i < 5; ++i) {
      const P8 p = mkpairs(wv[2 * i], wv[2 * i + 1]);
#pragma unroll
      for (int j = 0; j < 8; ++j) acc[j] = dot2(p.p[j], sw[i], acc[j]);
    }
    float r = red8(acc, lane);
    r += dpp<SHL4>(r); r += dpp<XOR2>(r);
    float* red = (float*)(smem + L_RED);
    if ((lane & 6) == 0) red[w * 16 + cq * 8 + red8col(lane)] = r;
    bar();
    if (w == 0) {
      const int col = lane & 15;
      const float sa = rbf(red[col] + red[16 + col]), ca = rbf(red[32 + col] + red[48 + col]);
      const float hv = rbf(rbf(nrm * rbf(1.f + ca)) + sa);
      const unsigned hb = asu(hv) >> 16;
      const unsigned hw = hb | (asu(dpp<XOR1>(hv)) & 0xffff0000u);
      const unsigned hw2 = asu(dpp<XOR2>(asf(hw)));
      tag = __builtin_amdgcn_readfirstlane(ew) + 1;
      if ((lane & 3) == 0 && (lane >> 4) < RH) stg(rw, OFF_H + (lane >> 4) * 2560 + (4 * mi + ((lane & 15) >> 2)) * 16, hw, hw2, tag);
    }
    ST(1);
  } else if (isQ) {
    const int qi = 8 * xc + slot, rho = lane >> 3, cq = lane & 7;
    const rsrc_t rq = mkrsrc(wq, D * 4096 * 2);
    const int vo = (160 * w + 20 * rho) * 8192 + (64 * qi + 8 * cq) * 2;
    v4u wv[20];
#pragma unroll
    for (int i = 0; i < 20; ++i) wv[i] = __builtin_amdgcn_raw_buffer_load_b128(rq, vo, i * 8192, 0);
    __builtin_amdgcn_sched_barrier(0);
    unsigned pr[80];
#pragma unroll
    for (int i = 0; i < 10; ++i) {
      const P8 p = mkpairs(wv[2 * i], wv[2 * i + 1]);
#pragma unroll
      for (int j = 0; j < 8; ++j) pr[8 * i + j] = p.p[j];
    }
    tag = __builtin_amdgcn_readfirstlane(ew) + 1;
    ST(1);
    v4u g[5];
    const int go = OFF_H + (xc % RH) * 2560 + (40 * w + 5 * rho) * 16;
    for (int spin = 0; spin < SPIN_CAP; ++spin) {
      fence_c();
      bool ok = true;
#pragma unroll
      for (int i = 0; i < 5; ++i) { g[i] = ldg(rw, go + i * 16); }
#pragma unroll
      for (int i = 0; i < 5; ++i) ok &= okg(g[i], tag);
      if (__all(ok)) break;
      __builtin_amdgcn_s_sleep(1);
    }
    ST(2);
    float acc[8] = {0, 0, 0, 0, 0, 0, 0, 0};
#pragma unroll
    for (int i = 0; i < 10; ++i) {
      const unsigned hw = g[i >> 1][(i & 1) * 2];
#pragma unroll
      for (int j = 0; j < 8; ++j) acc[j] = dot2(pr[8 * i + j], hw, acc[j]);
    }
    const float r = red8(acc, lane);
    float* red = (float*)(smem + L_RED);
    red[w * 64 + cq * 8 + red8col(lane)] = r;
    bar();
    if (w == 0 && lane < 16) {
      const f32x4 a0 = *(const f32x4*)(red + 4 * lane), a1 = *(const f32x4*)(red + 64 + 4 * lane), a2 = *(const f32x4*)(red + 128 + 4 * lane), a3 = *(const f32x4*)(red + 192 + 4 * lane);
      const f32x4 s = (a0 + a1) + (a2 + a3);
      stg(rw, OFF_Q + (16 * qi + lane) * 16, pack2(s[0], s[1]), pack2(s[2], s[3]), tag);
    }
    ST(3);
  }
  __builtin_amdgcn_sched_barrier(0);

  const int nkeys = 2048, key0 = (b & 7) * 2048;
  const int NLD = nkeys >> 4;
  const rsrc_t rk = mkrsrc(K + ((size_t)head * SK + key0) * 128, nkeys * 256);
  const rsrc_t rv = mkrsrc(V + ((size_t)head * SK + key0) * 128, nkeys * 256);
  const rsrc_t rz = mkrsrc(V, 0);
  unsigned cvm[5];
  if (isW) {
#pragma unroll
    for (int i = 0; i < 5; ++i) cvm[i] = ((const unsigned*)c)[80 * w + 5 * (lane >> 2) + i];
  }
  __builtin_amdgcn_sched_barrier(0);
  const int voff = lane * 16 + w * 4096;
  v4u a[WIN];
#pragma unroll
  for (int i = 0; i < WIN; ++i) a[i] = __builtin_amdgcn_raw_buffer_load_b128(rk, voff + (i & 3) * 1024, (i >> 2) * 16384, AUXKV);
  __builtin_amdgcn_sched_barrier(0);

  auto role_dma = [&](int i0, int i1) {
    if (isW) {
      const int C = slot - 20, m2 = 10 * xc + C;
      const rsrc_t ro = mkrsrc(wo, 4096 * D * 2), rm = mkrsrc(wmod, D * 6 * D * 2);
      const unsigned vo = ((512 * xc + 128 * w + 16 * (lane >> 3)) * D + 64 * C + 8 * (lane & 7)) * 2;
      const unsigned vm = (160 * w + 10 * (lane >> 2)) * 7680 + (2 * D + 32 * m2 + 8 * (lane & 3)) * 2;
#pragma unroll
      for (int i = 0; i < 26; ++i) if (i >= i0 && i < i1) {
        if (i < 10) dma16(rm, vm, i * 7680, ldsb + L_W + 65536 + (w * 10 + i) * 1024);
        else dma16(ro, vo, (i - 10) * 1280, ldsb + L_W + (w * 16 + i - 10) * 1024);
      }
    } else if (isF) {
      const int fj = 7 * xc + slot - 13;
      const rsrc_t rg = mkrsrc(w < 2 ? wg : wu, D * F * 2), rd = mkrsrc(wd, F * D * 2);
      const unsigned vg = ((320 * (w & 1) + 20 * (lane >> 2)) * F + 32 * fj + 8 * (lane & 3)) * 2;
      const unsigned vd = (32 * fj * D) * 2 + w * 10240 + lane * 16;
#pragma unroll
      for (int i = 0; i < 30; ++i) if (i >= i0 && i < i1) {
        if (i < 20) dma16(rg, vg, i * (F * 2), ldsb + L_W + (w * 20 + i) * 1024);
        else dma16(rd, vd, (i - 20) * 1024, ldsb + L_W + 81920 + (w * 10 + i - 20) * 1024);
      }
    }
  };
  role_dma(0, DMA_PRE);
  if (isW) {
#pragma unroll
    for (int i = 0; i < 5; ++i) sw[i] = pack2(silu(lo16(cvm[i])), silu(hi16(cvm[i])));
  }
  tag = __builtin_amdgcn_readfirstlane(ew) + 1;
  const int r4 = lane >> 4, cc = lane & 15;
  v4u qp;
  {
    v4u g0, g1;
    for (int spin = 0; spin < SPIN_CAP; ++spin) {
      fence_c();
      g0 = ldg(rw, OFF_Q + head * 512 + cc * 32); g1 = ldg(rw, OFF_Q + head * 512 + cc * 32 + 16);
      if (__all(okg(g0, tag) && okg(g1, tag))) break;
      __builtin_amdgcn_s_sleep(1);
    }
    qp[0] = g0[0]; qp[1] = g0[2]; qp[2] = g1[0]; qp[3] = g1[2];
  }
  ST(4);
  role_dma(DMA_PRE, 30);
  __builtin_amdgcn_sched_barrier(0);

  float* sp = (float*)(smem + L_SP) + (w * 4 + r4) * NLMAX;
  float mx = -INFINITY;
  const int NIT = (NLD + WIN - 1) / WIN;
#pragma unroll 1
  for (int it = 0; it < NIT; ++it) {
    const bool lastk = it == NIT - 1;
    const int so = lastk ? 0 : (it + 1) * (WIN / 4) * 16384;
    const rsrc_t rr = lastk ? rv : rk;
#pragma unroll
    for (int g = 0; g < WIN / 16; ++g) {
      float xs[16];
#pragma unroll
      for (int t = 0; t < 16; ++t) {
        const int j = g * 16 + t;
        float d = 0;
#pragma unroll
        for (int jj = 0; jj < 4; ++jj) d = dot2(a[j][jj], qp[jj], d);
        xs[t] = d;
        __builtin_amdgcn_sched_barrier(0);
        a[j] = __builtin_amdgcn_raw_buffer_load_b128(rr, voff + (j & 3) * 1024, so + (j >> 2) * 16384, AUXKV);
        __builtin_amdgcn_sched_barrier(0);
      }
#define TR_STEP(HALF, CTRL, BIT) _Pragma("unroll") for (int hh = 0; hh < (HALF); ++hh) { \
      const float lo_ = xs[hh], hi_ = xs[hh + (HALF)]; const float t1_ = lo_ + dpp<CTRL>(lo_), t2_ = hi_ + dpp<CTRL>(hi_); xs[hh] = (cc & (BIT)) ? t2_ : t1_; }
      TR_STEP(8, XOR8, 8) TR_STEP(4, XOR7, 4) TR_STEP(2, XOR2, 2) TR_STEP(1, XOR1, 1)
#undef TR_STEP
      const int li = it * WIN + g * 16 + cc;
      const float sc = li < NLD ? rbf(rbf(xs[0]) * 0.08838834764831845f) : -INFINITY;
      mx = fmaxf(mx, sc);
      sp[li] = sc;
    }
  }
  ST(5);
  mx = wmax(mx);
  float l = 0;
  for (int g = 0; g < NIT * (WIN / 16); ++g) { const float p = __expf(sp[g * 16 + cc] - mx); l += p; sp[g * 16 + cc] = p; }
  l = wsum(l);

  if (isW) {
    const int m2 = 10 * xc + slot - 20, cq = lane & 3;
    const v4u* wl = (const v4u*)(smem + L_W + 65536 + w * 10240) + lane;
    float acc[8] = {0, 0, 0, 0, 0, 0, 0, 0};
#pragma unroll
    for (int i = 0; i < 5; ++i) {
      const P8 p = mkpairs(wl[(2 * i) * 64], wl[(2 * i + 1) * 64]);
#pragma unroll
      for (int j = 0; j < 8; ++j) acc[j] = dot2(p.p[j], sw[i], acc[j]);
    }
    float r = red8(acc, lane);
    r += dpp<SHL4>(r);
    float* red = (float*)(smem + L_RED);
    if ((lane & 4) == 0) red[w * 32 + cq * 8 + red8col(lane)] = r;
    bar();
    if (w == 0) {
      const int g = lane & 7, rep = lane >> 3;
      const f32x4 a0 = *(const f32x4*)(red + 4 * g), a1 = *(const f32x4*)(red + 32 + 4 * g), a2 = *(const f32x4*)(red + 64 + 4 * g), a3 = *(const f32x4*)(red + 96 + 4 * g);
      const f32x4 s = (a0 + a1) + (a2 + a3);
      if (rep < RM) stg(rw, OFF_M + rep * 10240 + (8 * m2 + g) * 16, pack2(s[0], s[1]), pack2(s[2], s[3]), tag);
    }
    bar();
  }

  float acc[8] = {0, 0, 0, 0, 0, 0, 0, 0};
#pragma unroll 1
  for (int it = 0; it < NIT; ++it) {
    const bool lastv = it == NIT - 1;
    const rsrc_t rr = lastv ? rz : rv;
    const int so = (it + 1) * (WIN / 4) * 16384;
#pragma unroll
    for (int g = 0; g < WIN / 4; ++g) {
      const f32x4 p4 = *(const f32x4*)&sp[it * WIN + g * 4];
#pragma unroll
      for (int t = 0; t < 4; ++t) {
        const int j = g * 4 + t;
        const float pp = p4[t];
#pragma unroll
        for (int jj = 0; jj < 4; ++jj) { const unsigned wv = a[j][jj]; acc[2 * jj] += pp * lo16(wv); acc[2 * jj + 1] += pp * hi16(wv); }
        asm volatile("" : "+v"(acc[0]), "+v"(acc[1]), "+v"(acc[2]), "+v"(acc[3]), "+v"(acc[4]), "+v"(acc[5]), "+v"(acc[6]), "+v"(acc[7]));
        __builtin_amdgcn_sched_barrier(0);
        a[j] = __builtin_amdgcn_raw_buffer_load_b128(rr, voff + (j & 3) * 1024, so + (j >> 2) * 16384, AUXKV);
        __builtin_amdgcn_sched_barrier(0);
      }
    }
  }
  ST(6);
  {
    float* sm = (float*)(smem + L_RED);
#pragma unroll
    for (int j = 0; j < 8; ++j) { acc[j] = tr16(acc[j], acc[j]); acc[j] = tr32(acc[j], acc[j]); }
    if (r4 == 0) { *(f32x4*)(sm + w * 132 + 4 + 8 * cc) = f32x4{acc[0], acc[1], acc[2], acc[3]}; *(f32x4*)(sm + w * 132 + 8 + 8 * cc) = f32x4{acc[4], acc[5], acc[6], acc[7]}; }
    if (lane == 0) { sm[w * 132] = mx; sm[w * 132 + 1] = l; }
    asm volatile("s_waitcnt vmcnt(0)" ::: "memory");
    bar();
    if (w < 2) {
      const float m0 = sm[0], m1 = sm[132], m2 = sm[264], m3 = sm[396];
      const float M = fmaxf(fmaxf(m0, m1), fmaxf(m2, m3));
      const float e0 = __expf(m0 - M), e1 = __expf(m1 - M), e2 = __expf(m2 - M), e3 = __expf(m3 - M);
      const int po = OFF_P + b * PSTR;
      if (w == 0) {
        const float* s0 = sm + 4 + 2 * lane;
        const float o0 = e0 * s0[0] + e1 * s0[132] + e2 * s0[264] + e3 * s0[396], o1 = e0 * s0[1] + e1 * s0[133] + e2 * s0[265] + e3 * s0[397];
        stg(rw, po + lane * 16, asu(o0), asu(o1), tag);
      } else if (lane == 0) {
        stg(rw, po + 64 * 16, asu(M), asu(e0 * sm[1] + e1 * sm[133] + e2 * sm[265] + e3 * sm[397]), tag);
      }
    }
    bar();
  }
  ST(7);

  if (isW) {
    const int C = slot - 20, hh = 4 * xc + w, cq = lane & 7;
    const v4u* wl = (const v4u*)(smem + L_W + w * 16384) + lane;
    unsigned pr[64];
#pragma unroll
    for (int i = 0; i < 8; ++i) {
      const P8 p = mkpairs(wl[(2 * i) * 64], wl[(2 * i + 1) * 64]);
#pragma unroll
      for (int j = 0; j < 8; ++j) pr[8 * i + j] = p.p[j];
    }
    const int po = OFF_P + hh * 8 * PSTR;
    v4u g[8], ml;
    bool have[9] = {false, false, false, false, false, false, false, false, false};
    for (int spin = 0; spin < SPIN_CAP; ++spin) {
      fence_c();
#pragma unroll
      for (int s = 0; s < 8; ++s) if (!have[s]) g[s] = ldg(rw, po + s * PSTR + lane * 16);
      if (!have[8]) ml = ldg(rw, po + (lane & 7) * PSTR + 64 * 16);
      bool all = true;
#pragma unroll
      for (int s = 0; s < 8; ++s) { have[s] = __all(okg(g[s], tag)); all &= have[s]; }
      have[8] = __all(okg(ml, tag)); all &= have[8];
      if (all) break;
      __builtin_amdgcn_s_sleep(1);
    }
    ST(8);
    const unsigned mu = ml[0], lu = ml[2];
    const float ms = asf(mu);
    float M = fmaxf(ms, dpp<XOR1>(ms)); M = fmaxf(M, dpp<XOR2>(M)); M = fmaxf(M, dpp<XOR7>(M));
    const float e = __expf(ms - M);
    float L = e * asf(lu); L += dpp<XOR1>(L); L += dpp<XOR2>(L); L += dpp<XOR7>(L);
    const float f = e * __builtin_amdgcn_rcpf(L);
    float o0 = 0, o1 = 0;
#pragma unroll
    for (int s = 0; s < 8; ++s) {
      const float fs = asf(__builtin_amdgcn_readlane(asu(f), s));
      const unsigned u0 = g[s][0], u1 = g[s][2];
      o0 += fs * asf(u0); o1 += fs * asf(u1);
    }
    unsigned* hw = (unsigned*)(smem + L_HW) + w * 192;
    hw[lane] = pack2(o0, o1);
    fence_c();
    const v4u h0 = *(const v4u*)(hw + 8 * (lane >> 3)), h1 = *(const v4u*)(hw + 8 * (lane >> 3) + 4);
    float y[8] = {0, 0, 0, 0, 0, 0, 0, 0};
#pragma unroll
    for (int i = 0; i < 8; ++i) {
      const unsigned hv = i < 4 ? h0[i & 3] : h1[i & 3];
#pragma unroll
      for (int j = 0; j < 8; ++j) y[j] = dot2(pr[8 * i + j], hv, y[j]);
    }
    const float r = red8(y, lane);
    float* red = (float*)(smem + L_RED);
    red[w * 64 + cq * 8 + red8col(lane)] = r;
    bar();
    if (w == 0 && lane < 32) {
      const float* rp = red + 2 * lane;
      const float y0 = (rp[0] + rp[64]) + (rp[128] + rp[192]), y1 = (rp[1] + rp[65]) + (rp[129] + rp[193]);
      stg(rw, OFF_Y + (xc * 320 + 32 * C + lane) * 16, asu(y0), asu(y1), tag);
    }
    ST(9);
  } else if (isF) {
    const int fj = 7 * xc + slot - 13, wh = w & 1, rho = lane >> 2, cq = lane & 3;
    unsigned pr[80], pd[64];
    {
      const v4u* wl = (const v4u*)(smem + L_W + w * 20480) + lane;
#pragma unroll
      for (int i = 0; i < 10; ++i) {
        const P8 p = mkpairs(wl[(2 * i) * 64], wl[(2 * i + 1) * 64]);
#pragma unroll
        for (int j = 0; j < 8; ++j) pr[8 * i + j] = p.p[j];
      }
      const int tc = tid < 160 ? tid : 0;
      const v2u* dl = (const v2u*)(smem + L_W + 81920) + tc;
#pragma unroll
      for (int i = 0; i < 16; ++i) {
        const v2u r0 = dl[(2 * i) * 160], r1 = dl[(2 * i + 1) * 160];
        const unsigned a0 = r0[0], a1 = r0[1], b0 = r1[0], b1 = r1[1];
        pd[4 * i] = plo(a0, b0); pd[4 * i + 1] = phi(a0, b0); pd[4 * i + 2] = plo(a1, b1); pd[4 * i + 3] = phi(a1, b1);
      }
    }
    float* x1s = (float*)(smem + L_X1); float* cbs = (float*)(smem + L_CB); float* sbs = (float*)(smem + L_SB);
    if (tid < 160) {
      const int mo = OFF_M + (xc % RM) * 10240 + tid * 16;
      v4u ga, sb, cb;
      for (int spin = 0; spin < SPIN_CAP; ++spin) {
        fence_c();
        ga = ldg(rw, mo); sb = ldg(rw, mo + 2560); cb = ldg(rw, mo + 5120);
        if (__all(okg(ga, tag) && okg(sb, tag) && okg(cb, tag))) break;
        __builtin_amdgcn_s_sleep(1);
      }
      const v2u xw = *(const v2u*)(x + 4 * tid);
      { const unsigned c0 = cb[0], c1 = cb[2], s0 = sb[0], s1 = sb[2];
        *(f32x4*)(cbs + 4 * tid) = f32x4{rbf(1.f + lo16(c0)), rbf(1.f + hi16(c0)), rbf(1.f + lo16(c1)), rbf(1.f + hi16(c1))};
        *(f32x4*)(sbs + 4 * tid) = f32x4{lo16(s0), hi16(s0), lo16(s1), hi16(s1)}; }
      v4u g[16];
      bool have[8] = {false, false, false, false, false, false, false, false};
      for (int spin = 0; spin < SPIN_CAP; ++spin) {
        fence_c();
#pragma unroll
        for (int G = 0; G < 8; ++G) if (!have[G]) { g[2 * G] = ldg(rw, OFF_Y + (G * 320 + 2 * tid) * 16); g[2 * G + 1] = ldg(rw, OFF_Y + (G * 320 + 2 * tid + 1) * 16); }
        bool all = true;
#pragma unroll
        for (int G = 0; G < 8; ++G) { have[G] = __all(okg(g[2 * G], tag) && okg(g[2 * G + 1], tag)); all &= have[G]; }
        if (all) break;
        __builtin_amdgcn_s_sleep(1);
      }
      float y[4] = {0, 0, 0, 0};
#pragma unroll
      for (int G = 0; G < 8; ++G) { const unsigned u0 = g[2 * G][0], u1 = g[2 * G][2], u2 = g[2 * G + 1][0], u3 = g[2 * G + 1][2]; y[0] += asf(u0); y[1] += asf(u1); y[2] += asf(u2); y[3] += asf(u3); }
      const unsigned g0 = ga[0], g1 = ga[2], x0 = xw[0], x1w = xw[1];
      *(f32x4*)(x1s + 4 * tid) = f32x4{rbf(lo16(x0) + rbf(lo16(g0) * rbf(y[0]))), rbf(hi16(x0) + rbf(hi16(g0) * rbf(y[1]))),
                                       rbf(lo16(x1w) + rbf(lo16(g1) * rbf(y[2]))), rbf(hi16(x1w) + rbf(hi16(g1) * rbf(y[3])))};
    }
    ST(8);
    bar();
    float xa[5], xb[5];
#pragma unroll
    for (int i = 0; i < 5; ++i) { xa[i] = x1s[5 * lane + i]; xb[i] = x1s[320 + 5 * lane + i]; }
    float s = 0;
#pragma unroll
    for (int i = 0; i < 5; ++i) s += xa[i] + xb[i];
    const float mean = wsum(s) * (1.f / D);
    float v = 0;
#pragma unroll
    for (int i = 0; i < 5; ++i) { const float d0 = xa[i] - mean, d1 = xb[i] - mean; v += d0 * d0 + d1 * d1; }
    const float var = wsum(v) * (1.f / D);
    const float rs = __builtin_amdgcn_rcpf(rbf(__builtin_sqrtf(rbf(rbf(var) + 1e-5f)))), mb = rbf(mean);
    u16* hs = (u16*)((unsigned*)(smem + L_HW) + w * 192);
#pragma unroll
    for (int i = 0; i < 5; ++i) {
      const int col = 320 * wh + 5 * lane + i;
      const float xx = wh ? xb[i] : xa[i];
      hs[5 * lane + i] = (u16)f2bf(rbf(rbf(rbf(xx - mb) * rs) * cbs[col]) + sbs[col]);
    }
    fence_c();
    const v2u* hp = (const v2u*)((const unsigned*)hs + 10 * rho);
    float ga8[8] = {0, 0, 0, 0, 0, 0, 0, 0};
#pragma unroll
    for (int i = 0; i < 5; ++i) {
      const v2u hv = hp[i];
      const unsigned h0 = hv[0], h1 = hv[1];
#pragma unroll
      for (int j = 0; j < 8; ++j) ga8[j] = dot2(pr[16 * i + j], h0, ga8[j]);
#pragma unroll
      for (int j = 0; j < 8; ++j) ga8[j] = dot2(pr[16 * i + 8 + j], h1, ga8[j]);
    }
    float r = red8(ga8, lane);
    r += dpp<SHL4>(r);
    float* red = (float*)(smem + L_RED);
    if ((lane & 4) == 0) red[w * 32 + cq * 8 + red8col(lane)] = r;
    bar();
    const int col = lane & 31;
    const float gg = rbf(red[col] + red[32 + col]), uu = rbf(red[64 + col] + red[96 + col]);
    const float av = rbf(rbf(silu(gg)) * uu);
    const unsigned aw = (asu(av) >> 16) | (asu(dpp<XOR1>(av)) & 0xffff0000u);
    float z[4] = {0, 0, 0, 0};
#pragma unroll
    for (int i = 0; i < 16; ++i) {
      const unsigned as = __builtin_amdgcn_readlane(aw, 2 * i);
#pragma unroll
      for (int j = 0; j < 4; ++j) z[j] = dot2(pd[4 * i + j], as, z[j]);
    }
    if (tid < 160) {
      stg(rw, OFF_Z + (fj * 320 + 2 * tid) * 16, asu(z[0]), asu(z[1]), tag);
      stg(rw, OFF_Z + (fj * 320 + 2 * tid + 1) * 16, asu(z[2]), asu(z[3]), tag);
    }
    ST(9);
  } else if (isM1) {
    const int fi = 5 * xc + slot - 8, g8 = lane & 7;
    float x1a = 0, x1b = 0, gb0 = 0, gb1 = 0;
    if (w == 0) {
      const int mo = OFF_M + (xc % RM) * 10240 + (4 * fi + (g8 >> 1)) * 16;
      v4u ga, gb, yy;
      for (int spin = 0; spin < SPIN_CAP; ++spin) {
        fence_c();
        ga = ldg(rw, mo); gb = ldg(rw, mo + 7680); yy = ldg(rw, OFF_Y + ((lane >> 3) * 320 + 8 * fi + g8) * 16);
        if (__all(okg(ga, tag) && okg(gb, tag) && okg(yy, tag))) break;
        __builtin_amdgcn_s_sleep(1);
      }
      const unsigned xw = ((const unsigned*)x)[8 * fi + g8];
      const unsigned gaw = (g8 & 1) ? ga[2] : ga[0], gbw = (g8 & 1) ? gb[2] : gb[0], u0 = yy[0], u1 = yy[2];
      float y0 = 0, y1 = 0;
#pragma unroll
      for (int G = 0; G < 8; ++G) { y0 += __shfl(asf(u0), G * 8 + g8); y1 += __shfl(asf(u1), G * 8 + g8); }
      x1a = rbf(lo16(xw) + rbf(lo16(gaw) * rbf(y0))); x1b = rbf(hi16(xw) + rbf(hi16(gaw) * rbf(y1)));
      gb0 = lo16(gbw); gb1 = hi16(gbw);
    }
    ST(8);
    const int p0 = tid >> 3;
    v4u z0, z1 = {0, 0, 0, 0};
    bool h0 = false, h1 = false;
    for (int spin = 0; spin < SPIN_CAP; ++spin) {
      fence_c();
      if (!h0) z0 = ldg(rw, OFF_Z + (p0 * 320 + 8 * fi + g8) * 16);
      if (!h1 && p0 < 22) z1 = ldg(rw, OFF_Z + ((p0 + 32) * 320 + 8 * fi + g8) * 16);
      h0 = __all(okg(z0, tag)); h1 = __all(p0 >= 22 || okg(z1, tag));
      if (h0 && h1) break;
      __builtin_amdgcn_s_sleep(1);
    }
    ST(9);
    const unsigned u0 = z0[0], u1 = z0[2], u2 = z1[0], u3 = z1[2];
    float s0 = asf(u0) + asf(u2), s1 = asf(u1) + asf(u3);
    s0 += dpp<XOR8>(s0); s1 += dpp<XOR8>(s1);
    s0 = tr16(s0, s0); s1 = tr16(s1, s1); s0 = tr32(s0, s0); s1 = tr32(s1, s1);
    float* red = (float*)(smem + L_RED);
    if (lane < 8) { red[w * 16 + 2 * lane] = s0; red[w * 16 + 2 * lane + 1] = s1; }
    bar();
    if (w == 0 && lane < 8) {
      const float* rp = red + 2 * lane;
      const float y0 = (rp[0] + rp[16]) + (rp[32] + rp[48]), y1 = (rp[1] + rp[17]) + (rp[33] + rp[49]);
      ((unsigned*)out)[8 * fi + lane] = pack2(x1a + rbf(gb0 * rbf(y0)), x1b + rbf(gb1 * rbf(y1)));
      if (fi == 0 && lane == 0) __builtin_amdgcn_raw_buffer_store_b32(tag, rw, 0, 0, 16);
    }
    ST(10);
    if (fi == 0 && w == 0) {
      const unsigned long long te = __builtin_amdgcn_s_memrealtime(),
        d = PROBE == 0 ? 0 : pt1 - pt0;
      for (int i = 0; i < (1 << 20); ++i) { if (__builtin_amdgcn_s_memrealtime() - te >= d) break; __builtin_amdgcn_s_sleep(1); }
    }
  }
}

__global__ void k0(unsigned long long* p) { if (threadIdx.x == 0) *p = __builtin_amdgcn_s_memrealtime(); }
__global__ void k2(unsigned long long* p, int mult) {
  const unsigned long long t = __builtin_amdgcn_s_memrealtime(), d = mult * (t - *(volatile unsigned long long*)p);
  for (int i = 0; i < (1 << 20); ++i) { if (__builtin_amdgcn_s_memrealtime() - t >= d) break; __builtin_amdgcn_s_sleep(1); }
}
extern "C" void kernel_launch(void* const* d_in, const int* in_sizes, int n_in, void* d_out, int out_size, void* d_ws, size_t ws_size,
                              hipStream_t stream) {
  hipFuncSetAttribute((const void*)mega, hipFuncAttributeMaxDynamicSharedMemorySize, LDS_BYTES);
  mega<<<256, 256, LDS_BYTES, stream>>>((const u16*)d_in[0], (const u16*)d_in[1], (const u16*)d_in[2], (const u16*)d_in[3], (const u16*)d_in[4],
                                        (const u16*)d_in[6], (const u16*)d_in[7], (const u16*)d_in[8], (const u16*)d_in[9], (const u16*)d_in[10],
                                        (u16*)d_out, (unsigned*)d_ws);
}
```

```cpp
#define PROBE 0
#define USEK2 4
#define K2PROBE
#define K2G 256
#define K2T 256
#define K2L 150000
#include <hip/hip_runtime.h>
#include <stdint.h>

typedef unsigned short u16;
typedef unsigned v4u __attribute__((ext_vector_type(4)));
typedef unsigned v2u __attribute__((ext_vector_type(2)));
typedef float f32x4 __attribute__((ext_vector_type(4)));
typedef __bf16 bf16x2 __attribute__((ext_vector_type(2)));
typedef __amdgpu_buffer_rsrc_t rsrc_t;
#define LAS __attribute__((address_space(3)))
#define DI __device__ __forceinline__

#define WIN 32
#define AUXKV 2
#define DMA_PRE 12
#define RH 4
#define RM 4
#define SPIN_CAP (1 << 16)

constexpr int D = 640, SK = 16384, F = 1728;
constexpr int OFF_H = 256;
constexpr int OFF_Q = OFF_H + RH * 2560;
constexpr int OFF_M = OFF_Q + 32 * 512;
constexpr int PSTR = 72 * 16;
constexpr int OFF_P = OFF_M + RM * 10240;
constexpr int OFF_Y = OFF_P + 256 * PSTR;
constexpr int OFF_Z = OFF_Y + 8 * 5120;
constexpr int WS_BYTES = OFF_Z + 54 * 5120;
constexpr int OFF_STAMP = 4 << 20;
constexpr int NLMAX = 160;
constexpr int L_SP = 0;
constexpr int L_RED = L_SP + 16 * NLMAX * 4;
constexpr int L_X1 = L_RED + 4096;
constexpr int L_CB = L_X1 + 2560;
constexpr int L_SB = L_CB + 2560;
constexpr int L_HW = L_SB + 2560;
constexpr int L_W = L_HW + 4 * 768;
constexpr int L_DUMMY = L_W + 120 * 1024;
constexpr int LDS_BYTES = L_DUMMY + 1024;
static_assert(L_W % 16 == 0 && LDS_BYTES <= 160 * 1024, "lds");

DI float asf(unsigned u) { return __builtin_bit_cast(float, u); }
DI unsigned asu(float f) { return __builtin_bit_cast(unsigned, f); }
DI float lo16(unsigned w) { return asf(w << 16); }
DI float hi16(unsigned w) { return asf(w & 0xffff0000u); }
DI unsigned f2bf(float f) { unsigned u = asu(f); return (u + 0x7fffu + ((u >> 16) & 1u)) >> 16; }
DI float rbf(float f) { unsigned u = asu(f); return asf((u + 0x7fffu + ((u >> 16) & 1u)) & 0xffff0000u); }
DI unsigned pack2(float a, float b) { return f2bf(a) | (f2bf(b) << 16); }
DI float dot2(unsigned k, unsigned q, float acc) {
  return __builtin_amdgcn_fdot2_f32_bf16(__builtin_bit_cast(bf16x2, k), __builtin_bit_cast(bf16x2, q), acc, false);
}
template <int CTRL> DI float dpp(float x) { return asf(__builtin_amdgcn_mov_dpp(asu(x), CTRL, 0xf, 0xf, true)); }
constexpr int XOR1 = 0xB1, XOR2 = 0x4E, XOR7 = 0x141, XOR8 = 0x128, ROR4 = 0x124, SHL4 = 0x104;
DI float tr32(float a, float b) { v2u r = __builtin_amdgcn_permlane32_swap(asu(a), asu(b), false, false); unsigned p = r[0], q = r[1]; return asf(p) + asf(q); }
DI float tr16(float a, float b) { v2u r = __builtin_amdgcn_permlane16_swap(asu(a), asu(b), false, false); unsigned p = r[0], q = r[1]; return asf(p) + asf(q); }
DI float tr8(float a, float b, bool hi) { const float t1 = a + dpp<XOR8>(a), t2 = b + dpp<XOR8>(b); return hi ? t2 : t1; }
DI float wsum(float v) {
  v += dpp<XOR1>(v); v += dpp<XOR2>(v); v += dpp<XOR7>(v); v += dpp<XOR8>(v); v = tr16(v, v); return tr32(v, v);
}
DI float mx16(float a) { v2u r = __builtin_amdgcn_permlane16_swap(asu(a), asu(a), false, false); unsigned p = r[0], q = r[1]; return fmaxf(asf(p), asf(q)); }
DI float mx32(float a) { v2u r = __builtin_amdgcn_permlane32_swap(asu(a), asu(a), false, false); unsigned p = r[0], q = r[1]; return fmaxf(asf(p), asf(q)); }
DI float wmax(float v) {
  v = fmaxf(v, dpp<XOR1>(v)); v = fmaxf(v, dpp<XOR2>(v)); v = fmaxf(v, dpp<XOR7>(v)); v = fmaxf(v, dpp<XOR8>(v)); return mx32(mx16(v));
}
DI float silu(float v) { return v * __builtin_amdgcn_rcpf(1.f + __expf(-v)); }
DI unsigned plo(unsigned a, unsigned b) { return __builtin_amdgcn_perm(b, a, 0x05040100u); }
DI unsigned phi(unsigned a, unsigned b) { return __builtin_amdgcn_perm(b, a, 0x07060302u); }
struct P8 { unsigned p[8]; };
DI P8 mkpairs(v4u a, v4u b) {
  P8 r;
#pragma unroll
  for (int i = 0; i < 4; ++i) { const unsigned x = a[i], y = b[i]; r.p[2 * i] = plo(x, y); r.p[2 * i + 1] = phi(x, y); }
  return r;
}
DI rsrc_t mkrsrc(const void* p, unsigned bytes) { return __builtin_amdgcn_make_buffer_rsrc((void*)p, 0, bytes, 0x00020000); }
DI v4u ldg(rsrc_t r, int off) { return __builtin_amdgcn_raw_buffer_load_b128(r, off, 0, 16); }
DI void stg(rsrc_t r, int off, unsigned a, unsigned b, unsigned tag) { v4u g = {a, tag, b, tag}; __builtin_amdgcn_raw_buffer_store_b128(g, r, off, 0, 16); }
DI bool okg(v4u g, unsigned tag) { const unsigned a = g[1], b = g[3]; return a == tag && b == tag; }
DI void fence_c() { asm volatile("" ::: "memory"); }
DI void dma16(rsrc_t r, unsigned voff, unsigned soff, unsigned lds) {
  unsigned keep;
  asm volatile("s_mov_b32 %0, m0\n\ts_mov_b32 m0, %1\n\ts_nop 0\n\tbuffer_load_dwordx4 %2, %3, %4 offen lds\n\ts_mov_b32 m0, %0"
               : "=&s"(keep) : "s"(lds), "v"(voff), "s"(r), "s"(soff) : "memory");
}
DI void bar() { asm volatile("s_waitcnt lgkmcnt(0)\n\ts_barrier" ::: "memory"); }
DI float red8(const float (&a)[8], int lane) {
  const float b0 = tr32(a[0], a[4]), b1 = tr32(a[1], a[5]), b2 = tr32(a[2], a[6]), b3 = tr32(a[3], a[7]);
  const float c0 = tr16(b0, b2), c1 = tr16(b1, b3);
  return tr8(c0, c1, lane & 8);
}
DI int red8col(int lane) { return ((lane >> 5) & 1) * 4 + ((lane >> 4) & 1) * 2 + ((lane >> 3) & 1); }

#define ST(i) do { if ((i) == 0) pt0 = __builtin_amdgcn_s_memrealtime(); if ((i) == PROBE) pt1 = __builtin_amdgcn_s_memrealtime(); } while (0)

__global__ void __launch_bounds__(256, 1)
mega(const u16* __restrict__ x, const u16* __restrict__ K, const u16* __restrict__ V, const u16* __restrict__ wq, const u16* __restrict__ wo,
     const u16* __restrict__ wg, const u16* __restrict__ wu, const u16* __restrict__ wd, const u16* __restrict__ c, const u16* __restrict__ wmod,
     u16* __restrict__ out, unsigned* ws) {
  extern __shared__ __attribute__((aligned(16))) char smem[];
  const int tid = threadIdx.x, lane = tid & 63, w = __builtin_amdgcn_readfirstlane(tid >> 6), b = blockIdx.x;
  const int xc = b & 7, head = b >> 3, slot = (head + 4 * xc) & 31;
  unsigned long long pt0 = 0, pt1 = 0;
  ST(0);
  const rsrc_t rw = mkrsrc(ws, WS_BYTES);
  const unsigned ldsb = (unsigned)(size_t)(LAS char*)smem;
  const unsigned ew = __builtin_amdgcn_raw_buffer_load_b32(rw, 0, 0, 16);
  const bool isQ = slot < 8, isM1 = slot >= 8 && slot < 13, isF = slot >= 13 && slot < 20 && 7 * xc + slot - 13 < 54, isW = slot >= 20 && slot < 30;
  unsigned tag = 0;
  unsigned sw[5];

  if (isM1) {
    const int mi = 5 * xc + slot - 8, wh = w & 1, rho = lane >> 1, cq = lane & 1;
    const rsrc_t rm = mkrsrc(wmod, D * 6 * D * 2);
    const unsigned* cw = (const unsigned*)c + 160 * wh + 5 * rho;
    unsigned cv[5], xv[5];
#pragma unroll
    for (int i = 0; i < 5; ++i) cv[i] = cw[i];
#pragma unroll
    for (int i = 0; i < 5; ++i) xv[i] = ((const unsigned*)x)[5 * lane + i];
    const unsigned xo = x[16 * mi + (lane & 15)];
    const int vo = (320 * wh + 10 * rho) * 7680 + ((w >> 1) * D + 16 * mi + 8 * cq) * 2;
    v4u wv[10];
#pragma unroll
    for (int i = 0; i < 10; ++i) wv[i] = __builtin_amdgcn_raw_buffer_load_b128(rm, vo, i * 7680, 0);
    __builtin_amdgcn_sched_barrier(0);
#pragma unroll
    for (int i = 0; i < 5; ++i) sw[i] = pack2(silu(lo16(cv[i])), silu(hi16(cv[i])));
    float s = 0;
#pragma unroll
    for (int i = 0; i < 5; ++i) s += lo16(xv[i]) + hi16(xv[i]);
    const float mean = wsum(s) * (1.f / D);
    float v = 0;
#pragma unroll
    for (int i = 0; i < 5; ++i) { const float d0 = lo16(xv[i]) - mean, d1 = hi16(xv[i]) - mean; v += d0 * d0 + d1 * d1; }
    const float var = wsum(v) * (1.f / D);
    const float rs = __builtin_amdgcn_rcpf(rbf(__builtin_sqrtf(rbf(rbf(var) + 1e-5f))));
    const float nrm = rbf(rbf(lo16(xo) - rbf(mean)) * rs);
    float acc[8] = {0, 0, 0, 0, 0, 0, 0, 0};
#pragma unroll
    for (int i = 0; i < 5; ++i) {
      const P8 p = mkpairs(wv[2 * i], wv[2 * i + 1]);
#pragma unroll
      for (int j = 0; j < 8; ++j) acc[j] = dot2(p.p[j], sw[i], acc[j]);
    }
    float r = red8(acc, lane);
    r += dpp<SHL4>(r); r += dpp<XOR2>(r);
    float* red = (float*)(smem + L_RED);
    if ((lane & 6) == 0) red[w * 16 + cq * 8 + red8col(lane)] = r;
    bar();
    if (w == 0) {
      const int col = lane & 15;
      const float sa = rbf(red[col] + red[16 + col]), ca = rbf(red[32 + col] + red[48 + col]);
      const float hv = rbf(rbf(nrm * rbf(1.f + ca)) + sa);
      const unsigned hb = asu(hv) >> 16;
      const unsigned hw = hb | (asu(dpp<XOR1>(hv)) & 0xffff0000u);
      const unsigned hw2 = asu(dpp<XOR2>(asf(hw)));
      tag = __builtin_amdgcn_readfirstlane(ew) + 1;
      if ((lane & 3) == 0 && (lane >> 4) < RH) stg(rw, OFF_H + (lane >> 4) * 2560 + (4 * mi + ((lane & 15) >> 2)) * 16, hw, hw2, tag);
    }
    ST(1);
  } else if (isQ) {
    const int qi = 8 * xc + slot, rho = lane >> 3, cq = lane & 7;
    const rsrc_t rq = mkrsrc(wq, D * 4096 * 2);
    const int vo = (160 * w + 20 * rho) * 8192 + (64 * qi + 8 * cq) * 2;
    v4u wv[20];
#pragma unroll
    for (int i = 0; i < 20; ++i) wv[i] = __builtin_amdgcn_raw_buffer_load_b128(rq, vo, i * 8192, 0);
    __builtin_amdgcn_sched_barrier(0);
    unsigned pr[80];
#pragma unroll
    for (int i = 0; i < 10; ++i) {
      const P8 p = mkpairs(wv[2 * i], wv[2 * i + 1]);
#pragma unroll
      for (int j = 0; j < 8; ++j) pr[8 * i + j] = p.p[j];
    }
    tag = __builtin_amdgcn_readfirstlane(ew) + 1;
    ST(1);
    v4u g[5];
    const int go = OFF_H + (xc % RH) * 2560 + (40 * w + 5 * rho) * 16;
    for (int spin = 0; spin < SPIN_CAP; ++spin) {
      fence_c();
      bool ok = true;
#pragma unroll
      for (int i = 0; i < 5; ++i) { g[i] = ldg(rw, go + i * 16); }
#pragma unroll
      for (int i = 0; i < 5; ++i) ok &= okg(g[i], tag);
      if (__all(ok)) break;
      __builtin_amdgcn_s_sleep(1);
    }
    ST(2);
    float acc[8] = {0, 0, 0, 0, 0, 0, 0, 0};
#pragma unroll
    for (int i = 0; i < 10; ++i) {
      const unsigned hw = g[i >> 1][(i & 1) * 2];
#pragma unroll
      for (int j = 0; j < 8; ++j) acc[j] = dot2(pr[8 * i + j], hw, acc[j]);
    }
    const float r = red8(acc, lane);
    float* red = (float*)(smem + L_RED);
    red[w * 64 + cq * 8 + red8col(lane)] = r;
    bar();
    if (w == 0 && lane < 16) {
      const f32x4 a0 = *(const f32x4*)(red + 4 * lane), a1 = *(const f32x4*)(red + 64 + 4 * lane), a2 = *(const f32x4*)(red + 128 + 4 * lane), a3 = *(const f32x4*)(red + 192 + 4 * lane);
      const f32x4 s = (a0 + a1) + (a2 + a3);
      stg(rw, OFF_Q + (16 * qi + lane) * 16, pack2(s[0], s[1]), pack2(s[2], s[3]), tag);
    }
    ST(3);
  }
  __builtin_amdgcn_sched_barrier(0);

  const int nkeys = 2048, key0 = (b & 7) * 2048;
  const int NLD = nkeys >> 4;
  const rsrc_t rk = mkrsrc(K + ((size_t)head * SK + key0) * 128, nkeys * 256);
  const rsrc_t rv = mkrsrc(V + ((size_t)head * SK + key0) * 128, nkeys * 256);
  const rsrc_t rz = mkrsrc(V, 0);
  unsigned cvm[5];
  if (isW) {
#pragma unroll
    for (int i = 0; i < 5; ++i) cvm[i] = ((const unsigned*)c)[80 * w + 5 * (lane >> 2) + i];
  }
  __builtin_amdgcn_sched_barrier(0);
  const int voff = lane * 16 + w * 4096;
  v4u a[WIN];
#pragma unroll
  for (int i = 0; i < WIN; ++i) a[i] = __builtin_amdgcn_raw_buffer_load_b128(rk, voff + (i & 3) * 1024, (i >> 2) * 16384, AUXKV);
  __builtin_amdgcn_sched_barrier(0);

  auto role_dma = [&](int i0, int i1) {
    if (isW) {
      const int C = slot - 20, m2 = 10 * xc + C;
      const rsrc_t ro = mkrsrc(wo, 4096 * D * 2), rm = mkrsrc(wmod, D * 6 * D * 2);
      const unsigned vo = ((512 * xc + 128 * w + 16 * (lane >> 3)) * D + 64 * C + 8 * (lane & 7)) * 2;
      const unsigned vm = (160 * w + 10 * (lane >> 2)) * 7680 + (2 * D + 32 * m2 + 8 * (lane & 3)) * 2;
#pragma unroll
      for (int i = 0; i < 26; ++i) if (i >= i0 && i < i1) {
        if (i < 10) dma16(rm, vm, i * 7680, ldsb + L_W + 65536 + (w * 10 + i) * 1024);
        else dma16(ro, vo, (i - 10) * 1280, ldsb + L_W + (w * 16 + i - 10) * 1024);
      }
    } else if (isF) {
      const int fj = 7 * xc + slot - 13;
      const rsrc_t rg = mkrsrc(w < 2 ? wg : wu, D * F * 2), rd = mkrsrc(wd, F * D * 2);
      const unsigned vg = ((320 * (w & 1) + 20 * (lane >> 2)) * F + 32 * fj + 8 * (lane & 3)) * 2;
      const unsigned vd = (32 * fj * D) * 2 + w * 10240 + lane * 16;
#pragma unroll
      for (int i = 0; i < 30; ++i) if (i >= i0 && i < i1) {
        if (i < 20) dma16(rg, vg, i * (F * 2), ldsb + L_W + (w * 20 + i) * 1024);
        else dma16(rd, vd, (i - 20) * 1024, ldsb + L_W + 81920 + (w * 10 + i - 20) * 1024);
      }
    }
  };
  role_dma(0, DMA_PRE);
  if (isW) {
#pragma unroll
    for (int i = 0; i < 5; ++i) sw[i] = pack2(silu(lo16(cvm[i])), silu(hi16(cvm[i])));
  }
  tag = __builtin_amdgcn_readfirstlane(ew) + 1;
  const int r4 = lane >> 4, cc = lane & 15;
  v4u qp;
  {
    v4u g0, g1;
    for (int spin = 0; spin < SPIN_CAP; ++spin) {
      fence_c();
      g0 = ldg(rw, OFF_Q + head * 512 + cc * 32); g1 = ldg(rw, OFF_Q + head * 512 + cc * 32 + 16);
      if (__all(okg(g0, tag) && okg(g1, tag))) break;
      __builtin_amdgcn_s_sleep(1);
    }
    qp[0] = g0[0]; qp[1] = g0[2]; qp[2] = g1[0]; qp[3] = g1[2];
  }
  ST(4);
  role_dma(DMA_PRE, 30);
  __builtin_amdgcn_sched_barrier(0);

  float* sp = (float*)(smem + L_SP) + (w * 4 + r4) * NLMAX;
  float mx = -INFINITY;
  const int NIT = (NLD + WIN - 1) / WIN;
#pragma unroll 1
  for (int it = 0; it < NIT; ++it) {
    const bool lastk = it == NIT - 1;
    const int so = lastk ? 0 : (it + 1) * (WIN / 4) * 16384;
    const rsrc_t rr = lastk ? rv : rk;
#pragma unroll
    for (int g = 0; g < WIN / 16; ++g) {
      float xs[16];
#pragma unroll
      for (int t = 0; t < 16; ++t) {
        const int j = g * 16 + t;
        float d = 0;
#pragma unroll
        for (int jj = 0; jj < 4; ++jj) d = dot2(a[j][jj], qp[jj], d);
        xs[t] = d;
        __builtin_amdgcn_sched_barrier(0);
        a[j] = __builtin_amdgcn_raw_buffer_load_b128(rr, voff + (j & 3) * 1024, so + (j >> 2) * 16384, AUXKV);
        __builtin_amdgcn_sched_barrier(0);
      }
#define TR_STEP(HALF, CTRL, BIT) _Pragma("unroll") for (int hh = 0; hh < (HALF); ++hh) { \
      const float lo_ = xs[hh], hi_ = xs[hh + (HALF)]; const float t1_ = lo_ + dpp<CTRL>(lo_), t2_ = hi_ + dpp<CTRL>(hi_); xs[hh] = (cc & (BIT)) ? t2_ : t1_; }
      TR_STEP(8, XOR8, 8) TR_STEP(4, XOR7, 4) TR_STEP(2, XOR2, 2) TR_STEP(1, XOR1, 1)
#undef TR_STEP
      const int li = it * WIN + g * 16 + cc;
      const float sc = li < NLD ? rbf(rbf(xs[0]) * 0.08838834764831845f) : -INFINITY;
      mx = fmaxf(mx, sc);
      sp[li] = sc;
    }
  }
  ST(5);
  mx = wmax(mx);
  float l = 0;
  for (int g = 0; g < NIT * (WIN / 16); ++g) { const float p = __expf(sp[g * 16 + cc] - mx); l += p; sp[g * 16 + cc] = p; }
  l = wsum(l);

  if (isW) {
    const int m2 = 10 * xc + slot - 20, cq = lane & 3;
    const v4u* wl = (const v4u*)(smem + L_W + 65536 + w * 10240) + lane;
    float acc[8] = {0, 0, 0, 0, 0, 0, 0, 0};
#pragma unroll
    for (int i = 0; i < 5; ++i) {
      const P8 p = mkpairs(wl[(2 * i) * 64], wl[(2 * i + 1) * 64]);
#pragma unroll
      for (int j = 0; j < 8; ++j) acc[j] = dot2(p.p[j], sw[i], acc[j]);
    }
    float r = red8(acc, lane);
    r += dpp<SHL4>(r);
    float* red = (float*)(smem + L_RED);
    if ((lane & 4) == 0) red[w * 32 + cq * 8 + red8col(lane)] = r;
    bar();
    if (w == 0) {
      const int g = lane & 7, rep = lane >> 3;
      const f32x4 a0 = *(const f32x4*)(red + 4 * g), a1 = *(const f32x4*)(red + 32 + 4 * g), a2 = *(const f32x4*)(red + 64 + 4 * g), a3 = *(const f32x4*)(red + 96 + 4 * g);
      const f32x4 s = (a0 + a1) + (a2 + a3);
      if (rep < RM) stg(rw, OFF_M + rep * 10240 + (8 * m2 + g) * 16, pack2(s[0], s[1]), pack2(s[2], s[3]), tag);
    }
    bar();
  }

  float acc[8] = {0, 0, 0, 0, 0, 0, 0, 0};
#pragma unroll 1
  for (int it = 0; it < NIT; ++it) {
    const bool lastv = it == NIT - 1;
    const rsrc_t rr = lastv ? rz : rv;
    const int so = (it + 1) * (WIN / 4) * 16384;
#pragma unroll
    for (int g = 0; g < WIN / 4; ++g) {
      const f32x4 p4 = *(const f32x4*)&sp[it * WIN + g * 4];
#pragma unroll
      for (int t = 0; t < 4; ++t) {
        const int j = g * 4 + t;
        const float pp = p4[t];
#pragma unroll
        for (int jj = 0; jj < 4; ++jj) { const unsigned wv = a[j][jj]; acc[2 * jj] += pp * lo16(wv); acc[2 * jj + 1] += pp * hi16(wv); }
        asm volatile("" : "+v"(acc[0]), "+v"(acc[1]), "+v"(acc[2]), "+v"(acc[3]), "+v"(acc[4]), "+v"(acc[5]), "+v"(acc[6]), "+v"(acc[7]));
        __builtin_amdgcn_sched_barrier(0);
        a[j] = __builtin_amdgcn_raw_buffer_load_b128(rr, voff + (j & 3) * 1024, so + (j >> 2) * 16384, AUXKV);
        __builtin_amdgcn_sched_barrier(0);
      }
    }
  }
  ST(6);
  {
    float* sm = (float*)(smem + L_RED);
#pragma unroll
    for (int j = 0; j < 8; ++j) { acc[j] = tr16(acc[j], acc[j]); acc[j] = tr32(acc[j], acc[j]); }
    if (r4 == 0) { *(f32x4*)(sm + w * 132 + 4 + 8 * cc) = f32x4{acc[0], acc[1], acc[2], acc[3]}; *(f32x4*)(sm + w * 132 + 8 + 8 * cc) = f32x4{acc[4], acc[5], acc[6], acc[7]}; }
    if (lane == 0) { sm[w * 132] = mx; sm[w * 132 + 1] = l; }
    asm volatile("s_waitcnt vmcnt(0)" ::: "memory");
    bar();
    if (w < 2) {
      const float m0 = sm[0], m1 = sm[132], m2 = sm[264], m3 = sm[396];
      const float M = fmaxf(fmaxf(m0, m1), fmaxf(m2, m3));
      const float e0 = __expf(m0 - M), e1 = __expf(m1 - M), e2 = __expf(m2 - M), e3 = __expf(m3 - M);
      const int po = OFF_P + b * PSTR;
      if (w == 0) {
        const float* s0 = sm + 4 + 2 * lane;
        const float o0 = e0 * s0[0] + e1 * s0[132] + e2 * s0[264] + e3 * s0[396], o1 = e0 * s0[1] + e1 * s0[133] + e2 * s0[265] + e3 * s0[397];
        stg(rw, po + lane * 16, asu(o0), asu(o1), tag);
      } else if (lane == 0) {
        stg(rw, po + 64 * 16, asu(M), asu(e0 * sm[1] + e1 * sm[133] + e2 * sm[265] + e3 * sm[397]), tag);
      }
    }
    bar();
  }
  ST(7);

  if (isW) {
    const int C = slot - 20, hh = 4 * xc + w, cq = lane & 7;
    const v4u* wl = (const v4u*)(smem + L_W + w * 16384) + lane;
    unsigned pr[64];
#pragma unroll
    for (int i = 0; i < 8; ++i) {
      const P8 p = mkpairs(wl[(2 * i) * 64], wl[(2 * i + 1) * 64]);
#pragma unroll
      for (int j = 0; j < 8; ++j) pr[8 * i + j] = p.p[j];
    }
    const int po = OFF_P + hh * 8 * PSTR;
    v4u g[8], ml;
    bool have[9] = {false, false, false, false, false, false, false, false, false};
    for (int spin = 0; spin < SPIN_CAP; ++spin) {
      fence_c();
#pragma unroll
      for (int s = 0; s < 8; ++s) if (!have[s]) g[s] = ldg(rw, po + s * PSTR + lane * 16);
      if (!have[8]) ml = ldg(rw, po + (lane & 7) * PSTR + 64 * 16);
      bool all = true;
#pragma unroll
      for (int s = 0; s < 8; ++s) { have[s] = __all(okg(g[s], tag)); all &= have[s]; }
      have[8] = __all(okg(ml, tag)); all &= have[8];
      if (all) break;
      __builtin_amdgcn_s_sleep(1);
    }
    ST(8);
    const unsigned mu = ml[0], lu = ml[2];
    const float ms = asf(mu);
    float M = fmaxf(ms, dpp<XOR1>(ms)); M = fmaxf(M, dpp<XOR2>(M)); M = fmaxf(M, dpp<XOR7>(M));
    const float e = __expf(ms - M);
    float L = e * asf(lu); L += dpp<XOR1>(L); L += dpp<XOR2>(L); L += dpp<XOR7>(L);
    const float f = e * __builtin_amdgcn_rcpf(L);
    float o0 = 0, o1 = 0;
#pragma unroll
    for (int s = 0; s < 8; ++s) {
      const float fs = asf(__builtin_amdgcn_readlane(asu(f), s));
      const unsigned u0 = g[s][0], u1 = g[s][2];
      o0 += fs * asf(u0); o1 += fs * asf(u1);
    }
    unsigned* hw = (unsigned*)(smem + L_HW) + w * 192;
    hw[lane] = pack2(o0, o1);
    fence_c();
    const v4u h0 = *(const v4u*)(hw + 8 * (lane >> 3)), h1 = *(const v4u*)(hw + 8 * (lane >> 3) + 4);
    float y[8] = {0, 0, 0, 0, 0, 0, 0, 0};
#pragma unroll
    for (int i = 0; i < 8; ++i) {
      const unsigned hv = i < 4 ? h0[i & 3] : h1[i & 3];
#pragma unroll
      for (int j = 0; j < 8; ++j) y[j] = dot2(pr[8 * i + j], hv, y[j]);
    }
    const float r = red8(y, lane);
    float* red = (float*)(smem + L_RED);
    red[w * 64 + cq * 8 + red8col(lane)] = r;
    bar();
    if (w == 0 && lane < 32) {
      const float* rp = red + 2 * lane;
      const float y0 = (rp[0] + rp[64]) + (rp[128] + rp[192]), y1 = (rp[1] + rp[65]) + (rp[129] + rp[193]);
      stg(rw, OFF_Y + (xc * 320 + 32 * C + lane) * 16, asu(y0), asu(y1), tag);
    }
    ST(9);
  } else if (isF) {
    const int fj = 7 * xc + slot - 13, wh = w & 1, rho = lane >> 2, cq = lane & 3;
    unsigned pr[80], pd[64];
    {
      const v4u* wl = (const v4u*)(smem + L_W + w * 20480) + lane;
#pragma unroll
      for (int i = 0; i < 10; ++i) {
        const P8 p = mkpairs(wl[(2 * i) * 64], wl[(2 * i + 1) * 64]);
#pragma unroll
        for (int j = 0; j < 8; ++j) pr[8 * i + j] = p.p[j];
      }
      const int tc = tid < 160 ? tid : 0;
      const v2u* dl = (const v2u*)(smem + L_W + 81920) + tc;
#pragma unroll
      for (int i = 0; i < 16; ++i) {
        const v2u r0 = dl[(2 * i) * 160], r1 = dl[(2 * i + 1) * 160];
        const unsigned a0 = r0[0], a1 = r0[1], b0 = r1[0], b1 = r1[1];
        pd[4 * i] = plo(a0, b0); pd[4 * i + 1] = phi(a0, b0); pd[4 * i + 2] = plo(a1, b1); pd[4 * i + 3] = phi(a1, b1);
      }
    }
    float* x1s = (float*)(smem + L_X1); float* cbs = (float*)(smem + L_CB); float* sbs = (float*)(smem + L_SB);
    if (tid < 160) {
      const int mo = OFF_M + (xc % RM) * 10240 + tid * 16;
      v4u ga, sb, cb;
      for (int spin = 0; spin < SPIN_CAP; ++spin) {
        fence_c();
        ga = ldg(rw, mo); sb = ldg(rw, mo + 2560); cb = ldg(rw, mo + 5120);
        if (__all(okg(ga, tag) && okg(sb, tag) && okg(cb, tag))) break;
        __builtin_amdgcn_s_sleep(1);
      }
      const v2u xw = *(const v2u*)(x + 4 * tid);
      { const unsigned c0 = cb[0], c1 = cb[2], s0 = sb[0], s1 = sb[2];
        *(f32x4*)(cbs + 4 * tid) = f32x4{rbf(1.f + lo16(c0)), rbf(1.f + hi16(c0)), rbf(1.f + lo16(c1)), rbf(1.f + hi16(c1))};
        *(f32x4*)(sbs + 4 * tid) = f32x4{lo16(s0), hi16(s0), lo16(s1), hi16(s1)}; }
      v4u g[16];
      bool have[8] = {false, false, false, false, false, false, false, false};
      for (int spin = 0; spin < SPIN_CAP; ++spin) {
        fence_c();
#pragma unroll
        for (int G = 0; G < 8; ++G) if (!have[G]) { g[2 * G] = ldg(rw, OFF_Y + (G * 320 + 2 * tid) * 16); g[2 * G + 1] = ldg(rw, OFF_Y + (G * 320 + 2 * tid + 1) * 16); }
        bool all = true;
#pragma unroll
        for (int G = 0; G < 8; ++G) { have[G] = __all(okg(g[2 * G], tag) && okg(g[2 * G + 1], tag)); all &= have[G]; }
        if (all) break;
        __builtin_amdgcn_s_sleep(1);
      }
      float y[4] = {0, 0, 0, 0};
#pragma unroll
      for (int G = 0; G < 8; ++G) { const unsigned u0 = g[2 * G][0], u1 = g[2 * G][2], u2 = g[2 * G + 1][0], u3 = g[2 * G + 1][2]; y[0] += asf(u0); y[1] += asf(u1); y[2] += asf(u2); y[3] += asf(u3); }
      const unsigned g0 = ga[0], g1 = ga[2], x0 = xw[0], x1w = xw[1];
      *(f32x4*)(x1s + 4 * tid) = f32x4{rbf(lo16(x0) + rbf(lo16(g0) * rbf(y[0]))), rbf(hi16(x0) + rbf(hi16(g0) * rbf(y[1]))),
                                       rbf(lo16(x1w) + rbf(lo16(g1) * rbf(y[2]))), rbf(hi16(x1w) + rbf(hi16(g1) * rbf(y[3])))};
    }
    ST(8);
    bar();
    float xa[5], xb[5];
#pragma unroll
    for (int i = 0; i < 5; ++i) { xa[i] = x1s[5 * lane + i]; xb[i] = x1s[320 + 5 * lane + i]; }
    float s = 0;
#pragma unroll
    for (int i = 0; i < 5; ++i) s += xa[i] + xb[i];
    const float mean = wsum(s) * (1.f / D);
    float v = 0;
#pragma unroll
    for (int i = 0; i < 5; ++i) { const float d0 = xa[i] - mean, d1 = xb[i] - mean; v += d0 * d0 + d1 * d1; }
    const float var = wsum(v) * (1.f / D);
    const float rs = __builtin_amdgcn_rcpf(rbf(__builtin_sqrtf(rbf(rbf(var) + 1e-5f)))), mb = rbf(mean);
    u16* hs = (u16*)((unsigned*)(smem + L_HW) + w * 192);
#pragma unroll
    for (int i = 0; i < 5; ++i) {
      const int col = 320 * wh + 5 * lane + i;
      const float xx = wh ? xb[i] : xa[i];
      hs[5 * lane + i] = (u16)f2bf(rbf(rbf(rbf(xx - mb) * rs) * cbs[col]) + sbs[col]);
    }
    fence_c();
    const v2u* hp = (const v2u*)((const unsigned*)hs + 10 * rho);
    float ga8[8] = {0, 0, 0, 0, 0, 0, 0, 0};
#pragma unroll
    for (int i = 0; i < 5; ++i) {
      const v2u hv = hp[i];
      const unsigned h0 = hv[0], h1 = hv[1];
#pragma unroll
      for (int j = 0; j < 8; ++j) ga8[j] = dot2(pr[16 * i + j], h0, ga8[j]);
#pragma unroll
      for (int j = 0; j < 8; ++j) ga8[j] = dot2(pr[16 * i + 8 + j], h1, ga8[j]);
    }
    float r = red8(ga8, lane);
    r += dpp<SHL4>(r);
    float* red = (float*)(smem + L_RED);
    if ((lane & 4) == 0) red[w * 32 + cq * 8 + red8col(lane)] = r;
    bar();
    const int col = lane & 31;
    const float gg = rbf(red[col] + red[32 + col]), uu = rbf(red[64 + col] + red[96 + col]);
    const float av = rbf(rbf(silu(gg)) * uu);
    const unsigned aw = (asu(av) >> 16) | (asu(dpp<XOR1>(av)) & 0xffff0000u);
    float z[4] = {0, 0, 0, 0};
#pragma unroll
    for (int i = 0; i < 16; ++i) {
      const unsigned as = __builtin_amdgcn_readlane(aw, 2 * i);
#pragma unroll
      for (int j = 0; j < 4; ++j) z[j] = dot2(pd[4 * i + j], as, z[j]);
    }
    if (tid < 160) {
      stg(rw, OFF_Z + (fj * 320 + 2 * tid) * 16, asu(z[0]), asu(z[1]), tag);
      stg(rw, OFF_Z + (fj * 320 + 2 * tid + 1) * 16, asu(z[2]), asu(z[3]), tag);
    }
    ST(9);
  } else if (isM1) {
    const int fi = 5 * xc + slot - 8, g8 = lane & 7;
    float x1a = 0, x1b = 0, gb0 = 0, gb1 = 0;
    if (w == 0) {
      const int mo = OFF_M + (xc % RM) * 10240 + (4 * fi + (g8 >> 1)) * 16;
      v4u ga, gb, yy;
      for (int spin = 0; spin < SPIN_CAP; ++spin) {
        fence_c();
        ga = ldg(rw, mo); gb = ldg(rw, mo + 7680); yy = ldg(rw, OFF_Y + ((lane >> 3) * 320 + 8 * fi + g8) * 16);
        if (__all(okg(ga, tag) && okg(gb, tag) && okg(yy, tag))) break;
        __builtin_amdgcn_s_sleep(1);
      }
      const unsigned xw = ((const unsigned*)x)[8 * fi + g8];
      const unsigned gaw = (g8 & 1) ? ga[2] : ga[0], gbw = (g8 & 1) ? gb[2] : gb[0], u0 = yy[0], u1 = yy[2];
      float y0 = 0, y1 = 0;
#pragma unroll
      for (int G = 0; G < 8; ++G) { y0 += __shfl(asf(u0), G * 8 + g8); y1 += __shfl(asf(u1), G * 8 + g8); }
      x1a = rbf(lo16(xw) + rbf(lo16(gaw) * rbf(y0))); x1b = rbf(hi16(xw) + rbf(hi16(gaw) * rbf(y1)));
      gb0 = lo16(gbw); gb1 = hi16(gbw);
    }
    ST(8);
    const int p0 = tid >> 3;
    v4u z0, z1 = {0, 0, 0, 0};
    bool h0 = false, h1 = false;
    for (int spin = 0; spin < SPIN_CAP; ++spin) {
      fence_c();
      if (!h0) z0 = ldg(rw, OFF_Z + (p0 * 320 + 8 * fi + g8) * 16);
      if (!h1 && p0 < 22) z1 = ldg(rw, OFF_Z + ((p0 + 32) * 320 + 8 * fi + g8) * 16);
      h0 = __all(okg(z0, tag)); h1 = __all(p0 >= 22 || okg(z1, tag));
      if (h0 && h1) break;
      __builtin_amdgcn_s_sleep(1);
    }
    ST(9);
    const unsigned u0 = z0[0], u1 = z0[2], u2 = z1[0], u3 = z1[2];
    float s0 = asf(u0) + asf(u2), s1 = asf(u1) + asf(u3);
    s0 += dpp<XOR8>(s0); s1 += dpp<XOR8>(s1);
    s0 = tr16(s0, s0); s1 = tr16(s1, s1); s0 = tr32(s0, s0); s1 = tr32(s1, s1);
    float* red = (float*)(smem + L_RED);
    if (lane < 8) { red[w * 16 + 2 * lane] = s0; red[w * 16 + 2 * lane + 1] = s1; }
    bar();
    if (w == 0 && lane < 8) {
      const float* rp = red + 2 * lane;
      const float y0 = (rp[0] + rp[16]) + (rp[32] + rp[48]), y1 = (rp[1] + rp[17]) + (rp[33] + rp[49]);
      ((unsigned*)out)[8 * fi + lane] = pack2(x1a + rbf(gb0 * rbf(y0)), x1b + rbf(gb1 * rbf(y1)));
      if (fi == 0 && lane == 0) __builtin_amdgcn_raw_buffer_store_b32(tag, rw, 0, 0, 16);
    }
    ST(10);
    if (fi == 0 && w == 0) {
      const unsigned long long te = __builtin_amdgcn_s_memrealtime(),
        d = PROBE == 0 ? 0 : pt1 - pt0;
      if (lane == 0) *(volatile unsigned long long*)((char*)ws + (8 << 20)) = te;
      for (int i = 0; i < (1 << 20); ++i) { if (__builtin_amdgcn_s_memrealtime() - te >= d) break; __builtin_amdgcn_s_sleep(1); }
    }
  }
}

__global__ void k0(unsigned long long* p) { if (threadIdx.x == 0) *p = __builtin_amdgcn_s_memrealtime(); }
#define K2B 0
__global__ void __launch_bounds__(K2T, 1) k2(unsigned long long* p, int mult) {
  const unsigned long long t = __builtin_amdgcn_s_memrealtime();
  if (blockIdx.x == K2B && threadIdx.x < 64) {
    const unsigned long long d = mult * (t - *(volatile unsigned long long*)p);
    for (int i = 0; i < (1 << 20); ++i) { if (__builtin_amdgcn_s_memrealtime() - t >= d) break; __builtin_amdgcn_s_sleep(1); }
  }
}
extern "C" void kernel_launch(void* const* d_in, const int* in_sizes, int n_in, void* d_out, int out_size, void* d_ws, size_t ws_size,
                              hipStream_t stream) {
  hipFuncSetAttribute((const void*)mega, hipFuncAttributeMaxDynamicSharedMemorySize, LDS_BYTES);
  mega<<<256, 256, LDS_BYTES, stream>>>((const u16*)d_in[0], (const u16*)d_in[1], (const u16*)d_in[2], (const u16*)d_in[3], (const u16*)d_in[4],
                                        (const u16*)d_in[6], (const u16*)d_in[7], (const u16*)d_in[8], (const u16*)d_in[9], (const u16*)d_in[10],
                                        (u16*)d_out, (unsigned*)d_ws);
  hipFuncSetAttribute((const void*)k2, hipFuncAttributeMaxDynamicSharedMemorySize, 160 * 1024);
  k2<<<K2G, K2T, K2L, stream>>>((unsigned long long*)((char*)d_ws + (8 << 20)), USEK2);
}
```

```cpp
#include <hip/hip_runtime.h>
#include <stdint.h>

typedef unsigned short u16;
typedef unsigned v4u __attribute__((ext_vector_type(4)));
typedef unsigned v2u __attribute__((ext_vector_type(2)));
typedef float f32x4 __attribute__((ext_vector_type(4)));
typedef __bf16 bf16x2 __attribute__((ext_vector_type(2)));
typedef __amdgpu_buffer_rsrc_t rsrc_t;
#define LAS __attribute__((address_space(3)))
#define DI __device__ __forceinline__

#define AUXKV 2
#define BULK_DELAY 16
#define PREQ 0
#define NBPOLL 3
#define AUXQ 2
#define NTFULL 1
#define NTHALF 0
#define CODEPF 32
#define CODEPF2 32
#define ARGS1 1
#define RH 4
#define RM 4
#define DELTAS 5, 6, 5, 5, 6, -4, -5, -5, -5, -5, -5, -5, -6, -2, -3, -3, -4, -3, -4, -4, 1, 3, 1, 1, 2, 3, 1, 1, 1, 2, 10, 10
#define PARITY 0
#define SPIN_CAP (1 << 16)

constexpr int D = 640, SK = 16384, F = 1728;
constexpr int OFF_H = 256;
constexpr int OFF_Q = OFF_H + RH * 5120;
constexpr int OFF_M = OFF_Q + 32 * 512;
constexpr int PSTR = 72 * 16;
constexpr int OFF_P = OFF_M + RM * 10240;
constexpr int OFF_Y = OFF_P + 256 * PSTR;
constexpr int OFF_Z = OFF_Y + 8 * 5120;
constexpr int WS_BYTES = OFF_Z + 54 * 5120;
constexpr int OFF_STAMP = 4 << 20;
constexpr int KDELTA[32] = {DELTAS};
constexpr unsigned long long kcount(int c) { unsigned long long r = 0; for (int k = 0; k < 8; ++k) r |= (unsigned long long)(64 + KDELTA[c + 4 * k]) << (8 * k); return r; }
constexpr unsigned long long kstart(int c) { unsigned long long r = 0; int s = 0; for (int k = 0; k < 8; ++k) { r |= (unsigned long long)(64 + s) << (8 * k); s += KDELTA[c + 4 * k]; } return r; }
constexpr bool kok() { for (int c = 0; c < 4; ++c) { int s = 0; for (int k = 0; k < 8; ++k) { s += KDELTA[c + 4 * k]; if (s < -60 || s > 60) return false; } if (s) return false; } return true; }
constexpr int kmin() { int m = 0; for (int i = 0; i < 32; ++i) m = KDELTA[i] < m ? KDELTA[i] : m; return 128 + m - PARITY; }
constexpr int kmax() { int m = 0; for (int i = 0; i < 32; ++i) m = KDELTA[i] > m ? KDELTA[i] : m; return 128 + m + PARITY; }
static_assert(kok(), "key split");
constexpr int NIT = 4, ZMIN = kmin() / NIT, WIN = (kmax() + NIT - 1) / NIT, SPW = WIN <= 32 ? 32 : 36, NLMAX = NIT * SPW;
static_assert(WIN <= 36 && ZMIN >= 20, "ring");
constexpr int L_SP = 0;
constexpr int L_RED = L_SP + 16 * NLMAX * 4;
constexpr int L_X1 = L_RED + 4096;
constexpr int L_CB = L_X1 + 2560;
constexpr int L_SB = L_CB + 2560;
constexpr int L_HW = L_SB + 2560;
constexpr int L_W = L_HW + 4 * 768;
constexpr int L_DUMMY = L_W + 120 * 1024;
constexpr int LDS_BYTES = L_DUMMY + 1024;
static_assert(L_W % 16 == 0 && LDS_BYTES <= 160 * 1024, "lds");

DI float asf(unsigned u) { return __builtin_bit_cast(float, u); }
DI unsigned asu(float f) { return __builtin_bit_cast(unsigned, f); }
DI float lo16(unsigned w) { return asf(w << 16); }
DI float hi16(unsigned w) { return asf(w & 0xffff0000u); }
typedef float f32x2 __attribute__((ext_vector_type(2)));
DI unsigned pack2(float a, float b) { const f32x2 v = {a, b}; return __builtin_bit_cast(unsigned, __builtin_convertvector(v, bf16x2)); }
DI float rbf(float f) { return asf(pack2(0.f, f)); }
DI float dot2(unsigned k, unsigned q, float acc) {
  return __builtin_amdgcn_fdot2_f32_bf16(__builtin_bit_cast(bf16x2, k), __builtin_bit_cast(bf16x2, q), acc, false);
}
template <int CTRL> DI float dpp(float x) { return asf(__builtin_amdgcn_mov_dpp(asu(x), CTRL, 0xf, 0xf, true)); }
constexpr int XOR1 = 0xB1, XOR2 = 0x4E, XOR7 = 0x141, XOR8 = 0x128, ROR4 = 0x124, SHL4 = 0x104;
DI float tr32(float a, float b) { v2u r = __builtin_amdgcn_permlane32_swap(asu(a), asu(b), false, false); unsigned p = r[0], q = r[1]; return asf(p) + asf(q); }
DI float tr16(float a, float b) { v2u r = __builtin_amdgcn_permlane16_swap(asu(a), asu(b), false, false); unsigned p = r[0], q = r[1]; return asf(p) + asf(q); }
DI float tr8(float a, float b, bool hi) { const float t1 = a + dpp<XOR8>(a), t2 = b + dpp<XOR8>(b); return hi ? t2 : t1; }
DI float wsum(float v) {
  v += dpp<XOR1>(v); v += dpp<XOR2>(v); v += dpp<XOR7>(v); v += dpp<XOR8>(v); v = tr16(v, v); return tr32(v, v);
}
DI float mx16(float a) { v2u r = __builtin_amdgcn_permlane16_swap(asu(a), asu(a), false, false); unsigned p = r[0], q = r[1]; return fmaxf(asf(p), asf(q)); }
DI float mx32(float a) { v2u r = __builtin_amdgcn_permlane32_swap(asu(a), asu(a), false, false); unsigned p = r[0], q = r[1]; return fmaxf(asf(p), asf(q)); }
DI float wmax(float v) {
  v = fmaxf(v, dpp<XOR1>(v)); v = fmaxf(v, dpp<XOR2>(v)); v = fmaxf(v, dpp<XOR7>(v)); v = fmaxf(v, dpp<XOR8>(v)); return mx32(mx16(v));
}
DI float silu(float v) { return v * __builtin_amdgcn_rcpf(1.f + __expf(-v)); }
DI unsigned plo(unsigned a, unsigned b) { return __builtin_amdgcn_perm(b, a, 0x05040100u); }
DI unsigned phi(unsigned a, unsigned b) { return __builtin_amdgcn_perm(b, a, 0x07060302u); }
struct P8 { unsigned p[8]; };
DI P8 mkpairs(v4u a, v4u b) {
  P8 r;
#pragma unroll
  for (int i = 0; i < 4; ++i) { const unsigned x = a[i], y = b[i]; r.p[2 * i] = plo(x, y); r.p[2 * i + 1] = phi(x, y); }
  return r;
}
DI rsrc_t mkrsrc(const void* p, unsigned bytes) { return __builtin_amdgcn_make_buffer_rsrc((void*)p, 0, bytes, 0x00020000); }
DI v4u ldg(rsrc_t r, int off) { return __builtin_amdgcn_raw_buffer_load_b128(r, off, 0, 16); }
DI void stg(rsrc_t r, int off, unsigned a, unsigned b, unsigned tag) { v4u g = {a, tag, b, tag}; __builtin_amdgcn_raw_buffer_store_b128(g, r, off, 0, 16); }
DI bool okg(v4u g, unsigned tag) { const unsigned a = g[1], b = g[3]; return a == tag && b == tag; }
DI void fence_c() { asm volatile("" ::: "memory"); }
DI void dma16(rsrc_t r, unsigned voff, unsigned soff, unsigned lds) {
  unsigned keep;
  asm volatile("s_mov_b32 %0, m0\n\ts_mov_b32 m0, %1\n\ts_nop 0\n\tbuffer_load_dwordx4 %2, %3, %4 offen lds\n\ts_mov_b32 m0, %0"
               : "=&s"(keep) : "s"(lds), "v"(voff), "s"(r), "s"(soff) : "memory");
}
#define DMA_BODY(NT) ".rept %c[n]\n\ts_nop 0\n\tbuffer_load_dwordx4 %[vo], %[r], %[so] offen" NT " lds\n\ts_add_u32 m0, m0, 0x400\n\ts_add_u32 %[so], %[so], %[st]\n\t.endr\n\t"
template <int N, bool NT> DI void dma_batch(rsrc_t r, unsigned voff, unsigned soff, unsigned stride, unsigned lds) {
  unsigned keep;
  if (NT) asm volatile("s_mov_b32 %[k], m0\n\ts_mov_b32 m0, %[l]\n\t" DMA_BODY(" nt") "s_mov_b32 m0, %[k]"
               : [k] "=&s"(keep), [so] "+s"(soff) : [vo] "v"(voff), [r] "s"(r), [st] "s"(stride), [l] "s"(lds), [n] "n"(N) : "memory", "scc");
  else asm volatile("s_mov_b32 %[k], m0\n\ts_mov_b32 m0, %[l]\n\t" DMA_BODY("") "s_mov_b32 m0, %[k]"
               : [k] "=&s"(keep), [so] "+s"(soff) : [vo] "v"(voff), [r] "s"(r), [st] "s"(stride), [l] "s"(lds), [n] "n"(N) : "memory", "scc");
}
#define POLL2 "buffer_load_dwordx4 %[g0], %[go], %[rw], 0 offen sc1\n\tbuffer_load_dwordx4 %[g1], %[go], %[rw], 0 offen offset:16 sc1\n\t"
template <int N, bool NT> DI void poll_dma(v4u& g0, v4u& g1, int goff, rsrc_t rw, rsrc_t r, unsigned voff, unsigned soff, unsigned stride, unsigned lds) {
  unsigned keep;
  if (NT) asm volatile(POLL2 "s_mov_b32 %[k], m0\n\ts_mov_b32 m0, %[l]\n\t" DMA_BODY(" nt") "s_mov_b32 m0, %[k]\n\ts_waitcnt vmcnt(%c[n])"
               : [g0] "=&v"(g0), [g1] "=&v"(g1), [k] "=&s"(keep), [so] "+s"(soff)
               : [go] "v"(goff), [rw] "s"(rw), [vo] "v"(voff), [r] "s"(r), [st] "s"(stride), [l] "s"(lds), [n] "n"(N) : "memory", "scc");
  else asm volatile(POLL2 "s_mov_b32 %[k], m0\n\ts_mov_b32 m0, %[l]\n\t" DMA_BODY("") "s_mov_b32 m0, %[k]\n\ts_waitcnt vmcnt(%c[n])"
               : [g0] "=&v"(g0), [g1] "=&v"(g1), [k] "=&s"(keep), [so] "+s"(soff)
               : [go] "v"(goff), [rw] "s"(rw), [vo] "v"(voff), [r] "s"(r), [st] "s"(stride), [l] "s"(lds), [n] "n"(N) : "memory", "scc");
}
DI void bar() { asm volatile("s_waitcnt lgkmcnt(0)\n\ts_barrier" ::: "memory"); }
DI float red8(const float (&a)[8], int lane) {
  const float b0 = tr32(a[0], a[4]), b1 = tr32(a[1], a[5]), b2 = tr32(a[2], a[6]), b3 = tr32(a[3], a[7]);
  const float c0 = tr16(b0, b2), c1 = tr16(b1, b3);
  return tr8(c0, c1, lane & 8);
}
DI int red8col(int lane) { return ((lane >> 5) & 1) * 4 + ((lane >> 4) & 1) * 2 + ((lane >> 3) & 1); }

#define STAMPMASK 0xffff
#define ST(i) do { } while (0)
#define STD(i, x) do { } while (0)

__global__ void __launch_bounds__(256, 1)
mega(const u16* __restrict__ x, const u16* __restrict__ K, const u16* __restrict__ V, const u16* __restrict__ wq, const u16* __restrict__ wo,
     const u16* __restrict__ wg, const u16* __restrict__ wu, const u16* __restrict__ wd, const u16* __restrict__ c, const u16* __restrict__ wmod,
     u16* __restrict__ out, unsigned* ws) {
  extern __shared__ __attribute__((aligned(16))) char smem[];
  const int tid = threadIdx.x, lane = tid & 63, w = __builtin_amdgcn_readfirstlane(tid >> 6), b = blockIdx.x;
  const int xc = b & 7, slot = b >> 3, head = (slot - 4 * xc) & 31;
  ST(0);
  const unsigned ldsb = __builtin_amdgcn_readfirstlane((unsigned)(size_t)(LAS char*)smem);
  rsrc_t rc;
  {
    unsigned long long pc; unsigned len;
    asm volatile("s_getpc_b64 %0\n.Lmega_pf0:\n\ts_mov_b32 %1, .Lmega_end-.Lmega_pf0" : "=s"(pc), "=s"(len));
    rc = mkrsrc((const void*)pc, len);
  }
  auto code_pf = [&]() {
    unsigned keep;
    asm volatile("s_mov_b32 %0, m0\n\ts_mov_b32 m0, %1\n\ts_nop 0\n\tbuffer_load_dword %2, %3, 0 offen lds\n\ts_mov_b32 m0, %0"
                 : "=&s"(keep) : "s"(ldsb + L_DUMMY), "v"(tid * 128), "s"(rc) : "memory");
  };
  if (b < CODEPF) code_pf();
  if (ARGS1) asm volatile("" :: "s"(x), "s"(K), "s"(V), "s"(wq), "s"(wo), "s"(wg), "s"(wu), "s"(wd), "s"(c), "s"(wmod), "s"(out), "s"(ws));
  const rsrc_t rw = mkrsrc(ws, WS_BYTES);
  const unsigned ew = __builtin_amdgcn_raw_buffer_load_b32(rw, 0, 0, 16);
  const bool isM1 = slot < 5, isQ = slot >= 5 && slot < 13, isF = slot >= 13 && slot < 20 && 7 * xc + slot - 13 < 54, isW = slot >= 20 && slot < 30;
  unsigned tag = 0;
  int nch, ch0;
  {
    const int cl = slot & 3, k = slot >> 2, e = (xc & 1) ? PARITY : -PARITY;
    const unsigned long long tc = cl == 0 ? kcount(0) : cl == 1 ? kcount(1) : cl == 2 ? kcount(2) : kcount(3);
    const unsigned long long ts = cl == 0 ? kstart(0) : cl == 1 ? kstart(1) : cl == 2 ? kstart(2) : kstart(3);
    nch = 64 + (int)((tc >> (8 * k)) & 255) + e;
    ch0 = 128 * k - 64 + (int)((ts >> (8 * k)) & 255) - ((k & 1) ? e : 0);
  }
  const int nkeys = 16 * nch, key0 = 16 * ch0;
  auto cnt = [&](int it) { return (nch + NIT - 1 - it) / NIT; };
  const rsrc_t rk = mkrsrc(K + ((size_t)head * SK + key0) * 128, nkeys * 256);
  const rsrc_t rv = mkrsrc(V + ((size_t)head * SK + key0) * 128, nkeys * 256);
  const int voff = lane * 16 + w * 1024;
  v4u a[WIN];
  unsigned sw[5];

  if (isM1) {
    const int mi = 5 * xc + slot, t = mi >= 20, g32 = mi - 20 * t, rho = lane >> 2, cq = lane & 3;
    const rsrc_t rm = mkrsrc(wmod, D * 6 * D * 2);
    const unsigned* cw = (const unsigned*)c + 80 * w + 5 * rho;
    unsigned cv[5];
#pragma unroll
    for (int i = 0; i < 5; ++i) cv[i] = cw[i];
    const int vo = (160 * w + 10 * rho) * 7680 + (t * D + 32 * g32 + 8 * cq) * 2;
    v4u wv[10];
#pragma unroll
    for (int i = 0; i < 10; ++i) wv[i] = __builtin_amdgcn_raw_buffer_load_b128(rm, vo, i * 7680, 0);
    __builtin_amdgcn_sched_barrier(0);
    ST(11);
#pragma unroll
    for (int i = 0; i < 5; ++i) sw[i] = pack2(silu(lo16(cv[i])), silu(hi16(cv[i])));
    STD(12, sw[4]);
    float acc[8] = {0, 0, 0, 0, 0, 0, 0, 0};
#pragma unroll
    for (int i = 0; i < 5; ++i) {
      const P8 p = mkpairs(wv[2 * i], wv[2 * i + 1]);
#pragma unroll
      for (int j = 0; j < 8; ++j) acc[j] = dot2(p.p[j], sw[i], acc[j]);
    }
    STD(14, acc[7]);
    float r = red8(acc, lane);
    r += dpp<SHL4>(r);
    float* red = (float*)(smem + L_RED);
    if ((lane & 4) == 0) red[w * 32 + cq * 8 + red8col(lane)] = r;
    bar();
    if (w == 0) {
      const int g = lane & 7, rep = lane >> 3;
      const f32x4 a0 = *(const f32x4*)(red + 4 * g), a1 = *(const f32x4*)(red + 32 + 4 * g), a2 = *(const f32x4*)(red + 64 + 4 * g), a3 = *(const f32x4*)(red + 96 + 4 * g);
      f32x4 s = (a0 + a1) + (a2 + a3);
      if (t) { s[0] = 1.f + rbf(s[0]); s[1] = 1.f + rbf(s[1]); s[2] = 1.f + rbf(s[2]); s[3] = 1.f + rbf(s[3]); }
      tag = __builtin_amdgcn_readfirstlane(ew) + 1;
      if (rep < RH) stg(rw, OFF_H + rep * 5120 + (t * 160 + 8 * g32 + g) * 16, pack2(s[0], s[1]), pack2(s[2], s[3]), tag);
    }
    ST(1);
  } else if (isQ) {
    const int qi = 8 * xc + slot - 5, rho = lane >> 3, cq = lane & 7, lc = lane < 40 ? lane : 39;
    const rsrc_t rq = mkrsrc(wq, D * 4096 * 2);
    unsigned xv[5];
#pragma unroll
    for (int i = 0; i < 5; ++i) xv[i] = ((const unsigned*)x)[5 * lane + i];
    const v2u xo = *(const v2u*)(x + 160 * w + 4 * lc);
    const int vo = (160 * w + 20 * rho) * 8192 + (64 * qi + 8 * cq) * 2;
    v4u wv[20];
#pragma unroll
    for (int i = 0; i < 20; ++i) wv[i] = __builtin_amdgcn_raw_buffer_load_b128(rq, vo, i * 8192, AUXQ);
    __builtin_amdgcn_sched_barrier(0);
#pragma unroll
    for (int i = 0; i < PREQ; ++i) a[i] = __builtin_amdgcn_raw_buffer_load_b128(rk, voff, i * 4096, AUXKV);
    __builtin_amdgcn_sched_barrier(0);
    float s = 0;
#pragma unroll
    for (int i = 0; i < 5; ++i) s += lo16(xv[i]) + hi16(xv[i]);
    const float mean = wsum(s) * (1.f / D);
    float v = 0;
#pragma unroll
    for (int i = 0; i < 5; ++i) { const float d0 = lo16(xv[i]) - mean, d1 = hi16(xv[i]) - mean; v += d0 * d0 + d1 * d1; }
    const float var = wsum(v) * (1.f / D);
    const float rs = __builtin_amdgcn_rcpf(rbf(__builtin_sqrtf(rbf(rbf(var) + 1e-5f)))), mb = rbf(mean);
    const unsigned xo0 = xo[0], xo1 = xo[1];
    const float n0 = rbf(rbf(lo16(xo0) - mb) * rs), n1 = rbf(rbf(hi16(xo0) - mb) * rs), n2 = rbf(rbf(lo16(xo1) - mb) * rs), n3 = rbf(rbf(hi16(xo1) - mb) * rs);
    unsigned pr[80];
#pragma unroll
    for (int i = 0; i < 10; ++i) {
      const P8 p = mkpairs(wv[2 * i], wv[2 * i + 1]);
#pragma unroll
      for (int j = 0; j < 8; ++j) pr[8 * i + j] = p.p[j];
    }
    tag = __builtin_amdgcn_readfirstlane(ew) + 1;
    ST(1);
    STD(11, pr[79]);
    v4u gs, gc;
    const int go = OFF_H + (xc % RH) * 5120 + (40 * w + lc) * 16;
#pragma nounroll
    for (int spin = 0; spin < SPIN_CAP; ++spin) {
      fence_c();
      gs = ldg(rw, go); gc = ldg(rw, go + 2560);
      if (__all(okg(gs, tag) && okg(gc, tag))) break;
      __builtin_amdgcn_s_sleep(1);
    }
    ST(2);
    unsigned* hw = (unsigned*)(smem + L_HW) + w * 192;
    {
      const unsigned s0 = gs[0], s1 = gs[2], c0 = gc[0], c1 = gc[2];
      const v2u hh = {pack2(rbf(n0 * lo16(c0)) + lo16(s0), rbf(n1 * hi16(c0)) + hi16(s0)), pack2(rbf(n2 * lo16(c1)) + lo16(s1), rbf(n3 * hi16(c1)) + hi16(s1))};
      *(v2u*)(hw + 2 * lc) = hh;
    }
    fence_c();
    float acc[8] = {0, 0, 0, 0, 0, 0, 0, 0};
#pragma unroll
    for (int i = 0; i < 5; ++i) {
      const v2u hv = *(const v2u*)(hw + 10 * rho + 2 * i);
      const unsigned h0 = hv[0], h1 = hv[1];
#pragma unroll
      for (int j = 0; j < 8; ++j) acc[j] = dot2(pr[16 * i + j], h0, acc[j]);
#pragma unroll
      for (int j = 0; j < 8; ++j) acc[j] = dot2(pr[16 * i + 8 + j], h1, acc[j]);
    }
    const float r = red8(acc, lane);
    float* red = (float*)(smem + L_RED);
    red[w * 64 + cq * 8 + red8col(lane)] = r;
    bar();
    if (w == 0 && lane < 16) {
      const f32x4 a0 = *(const f32x4*)(red + 4 * lane), a1 = *(const f32x4*)(red + 64 + 4 * lane), a2 = *(const f32x4*)(red + 128 + 4 * lane), a3 = *(const f32x4*)(red + 192 + 4 * lane);
      const f32x4 s = (a0 + a1) + (a2 + a3);
      stg(rw, OFF_Q + (16 * qi + lane) * 16, pack2(s[0], s[1]), pack2(s[2], s[3]), tag);
    }
    ST(3);
  }
  __builtin_amdgcn_sched_barrier(0);

  unsigned cvm[5];
  if (isW) {
#pragma unroll
    for (int i = 0; i < 5; ++i) cvm[i] = ((const unsigned*)c)[80 * w + 5 * (lane >> 2) + i];
  }
  __builtin_amdgcn_sched_barrier(0);
  if (BULK_DELAY > 0 && !isM1 && !isQ) __builtin_amdgcn_s_sleep(BULK_DELAY);
  if (PREQ > 0 && !isQ) {
#pragma unroll
    for (int i = 0; i < PREQ; ++i) a[i] = __builtin_amdgcn_raw_buffer_load_b128(rk, voff, i * 4096, AUXKV);
  }
  {
    const int c0 = cnt(0);
#pragma unroll
    for (int i = PREQ; i < WIN; ++i) {
      if (i >= ZMIN) a[i] = v4u{0, 0, 0, 0};
      if (i < ZMIN || i < c0) a[i] = __builtin_amdgcn_raw_buffer_load_b128(rk, voff, i * 4096, AUXKV);
    }
  }
  __builtin_amdgcn_sched_barrier(0);

  if (isW) {
#pragma unroll
    for (int i = 0; i < 5; ++i) sw[i] = pack2(silu(lo16(cvm[i])), silu(hi16(cvm[i])));
  }
  tag = __builtin_amdgcn_readfirstlane(ew) + 1;
  const int r4 = lane >> 4, cc = lane & 15;
  v4u qp;
  {
    v4u g0, g1;
    const int go = OFF_Q + head * 512 + cc * 32;
    bool got = false;
    if (isW || isF) {
      rsrc_t r0, r1, r2; unsigned v0, v1, v2, s1, s2, st0, st1, st2, l0, l1, l2;
      if (isW) {
        const int C = slot - 20, m2 = 10 * xc + C;
        r0 = mkrsrc(wmod, D * 6 * D * 2); r1 = r2 = mkrsrc(wo, 4096 * D * 2);
        v0 = (160 * w + 10 * (lane >> 2)) * 7680 + (2 * D + 32 * m2 + 8 * (lane & 3)) * 2;
        v1 = v2 = ((512 * xc + 128 * w + 16 * (lane >> 3)) * D + 64 * C + 8 * (lane & 7)) * 2;
        st0 = 7680; st1 = st2 = 1280; s1 = 0; s2 = 8 * 1280;
        l0 = ldsb + L_W + 65536 + w * 10240; l1 = ldsb + L_W + w * 16384; l2 = l1 + 8192;
      } else {
        const int fj = 7 * xc + slot - 13;
        r0 = r1 = mkrsrc(w < 2 ? wg : wu, D * F * 2); r2 = mkrsrc(wd, F * D * 2);
        v0 = v1 = ((320 * (w & 1) + 20 * (lane >> 2)) * F + 32 * fj + 8 * (lane & 3)) * 2;
        v2 = (32 * fj * D) * 2 + w * 10240 + lane * 16;
        st0 = st1 = F * 2; st2 = 1024; s1 = 10 * F * 2; s2 = 0;
        l0 = ldsb + L_W + w * 20480; l1 = l0 + 10240; l2 = ldsb + L_W + 81920 + w * 10240;
      }
      l0 = __builtin_amdgcn_readfirstlane(l0); l1 = __builtin_amdgcn_readfirstlane(l1); l2 = __builtin_amdgcn_readfirstlane(l2);
      if (NBPOLL > 0) { poll_dma<10, NTHALF>(g0, g1, go, rw, r0, v0, 0, st0, l0); got = __all(okg(g0, tag) && okg(g1, tag)); }
      else dma_batch<10, NTHALF>(r0, v0, 0, st0, l0);
      if (isW) {
        if (NBPOLL > 1 && !got) { poll_dma<8, NTFULL>(g0, g1, go, rw, r1, v1, s1, st1, l1); got = __all(okg(g0, tag) && okg(g1, tag)); }
        else dma_batch<8, NTFULL>(r1, v1, s1, st1, l1);
        if (NBPOLL > 2 && !got) { poll_dma<8, NTFULL>(g0, g1, go, rw, r2, v2, s2, st2, l2); got = __all(okg(g0, tag) && okg(g1, tag)); }
        else dma_batch<8, NTFULL>(r2, v2, s2, st2, l2);
      } else {
        if (NBPOLL > 1 && !got) { poll_dma<10, NTHALF>(g0, g1, go, rw, r1, v1, s1, st1, l1); got = __all(okg(g0, tag) && okg(g1, tag)); }
        else dma_batch<10, NTHALF>(r1, v1, s1, st1, l1);
        if (NBPOLL > 2 && !got) { poll_dma<10, NTFULL>(g0, g1, go, rw, r2, v2, s2, st2, l2); got = __all(okg(g0, tag) && okg(g1, tag)); }
        else dma_batch<10, NTFULL>(r2, v2, s2, st2, l2);
      }
    }
    if (!got) {
#pragma nounroll
      for (int spin = 0; spin < SPIN_CAP; ++spin) {
        fence_c();
        g0 = ldg(rw, go); g1 = ldg(rw, go + 16);
        if (__all(okg(g0, tag) && okg(g1, tag))) break;
        __builtin_amdgcn_s_sleep(1);
      }
    }
    qp[0] = g0[0]; qp[1] = g0[2]; qp[2] = g1[0]; qp[3] = g1[2];
  }
  ST(4);
  __builtin_amdgcn_sched_barrier(0);

  float* sp = (float*)(smem + L_SP) + (w * 4 + r4) * NLMAX;
  float mx = -INFINITY;
  int so = 0;
#pragma unroll 1
  for (int it = 0; it < NIT; ++it) {
    const bool lastk = it == NIT - 1;
    const int c = cnt(it), r = cnt(lastk ? 0 : it + 1);
    so = lastk ? 0 : so + c * 4096;
    const rsrc_t rr = lastk ? rv : rk;
#define TR_STEP(HALF, CTRL, BIT) _Pragma("unroll") for (int hh = 0; hh < (HALF); ++hh) { \
      const float lo_ = xs[hh], hi_ = xs[hh + (HALF)]; const float t1_ = lo_ + dpp<CTRL>(lo_), t2_ = hi_ + dpp<CTRL>(hi_); xs[hh] = (cc & (BIT)) ? t2_ : t1_; }
#define K_GROUP(J0, N) float xs[N]; \
      _Pragma("unroll") for (int t = 0; t < (N); ++t) { \
        const int j = (J0) + t; \
        float d = 0; \
        if (j < WIN) { \
          if (j < ZMIN || j < c) { _Pragma("unroll") for (int jj = 0; jj < 4; ++jj) d = dot2(a[j < WIN ? j : 0][jj], qp[jj], d); } \
          asm volatile("" : "+v"(d)); \
          __builtin_amdgcn_sched_barrier(0); \
          if (j < ZMIN || j < r) a[j < WIN ? j : 0] = __builtin_amdgcn_raw_buffer_load_b128(rr, voff, so + j * 4096, AUXKV); \
          __builtin_amdgcn_sched_barrier(0); \
        } \
        xs[t] = d; \
      }
#pragma unroll
    for (int g = 0; g < 2; ++g) {
      K_GROUP(g * 16, 16)
      TR_STEP(8, XOR8, 8) TR_STEP(4, XOR7, 4) TR_STEP(2, XOR2, 2) TR_STEP(1, XOR1, 1)
      const int j = g * 16 + cc;
      const float sc = j < c ? rbf(rbf(xs[0]) * 0.08838834764831845f) : -INFINITY;
      mx = fmaxf(mx, sc);
      sp[it * SPW + j] = sc;
    }
    if (SPW > 32) {
      K_GROUP(32, 4)
#pragma unroll
      for (int t = 0; t < 4; ++t) { xs[t] += dpp<XOR8>(xs[t]); xs[t] += dpp<XOR7>(xs[t]); }
      TR_STEP(2, XOR2, 2) TR_STEP(1, XOR1, 1)
      const int j = 32 + (cc & 3);
      const float sc = j < c ? rbf(rbf(xs[0]) * 0.08838834764831845f) : -INFINITY;
      mx = fmaxf(mx, sc);
      if (cc < 4) sp[it * SPW + j] = sc;
    }
#undef K_GROUP
#undef TR_STEP
  }
  ST(5);
  mx = wmax(mx);
  float l = 0;
#pragma unroll
  for (int g = 0; g < NLMAX / 16; ++g) { const float p = __expf(sp[g * 16 + cc] - mx); l += p; sp[g * 16 + cc] = p; }
  l = wsum(l);

  if (isW) {
    const int m2 = 10 * xc + slot - 20, cq = lane & 3;
    const v4u* wl = (const v4u*)(smem + L_W + 65536 + w * 10240) + lane;
    float acc[8] = {0, 0, 0, 0, 0, 0, 0, 0};
#pragma unroll
    for (int i = 0; i < 5; ++i) {
      const P8 p = mkpairs(wl[(2 * i) * 64], wl[(2 * i + 1) * 64]);
#pragma unroll
      for (int j = 0; j < 8; ++j) acc[j] = dot2(p.p[j], sw[i], acc[j]);
    }
    float r = red8(acc, lane);
    r += dpp<SHL4>(r);
    float* red = (float*)(smem + L_RED);
    if ((lane & 4) == 0) red[w * 32 + cq * 8 + red8col(lane)] = r;
    bar();
    if (w == 0) {
      const int g = lane & 7, rep = lane >> 3;
      const f32x4 a0 = *(const f32x4*)(red + 4 * g), a1 = *(const f32x4*)(red + 32 + 4 * g), a2 = *(const f32x4*)(red + 64 + 4 * g), a3 = *(const f32x4*)(red + 96 + 4 * g);
      const f32x4 s = (a0 + a1) + (a2 + a3);
      if (rep < RM) stg(rw, OFF_M + rep * 10240 + (8 * m2 + g) * 16, pack2(s[0], s[1]), pack2(s[2], s[3]), tag);
    }
    bar();
  }

  if (CODEPF2 < 32 && slot >= CODEPF2) code_pf();
  float acc[8] = {0, 0, 0, 0, 0, 0, 0, 0};
#define V_SLOTS(LOAD) _Pragma("unroll") for (int g = 0; g < (WIN + 3) / 4; ++g) { \
      const f32x4 p4 = *(const f32x4*)&sp[it * SPW + g * 4]; \
      _Pragma("unroll") for (int t = 0; t < 4; ++t) { \
        const int j = g * 4 + t; \
        if (j < WIN) { \
          const float pp = p4[t]; \
          if (j < ZMIN || j < c) { \
            _Pragma("unroll") for (int jj = 0; jj < 4; ++jj) { const unsigned wv = a[j < WIN ? j : 0][jj]; acc[2 * jj] += pp * lo16(wv); acc[2 * jj + 1] += pp * hi16(wv); } \
          } \
          asm volatile("" : "+v"(acc[0]), "+v"(acc[1]), "+v"(acc[2]), "+v"(acc[3]), "+v"(acc[4]), "+v"(acc[5]), "+v"(acc[6]), "+v"(acc[7])); \
          __builtin_amdgcn_sched_barrier(0); \
          if (LOAD && (j < ZMIN || j < r)) a[j < WIN ? j : 0] = __builtin_amdgcn_raw_buffer_load_b128(rv, voff, so + j * 4096, AUXKV); \
          __builtin_amdgcn_sched_barrier(0); \
        } \
      } \
    }
  so = 0;
#pragma unroll 1
  for (int it = 0; it < NIT - 1; ++it) {
    const int c = cnt(it), r = cnt(it + 1);
    so += c * 4096;
    V_SLOTS(true)
  }
  {
    const int it = NIT - 1, c = cnt(it), r = 0;
    V_SLOTS(false)
  }
#undef V_SLOTS
  ST(6);
  {
    float* sm = (float*)(smem + L_RED);
#pragma unroll
    for (int j = 0; j < 8; ++j) { acc[j] = tr16(acc[j], acc[j]); acc[j] = tr32(acc[j], acc[j]); }
    if (r4 == 0) { *(f32x4*)(sm + w * 132 + 4 + 8 * cc) = f32x4{acc[0], acc[1], acc[2], acc[3]}; *(f32x4*)(sm + w * 132 + 8 + 8 * cc) = f32x4{acc[4], acc[5], acc[6], acc[7]}; }
    if (lane == 0) { sm[w * 132] = mx; sm[w * 132 + 1] = l; }
    bar();
    if (w < 2) {
      const float m0 = sm[0], m1 = sm[132], m2 = sm[264], m3 = sm[396];
      const float M = fmaxf(fmaxf(m0, m1), fmaxf(m2, m3));
      const float e0 = __expf(m0 - M), e1 = __expf(m1 - M), e2 = __expf(m2 - M), e3 = __expf(m3 - M);
      const int po = OFF_P + (head * 8 + xc) * PSTR;
      if (w == 0) {
        const float* s0 = sm + 4 + 2 * lane;
        const float o0 = e0 * s0[0] + e1 * s0[132] + e2 * s0[264] + e3 * s0[396], o1 = e0 * s0[1] + e1 * s0[133] + e2 * s0[265] + e3 * s0[397];
        stg(rw, po + lane * 16, asu(o0), asu(o1), tag);
      } else if (lane == 0) {
        stg(rw, po + 64 * 16, asu(M), asu(e0 * sm[1] + e1 * sm[133] + e2 * sm[265] + e3 * sm[397]), tag);
      }
    }
    bar();
  }
  ST(7);

  if (isW) {
    const int C = slot - 20, hh = 4 * xc + w, cq = lane & 7;
    const v4u* wl = (const v4u*)(smem + L_W + w * 16384) + lane;
    unsigned pr[64];
#pragma unroll
    for (int i = 0; i < 8; ++i) {
      const P8 p = mkpairs(wl[(2 * i) * 64], wl[(2 * i + 1) * 64]);
#pragma unroll
      for (int j = 0; j < 8; ++j) pr[8 * i + j] = p.p[j];
    }
    const int po = OFF_P + hh * 8 * PSTR;
    v4u g[8], ml;
    bool have[9] = {false, false, false, false, false, false, false, false, false};
#pragma nounroll
    for (int spin = 0; spin < SPIN_CAP; ++spin) {
      fence_c();
#pragma unroll
      for (int s = 0; s < 8; ++s) if (!have[s]) g[s] = ldg(rw, po + s * PSTR + lane * 16);
      if (!have[8]) ml = ldg(rw, po + (lane & 7) * PSTR + 64 * 16);
      bool all = true;
#pragma unroll
      for (int s = 0; s < 8; ++s) { have[s] = __all(okg(g[s], tag)); all &= have[s]; }
      have[8] = __all(okg(ml, tag)); all &= have[8];
      if (all) break;
      __builtin_amdgcn_s_sleep(1);
    }
    ST(8);
    const unsigned mu = ml[0], lu = ml[2];
    const float ms = asf(mu);
    float M = fmaxf(ms, dpp<XOR1>(ms)); M = fmaxf(M, dpp<XOR2>(M)); M = fmaxf(M, dpp<XOR7>(M));
    const float e = __expf(ms - M);
    float L = e * asf(lu); L += dpp<XOR1>(L); L += dpp<XOR2>(L); L += dpp<XOR7>(L);
    const float f = e * __builtin_amdgcn_rcpf(L);
    float o0 = 0, o1 = 0;
#pragma unroll
    for (int s = 0; s < 8; ++s) {
      const float fs = asf(__builtin_amdgcn_readlane(asu(f), s));
      const unsigned u0 = g[s][0], u1 = g[s][2];
      o0 += fs * asf(u0); o1 += fs * asf(u1);
    }
    unsigned* hw = (unsigned*)(smem + L_HW) + w * 192;
    hw[lane] = pack2(o0, o1);
    fence_c();
    const v4u h0 = *(const v4u*)(hw + 8 * (lane >> 3)), h1 = *(const v4u*)(hw + 8 * (lane >> 3) + 4);
    float y[8] = {0, 0, 0, 0, 0, 0, 0, 0};
#pragma unroll
    for (int i = 0; i < 8; ++i) {
      const unsigned hv = i < 4 ? h0[i & 3] : h1[i & 3];
#pragma unroll
      for (int j = 0; j < 8; ++j) y[j] = dot2(pr[8 * i + j], hv, y[j]);
    }
    const float r = red8(y, lane);
    float* red = (float*)(smem + L_RED);
    red[w * 64 + cq * 8 + red8col(lane)] = r;
    bar();
    if (w == 0 && lane < 32) {
      const float* rp = red + 2 * lane;
      const float y0 = (rp[0] + rp[64]) + (rp[128] + rp[192]), y1 = (rp[1] + rp[65]) + (rp[129] + rp[193]);
      stg(rw, OFF_Y + (xc * 320 + 32 * C + lane) * 16, asu(y0), asu(y1), tag);
    }
    ST(9);
  } else if (isF) {
    const int fj = 7 * xc + slot - 13, wh = w & 1, rho = lane >> 2, cq = lane & 3;
    unsigned pr[80], pd[64];
    {
      const v4u* wl = (const v4u*)(smem + L_W + w * 20480) + lane;
#pragma unroll
      for (int i = 0; i < 10; ++i) {
        const P8 p = mkpairs(wl[(2 * i) * 64], wl[(2 * i + 1) * 64]);
#pragma unroll
        for (int j = 0; j < 8; ++j) pr[8 * i + j] = p.p[j];
      }
      const int tc = tid < 160 ? tid : 0;
      const v2u* dl = (const v2u*)(smem + L_W + 81920) + tc;
#pragma unroll
      for (int i = 0; i < 16; ++i) {
        const v2u r0 = dl[(2 * i) * 160], r1 = dl[(2 * i + 1) * 160];
        const unsigned a0 = r0[0], a1 = r0[1], b0 = r1[0], b1 = r1[1];
        pd[4 * i] = plo(a0, b0); pd[4 * i + 1] = phi(a0, b0); pd[4 * i + 2] = plo(a1, b1); pd[4 * i + 3] = phi(a1, b1);
      }
    }
    float* x1s = (float*)(smem + L_X1); float* cbs = (float*)(smem + L_CB); float* sbs = (float*)(smem + L_SB);
    if (tid < 160) {
      const int mo = OFF_M + (xc % RM) * 10240 + tid * 16;
      v4u ga, sb, cb;
#pragma nounroll
      for (int spin = 0; spin < SPIN_CAP; ++spin) {
        fence_c();
        ga = ldg(rw, mo); sb = ldg(rw, mo + 2560); cb = ldg(rw, mo + 5120);
        if (__all(okg(ga, tag) && okg(sb, tag) && okg(cb, tag))) break;
        __builtin_amdgcn_s_sleep(1);
      }
      const v2u xw = *(const v2u*)(x + 4 * tid);
      { const unsigned c0 = cb[0], c1 = cb[2], s0 = sb[0], s1 = sb[2];
        *(f32x4*)(cbs + 4 * tid) = f32x4{rbf(1.f + lo16(c0)), rbf(1.f + hi16(c0)), rbf(1.f + lo16(c1)), rbf(1.f + hi16(c1))};
        *(f32x4*)(sbs + 4 * tid) = f32x4{lo16(s0), hi16(s0), lo16(s1), hi16(s1)}; }
      v4u g[16];
      bool have[8] = {false, false, false, false, false, false, false, false};
#pragma nounroll
      for (int spin = 0; spin < SPIN_CAP; ++spin) {
        fence_c();
#pragma unroll
        for (int G = 0; G < 8; ++G) if (!have[G]) { g[2 * G] = ldg(rw, OFF_Y + (G * 320 + 2 * tid) * 16); g[2 * G + 1] = ldg(rw, OFF_Y + (G * 320 + 2 * tid + 1) * 16); }
        bool all = true;
#pragma unroll
        for (int G = 0; G < 8; ++G) { have[G] = __all(okg(g[2 * G], tag) && okg(g[2 * G + 1], tag)); all &= have[G]; }
        if (all) break;
        __builtin_amdgcn_s_sleep(1);
      }
      float y[4] = {0, 0, 0, 0};
#pragma unroll
      for (int G = 0; G < 8; ++G) { const unsigned u0 = g[2 * G][0], u1 = g[2 * G][2], u2 = g[2 * G + 1][0], u3 = g[2 * G + 1][2]; y[0] += asf(u0); y[1] += asf(u1); y[2] += asf(u2); y[3] += asf(u3); }
      const unsigned g0 = ga[0], g1 = ga[2], x0 = xw[0], x1w = xw[1];
      *(f32x4*)(x1s + 4 * tid) = f32x4{rbf(lo16(x0) + rbf(lo16(g0) * rbf(y[0]))), rbf(hi16(x0) + rbf(hi16(g0) * rbf(y[1]))),
                                       rbf(lo16(x1w) + rbf(lo16(g1) * rbf(y[2]))), rbf(hi16(x1w) + rbf(hi16(g1) * rbf(y[3])))};
    }
    ST(8);
    bar();
    ST(11);
    float xa[5], xb[5];
#pragma unroll
    for (int i = 0; i < 5; ++i) { xa[i] = x1s[5 * lane + i]; xb[i] = x1s[320 + 5 * lane + i]; }
    float s = 0;
#pragma unroll
    for (int i = 0; i < 5; ++i) s += xa[i] + xb[i];
    const float mean = wsum(s) * (1.f / D);
    float v = 0;
#pragma unroll
    for (int i = 0; i < 5; ++i) { const float d0 = xa[i] - mean, d1 = xb[i] - mean; v += d0 * d0 + d1 * d1; }
    const float var = wsum(v) * (1.f / D);
    const float rs = __builtin_amdgcn_rcpf(rbf(__builtin_sqrtf(rbf(rbf(var) + 1e-5f)))), mb = rbf(mean);
    STD(12, rs);
    u16* hs = (u16*)((unsigned*)(smem + L_HW) + w * 192);
#pragma unroll
    for (int i = 0; i < 5; ++i) {
      const int col = 320 * wh + 5 * lane + i;
      const float xx = wh ? xb[i] : xa[i];
      hs[5 * lane + i] = (u16)pack2(rbf(rbf(rbf(xx - mb) * rs) * cbs[col]) + sbs[col], 0.f);
    }
    fence_c();
    const v2u* hp = (const v2u*)((const unsigned*)hs + 10 * rho);
    float ga8[8] = {0, 0, 0, 0, 0, 0, 0, 0};
#pragma unroll
    for (int i = 0; i < 5; ++i) {
      const v2u hv = hp[i];
      const unsigned h0 = hv[0], h1 = hv[1];
#pragma unroll
      for (int j = 0; j < 8; ++j) ga8[j] = dot2(pr[16 * i + j], h0, ga8[j]);
#pragma unroll
      for (int j = 0; j < 8; ++j) ga8[j] = dot2(pr[16 * i + 8 + j], h1, ga8[j]);
    }
    STD(13, ga8[7]);
    float r = red8(ga8, lane);
    r += dpp<SHL4>(r);
    float* red = (float*)(smem + L_RED);
    if ((lane & 4) == 0) red[w * 32 + cq * 8 + red8col(lane)] = r;
    bar();
    ST(14);
    const int col = lane & 31;
    const float gg = rbf(red[col] + red[32 + col]), uu = rbf(red[64 + col] + red[96 + col]);
    const float av = rbf(rbf(silu(gg)) * uu);
    const unsigned aw = (asu(av) >> 16) | (asu(dpp<XOR1>(av)) & 0xffff0000u);
    float z[4] = {0, 0, 0, 0};
#pragma unroll
    for (int i = 0; i < 16; ++i) {
      const unsigned as = __builtin_amdgcn_readlane(aw, 2 * i);
#pragma unroll
      for (int j = 0; j < 4; ++j) z[j] = dot2(pd[4 * i + j], as, z[j]);
    }
    STD(15, z[3]);
    if (tid < 160) {
      stg(rw, OFF_Z + (fj * 320 + 2 * tid) * 16, asu(z[0]), asu(z[1]), tag);
      stg(rw, OFF_Z + (fj * 320 + 2 * tid + 1) * 16, asu(z[2]), asu(z[3]), tag);
    }
    ST(9);
  } else if (isM1) {
    const int fi = 5 * xc + slot, g8 = lane & 7;
    float x1a = 0, x1b = 0, gb0 = 0, gb1 = 0;
    if (w == 0) {
      const int mo = OFF_M + (xc % RM) * 10240 + (4 * fi + (g8 >> 1)) * 16;
      v4u ga, gb, yy;
#pragma nounroll
      for (int spin = 0; spin < SPIN_CAP; ++spin) {
        fence_c();
        ga = ldg(rw, mo); gb = ldg(rw, mo + 7680); yy = ldg(rw, OFF_Y + ((lane >> 3) * 320 + 8 * fi + g8) * 16);
        if (__all(okg(ga, tag) && okg(gb, tag) && okg(yy, tag))) break;
        __builtin_amdgcn_s_sleep(1);
      }
      const unsigned xw = ((const unsigned*)x)[8 * fi + g8];
      const unsigned gaw = (g8 & 1) ? ga[2] : ga[0], gbw = (g8 & 1) ? gb[2] : gb[0], u0 = yy[0], u1 = yy[2];
      float y0 = 0, y1 = 0;
#pragma unroll
      for (int G = 0; G < 8; ++G) { y0 += __shfl(asf(u0), G * 8 + g8); y1 += __shfl(asf(u1), G * 8 + g8); }
      x1a = rbf(lo16(xw) + rbf(lo16(gaw) * rbf(y0))); x1b = rbf(hi16(xw) + rbf(hi16(gaw) * rbf(y1)));
      gb0 = lo16(gbw); gb1 = hi16(gbw);
    }
    ST(8);
    const int p0 = tid >> 3;
    v4u z0, z1 = {0, 0, 0, 0};
    bool h0 = false, h1 = false;
#pragma nounroll
    for (int spin = 0; spin < SPIN_CAP; ++spin) {
      fence_c();
      if (!h0) z0 = ldg(rw, OFF_Z + (p0 * 320 + 8 * fi + g8) * 16);
      if (!h1 && p0 < 22) z1 = ldg(rw, OFF_Z + ((p0 + 32) * 320 + 8 * fi + g8) * 16);
      h0 = __all(okg(z0, tag)); h1 = __all(p0 >= 22 || okg(z1, tag));
      if (h0 && h1) break;
      __builtin_amdgcn_s_sleep(1);
    }
    ST(9);
    const unsigned u0 = z0[0], u1 = z0[2], u2 = z1[0], u3 = z1[2];
    float s0 = asf(u0) + asf(u2), s1 = asf(u1) + asf(u3);
    s0 += dpp<XOR8>(s0); s1 += dpp<XOR8>(s1);
    s0 = tr16(s0, s0); s1 = tr16(s1, s1); s0 = tr32(s0, s0); s1 = tr32(s1, s1);
    float* red = (float*)(smem + L_RED);
    if (lane < 8) { red[w * 16 + 2 * lane] = s0; red[w * 16 + 2 * lane + 1] = s1; }
    bar();
    if (w == 0 && lane < 8) {
      const float* rp = red + 2 * lane;
      const float y0 = (rp[0] + rp[16]) + (rp[32] + rp[48]), y1 = (rp[1] + rp[17]) + (rp[33] + rp[49]);
      ((unsigned*)out)[8 * fi + lane] = pack2(x1a + rbf(gb0 * rbf(y0)), x1b + rbf(gb1 * rbf(y1)));
      if (fi == 0 && lane == 0) __builtin_amdgcn_raw_buffer_store_b32(tag, rw, 0, 0, 16);
    }
    ST(10);
  }
  asm volatile(".Lmega_end:");
}

extern "C" void kernel_launch(void* const* d_in, const int* in_sizes, int n_in, void* d_out, int out_size, void* d_ws, size_t ws_size,
                              hipStream_t stream) {
  hipFuncSetAttribute((const void*)mega, hipFuncAttributeMaxDynamicSharedMemorySize, LDS_BYTES);
  mega<<<256, 256, LDS_BYTES, stream>>>((const u16*)d_in[0], (const u16*)d_in[1], (const u16*)d_in[2], (const u16*)d_in[3], (const u16*)d_in[4],
                                        (const u16*)d_in[6], (const u16*)d_in[7], (const u16*)d_in[8], (const u16*)d_in[9], (const u16*)d_in[10],
                                        (u16*)d_out, (unsigned*)d_ws);
}
```

```cpp
#include <hip/hip_runtime.h>
#include <stdint.h>

typedef unsigned short u16;
typedef unsigned v4u __attribute__((ext_vector_type(4)));
typedef unsigned v2u __attribute__((ext_vector_type(2)));
typedef float f32x4 __attribute__((ext_vector_type(4)));
typedef __bf16 bf16x2 __attribute__((ext_vector_type(2)));
typedef __amdgpu_buffer_rsrc_t rsrc_t;
#define LAS __attribute__((address_space(3)))
#define DI __device__ __forceinline__

#define TFIX 7000
#define AUXKV 2
#define BULK_DELAY 16
#define PREQ 0
#define NBPOLL 3
#define AUXQ 2
#define NTFULL 1
#define NTHALF 0
#define CODEPF 32
#define CODEPF2 32
#define ARGS1 1
#define RH 4
#define RM 4
#define DELTAS 5, 6, 5, 5, 6, -4, -5, -5, -5, -5, -5, -5, -6, -2, -3, -3, -4, -3, -4, -4, 1, 3, 1, 1, 2, 3, 1, 1, 1, 2, 10, 10
#define PARITY 0
#define SPIN_CAP (1 << 16)

constexpr int D = 640, SK = 16384, F = 1728;
constexpr int OFF_H = 256;
constexpr int OFF_Q = OFF_H + RH * 5120;
constexpr int OFF_M = OFF_Q + 32 * 512;
constexpr int PSTR = 72 * 16;
constexpr int OFF_P = OFF_M + RM * 10240;
constexpr int OFF_Y = OFF_P + 256 * PSTR;
constexpr int OFF_Z = OFF_Y + 8 * 5120;
constexpr int WS_BYTES = OFF_Z + 54 * 5120;
constexpr int OFF_STAMP = 4 << 20;
constexpr int KDELTA[32] = {DELTAS};
constexpr unsigned long long kcount(int c) { unsigned long long r = 0; for (int k = 0; k < 8; ++k) r |= (unsigned long long)(64 + KDELTA[c + 4 * k]) << (8 * k); return r; }
constexpr unsigned long long kstart(int c) { unsigned long long r = 0; int s = 0; for (int k = 0; k < 8; ++k) { r |= (unsigned long long)(64 + s) << (8 * k); s += KDELTA[c + 4 * k]; } return r; }
constexpr bool kok() { for (int c = 0; c < 4; ++c) { int s = 0; for (int k = 0; k < 8; ++k) { s += KDELTA[c + 4 * k]; if (s < -60 || s > 60) return false; } if (s) return false; } return true; }
constexpr int kmin() { int m = 0; for (int i = 0; i < 32; ++i) m = KDELTA[i] < m ? KDELTA[i] : m; return 128 + m - PARITY; }
constexpr int kmax() { int m = 0; for (int i = 0; i < 32; ++i) m = KDELTA[i] > m ? KDELTA[i] : m; return 128 + m + PARITY; }
static_assert(kok(), "key split");
constexpr int NIT = 4, ZMIN = kmin() / NIT, WIN = (kmax() + NIT - 1) / NIT, SPW = WIN <= 32 ? 32 : 36, NLMAX = NIT * SPW;
static_assert(WIN <= 36 && ZMIN >= 20, "ring");
constexpr int L_SP = 0;
constexpr int L_RED = L_SP + 16 * NLMAX * 4;
constexpr int L_X1 = L_RED + 4096;
constexpr int L_CB = L_X1 + 2560;
constexpr int L_SB = L_CB + 2560;
constexpr int L_HW = L_SB + 2560;
constexpr int L_W = L_HW + 4 * 768;
constexpr int L_DUMMY = L_W + 120 * 1024;
constexpr int LDS_BYTES = L_DUMMY + 1024;
static_assert(L_W % 16 == 0 && LDS_BYTES <= 160 * 1024, "lds");

DI float asf(unsigned u) { return __builtin_bit_cast(float, u); }
DI unsigned asu(float f) { return __builtin_bit_cast(unsigned, f); }
DI float lo16(unsigned w) { return asf(w << 16); }
DI float hi16(unsigned w) { return asf(w & 0xffff0000u); }
typedef float f32x2 __attribute__((ext_vector_type(2)));
DI unsigned pack2(float a, float b) { const f32x2 v = {a, b}; return __builtin_bit_cast(unsigned, __builtin_convertvector(v, bf16x2)); }
DI float rbf(float f) { return asf(pack2(0.f, f)); }
DI float dot2(unsigned k, unsigned q, float acc) {
  return __builtin_amdgcn_fdot2_f32_bf16(__builtin_bit_cast(bf16x2, k), __builtin_bit_cast(bf16x2, q), acc, false);
}
template <int CTRL> DI float dpp(float x) { return asf(__builtin_amdgcn_mov_dpp(asu(x), CTRL, 0xf, 0xf, true)); }
constexpr int XOR1 = 0xB1, XOR2 = 0x4E, XOR7 = 0x141, XOR8 = 0x128, ROR4 = 0x124, SHL4 = 0x104;
DI float tr32(float a, float b) { v2u r = __builtin_amdgcn_permlane32_swap(asu(a), asu(b), false, false); unsigned p = r[0], q = r[1]; return asf(p) + asf(q); }
DI float tr16(float a, float b) { v2u r = __builtin_amdgcn_permlane16_swap(asu(a), asu(b), false, false); unsigned p = r[0], q = r[1]; return asf(p) + asf(q); }
DI float tr8(float a, float b, bool hi) { const float t1 = a + dpp<XOR8>(a), t2 = b + dpp<XOR8>(b); return hi ? t2 : t1; }
DI float wsum(float v) {
  v += dpp<XOR1>(v); v += dpp<XOR2>(v); v += dpp<XOR7>(v); v += dpp<XOR8>(v); v = tr16(v, v); return tr32(v, v);
}
DI float mx16(float a) { v2u r = __builtin_amdgcn_permlane16_swap(asu(a), asu(a), false, false); unsigned p = r[0], q = r[1]; return fmaxf(asf(p), asf(q)); }
DI float mx32(float a) { v2u r = __builtin_amdgcn_permlane32_swap(asu(a), asu(a), false, false); unsigned p = r[0], q = r[1]; return fmaxf(asf(p), asf(q)); }
DI float wmax(float v) {
  v = fmaxf(v, dpp<XOR1>(v)); v = fmaxf(v, dpp<XOR2>(v)); v = fmaxf(v, dpp<XOR7>(v)); v = fmaxf(v, dpp<XOR8>(v)); return mx32(mx16(v));
}
DI float silu(float v) { return v * __builtin_amdgcn_rcpf(1.f + __expf(-v)); }
DI unsigned plo(unsigned a, unsigned b) { return __builtin_amdgcn_perm(b, a, 0x05040100u); }
DI unsigned phi(unsigned a, unsigned b) { return __builtin_amdgcn_perm(b, a, 0x07060302u); }
struct P8 { unsigned p[8]; };
DI P8 mkpairs(v4u a, v4u b) {
  P8 r;
#pragma unroll
  for (int i = 0; i < 4; ++i) { const unsigned x = a[i], y = b[i]; r.p[2 * i] = plo(x, y); r.p[2 * i + 1] = phi(x, y); }
  return r;
}
DI rsrc_t mkrsrc(const void* p, unsigned bytes) { return __builtin_amdgcn_make_buffer_rsrc((void*)p, 0, bytes, 0x00020000); }
DI v4u ldg(rsrc_t r, int off) { return __builtin_amdgcn_raw_buffer_load_b128(r, off, 0, 16); }
DI void stg(rsrc_t r, int off, unsigned a, unsigned b, unsigned tag) { v4u g = {a, tag, b, tag}; __builtin_amdgcn_raw_buffer_store_b128(g, r, off, 0, 16); }
DI bool okg(v4u g, unsigned tag) { const unsigned a = g[1], b = g[3]; return a == tag && b == tag; }
DI void fence_c() { asm volatile("" ::: "memory"); }
DI void dma16(rsrc_t r, unsigned voff, unsigned soff, unsigned lds) {
  unsigned keep;
  asm volatile("s_mov_b32 %0, m0\n\ts_mov_b32 m0, %1\n\ts_nop 0\n\tbuffer_load_dwordx4 %2, %3, %4 offen lds\n\ts_mov_b32 m0, %0"
               : "=&s"(keep) : "s"(lds), "v"(voff), "s"(r), "s"(soff) : "memory");
}
#define DMA_BODY(NT) ".rept %c[n]\n\ts_nop 0\n\tbuffer_load_dwordx4 %[vo], %[r], %[so] offen" NT " lds\n\ts_add_u32 m0, m0, 0x400\n\ts_add_u32 %[so], %[so], %[st]\n\t.endr\n\t"
template <int N, bool NT> DI void dma_batch(rsrc_t r, unsigned voff, unsigned soff, unsigned stride, unsigned lds) {
  unsigned keep;
  if (NT) asm volatile("s_mov_b32 %[k], m0\n\ts_mov_b32 m0, %[l]\n\t" DMA_BODY(" nt") "s_mov_b32 m0, %[k]"
               : [k] "=&s"(keep), [so] "+s"(soff) : [vo] "v"(voff), [r] "s"(r), [st] "s"(stride), [l] "s"(lds), [n] "n"(N) : "memory", "scc");
  else asm volatile("s_mov_b32 %[k], m0\n\ts_mov_b32 m0, %[l]\n\t" DMA_BODY("") "s_mov_b32 m0, %[k]"
               : [k] "=&s"(keep), [so] "+s"(soff) : [vo] "v"(voff), [r] "s"(r), [st] "s"(stride), [l] "s"(lds), [n] "n"(N) : "memory", "scc");
}
#define POLL2 "buffer_load_dwordx4 %[g0], %[go], %[rw], 0 offen sc1\n\tbuffer_load_dwordx4 %[g1], %[go], %[rw], 0 offen offset:16 sc1\n\t"
template <int N, bool NT> DI void poll_dma(v4u& g0, v4u& g1, int goff, rsrc_t rw, rsrc_t r, unsigned voff, unsigned soff, unsigned stride, unsigned lds) {
  unsigned keep;
  if (NT) asm volatile(POLL2 "s_mov_b32 %[k], m0\n\ts_mov_b32 m0, %[l]\n\t" DMA_BODY(" nt") "s_mov_b32 m0, %[k]\n\ts_waitcnt vmcnt(%c[n])"
               : [g0] "=&v"(g0), [g1] "=&v"(g1), [k] "=&s"(keep), [so] "+s"(soff)
               : [go] "v"(goff), [rw] "s"(rw), [vo] "v"(voff), [r] "s"(r), [st] "s"(stride), [l] "s"(lds), [n] "n"(N) : "memory", "scc");
  else asm volatile(POLL2 "s_mov_b32 %[k], m0\n\ts_mov_b32 m0, %[l]\n\t" DMA_BODY("") "s_mov_b32 m0, %[k]\n\ts_waitcnt vmcnt(%c[n])"
               : [g0] "=&v"(g0), [g1] "=&v"(g1), [k] "=&s"(keep), [so] "+s"(soff)
               : [go] "v"(goff), [rw] "s"(rw), [vo] "v"(voff), [r] "s"(r), [st] "s"(stride), [l] "s"(lds), [n] "n"(N) : "memory", "scc");
}
DI void bar() { asm volatile("s_waitcnt lgkmcnt(0)\n\ts_barrier" ::: "memory"); }
DI float red8(const float (&a)[8], int lane) {
  const float b0 = tr32(a[0], a[4]), b1 = tr32(a[1], a[5]), b2 = tr32(a[2], a[6]), b3 = tr32(a[3], a[7]);
  const float c0 = tr16(b0, b2), c1 = tr16(b1, b3);
  return tr8(c0, c1, lane & 8);
}
DI int red8col(int lane) { return ((lane >> 5) & 1) * 4 + ((lane >> 4) & 1) * 2 + ((lane >> 3) & 1); }

#define STAMPMASK 0xffff
#define ST(i) do { } while (0)
#define STD(i, x) do { } while (0)

__global__ void __launch_bounds__(256, 1)
mega(const u16* __restrict__ x, const u16* __restrict__ K, const u16* __restrict__ V, const u16* __restrict__ wq, const u16* __restrict__ wo,
     const u16* __restrict__ wg, const u16* __restrict__ wu, const u16* __restrict__ wd, const u16* __restrict__ c, const u16* __restrict__ wmod,
     u16* __restrict__ out, unsigned* ws) {
  extern __shared__ __attribute__((aligned(16))) char smem[];
  const int tid = threadIdx.x, lane = tid & 63, w = __builtin_amdgcn_readfirstlane(tid >> 6), b = blockIdx.x;
  const int xc = b & 7, slot = b >> 3, head = (slot - 4 * xc) & 31;
  ST(0);
  const unsigned long long tstart = __builtin_amdgcn_s_memrealtime();
  const unsigned ldsb = __builtin_amdgcn_readfirstlane((unsigned)(size_t)(LAS char*)smem);
  rsrc_t rc;
  {
    unsigned long long pc; unsigned len;
    asm volatile("s_getpc_b64 %0\n.Lmega_pf0:\n\ts_mov_b32 %1, .Lmega_end-.Lmega_pf0" : "=s"(pc), "=s"(len));
    rc = mkrsrc((const void*)pc, len);
  }
  auto code_pf = [&]() {
    unsigned keep;
    asm volatile("s_mov_b32 %0, m0\n\ts_mov_b32 m0, %1\n\ts_nop 0\n\tbuffer_load_dword %2, %3, 0 offen lds\n\ts_mov_b32 m0, %0"
                 : "=&s"(keep) : "s"(ldsb + L_DUMMY), "v"(tid * 128), "s"(rc) : "memory");
  };
  if (b < CODEPF) code_pf();
  if (ARGS1) asm volatile("" :: "s"(x), "s"(K), "s"(V), "s"(wq), "s"(wo), "s"(wg), "s"(wu), "s"(wd), "s"(c), "s"(wmod), "s"(out), "s"(ws));
  const rsrc_t rw = mkrsrc(ws, WS_BYTES);
  const unsigned ew = __builtin_amdgcn_raw_buffer_load_b32(rw, 0, 0, 16);
  const bool isM1 = slot < 5, isQ = slot >= 5 && slot < 13, isF = slot >= 13 && slot < 20 && 7 * xc + slot - 13 < 54, isW = slot >= 20 && slot < 30;
  unsigned tag = 0;
  int nch, ch0;
  {
    const int cl = slot & 3, k = slot >> 2, e = (xc & 1) ? PARITY : -PARITY;
    const unsigned long long tc = cl == 0 ? kcount(0) : cl == 1 ? kcount(1) : cl == 2 ? kcount(2) : kcount(3);
    const unsigned long long ts = cl == 0 ? kstart(0) : cl == 1 ? kstart(1) : cl == 2 ? kstart(2) : kstart(3);
    nch = 64 + (int)((tc >> (8 * k)) & 255) + e;
    ch0 = 128 * k - 64 + (int)((ts >> (8 * k)) & 255) - ((k & 1) ? e : 0);
  }
  const int nkeys = 16 * nch, key0 = 16 * ch0;
  auto cnt = [&](int it) { return (nch + NIT - 1 - it) / NIT; };
  const rsrc_t rk = mkrsrc(K + ((size_t)head * SK + key0) * 128, nkeys * 256);
  const rsrc_t rv = mkrsrc(V + ((size_t)head * SK + key0) * 128, nkeys * 256);
  const int voff = lane * 16 + w * 1024;
  v4u a[WIN];
  unsigned sw[5];

  if (isM1) {
    const int mi = 5 * xc + slot, t = mi >= 20, g32 = mi - 20 * t, rho = lane >> 2, cq = lane & 3;
    const rsrc_t rm = mkrsrc(wmod, D * 6 * D * 2);
    const unsigned* cw = (const unsigned*)c + 80 * w + 5 * rho;
    unsigned cv[5];
#pragma unroll
    for (int i = 0; i < 5; ++i) cv[i] = cw[i];
    const int vo = (160 * w + 10 * rho) * 7680 + (t * D + 32 * g32 + 8 * cq) * 2;
    v4u wv[10];
#pragma unroll
    for (int i = 0; i < 10; ++i) wv[i] = __builtin_amdgcn_raw_buffer_load_b128(rm, vo, i * 7680, 0);
    __builtin_amdgcn_sched_barrier(0);
    ST(11);
#pragma unroll
    for (int i = 0; i < 5; ++i) sw[i] = pack2(silu(lo16(cv[i])), silu(hi16(cv[i])));
    STD(12, sw[4]);
    float acc[8] = {0, 0, 0, 0, 0, 0, 0, 0};
#pragma unroll
    for (int i = 0; i < 5; ++i) {
      const P8 p = mkpairs(wv[2 * i], wv[2 * i + 1]);
#pragma unroll
      for (int j = 0; j < 8; ++j) acc[j] = dot2(p.p[j], sw[i], acc[j]);
    }
    STD(14, acc[7]);
    float r = red8(acc, lane);
    r += dpp<SHL4>(r);
    float* red = (float*)(smem + L_RED);
    if ((lane & 4) == 0) red[w * 32 + cq * 8 + red8col(lane)] = r;
    bar();
    if (w == 0) {
      const int g = lane & 7, rep = lane >> 3;
      const f32x4 a0 = *(const f32x4*)(red + 4 * g), a1 = *(const f32x4*)(red + 32 + 4 * g), a2 = *(const f32x4*)(red + 64 + 4 * g), a3 = *(const f32x4*)(red + 96 + 4 * g);
      f32x4 s = (a0 + a1) + (a2 + a3);
      if (t) { s[0] = 1.f + rbf(s[0]); s[1] = 1.f + rbf(s[1]); s[2] = 1.f + rbf(s[2]); s[3] = 1.f + rbf(s[3]); }
      tag = __builtin_amdgcn_readfirstlane(ew) + 1;
      if (rep < RH) stg(rw, OFF_H + rep * 5120 + (t * 160 + 8 * g32 + g) * 16, pack2(s[0], s[1]), pack2(s[2], s[3]), tag);
    }
    ST(1);
  } else if (isQ) {
    const int qi = 8 * xc + slot - 5, rho = lane >> 3, cq = lane & 7, lc = lane < 40 ? lane : 39;
    const rsrc_t rq = mkrsrc(wq, D * 4096 * 2);
    unsigned xv[5];
#pragma unroll
    for (int i = 0; i < 5; ++i) xv[i] = ((const unsigned*)x)[5 * lane + i];
    const v2u xo = *(const v2u*)(x + 160 * w + 4 * lc);
    const int vo = (160 * w + 20 * rho) * 8192 + (64 * qi + 8 * cq) * 2;
    v4u wv[20];
#pragma unroll
    for (int i = 0; i < 20; ++i) wv[i] = __builtin_amdgcn_raw_buffer_load_b128(rq, vo, i * 8192, AUXQ);
    __builtin_amdgcn_sched_barrier(0);
#pragma unroll
    for (int i = 0; i < PREQ; ++i) a[i] = __builtin_amdgcn_raw_buffer_load_b128(rk, voff, i * 4096, AUXKV);
    __builtin_amdgcn_sched_barrier(0);
    float s = 0;
#pragma unroll
    for (int i = 0; i < 5; ++i) s += lo16(xv[i]) + hi16(xv[i]);
    const float mean = wsum(s) * (1.f / D);
    float v = 0;
#pragma unroll
    for (int i = 0; i < 5; ++i) { const float d0 = lo16(xv[i]) - mean, d1 = hi16(xv[i]) - mean; v += d0 * d0 + d1 * d1; }
    const float var = wsum(v) * (1.f / D);
    const float rs = __builtin_amdgcn_rcpf(rbf(__builtin_sqrtf(rbf(rbf(var) + 1e-5f)))), mb = rbf(mean);
    const unsigned xo0 = xo[0], xo1 = xo[1];
    const float n0 = rbf(rbf(lo16(xo0) - mb) * rs), n1 = rbf(rbf(hi16(xo0) - mb) * rs), n2 = rbf(rbf(lo16(xo1) - mb) * rs), n3 = rbf(rbf(hi16(xo1) - mb) * rs);
    unsigned pr[80];
#pragma unroll
    for (int i = 0; i < 10; ++i) {
      const P8 p = mkpairs(wv[2 * i], wv[2 * i + 1]);
#pragma unroll
      for (int j = 0; j < 8; ++j) pr[8 * i + j] = p.p[j];
    }
    tag = __builtin_amdgcn_readfirstlane(ew) + 1;
    ST(1);
    STD(11, pr[79]);
    v4u gs, gc;
    const int go = OFF_H + (xc % RH) * 5120 + (40 * w + lc) * 16;
#pragma nounroll
    for (int spin = 0; spin < SPIN_CAP; ++spin) {
      fence_c();
      gs = ldg(rw, go); gc = ldg(rw, go + 2560);
      if (__all(okg(gs, tag) && okg(gc, tag))) break;
      __builtin_amdgcn_s_sleep(1);
    }
    ST(2);
    unsigned* hw = (unsigned*)(smem + L_HW) + w * 192;
    {
      const unsigned s0 = gs[0], s1 = gs[2], c0 = gc[0], c1 = gc[2];
      const v2u hh = {pack2(rbf(n0 * lo16(c0)) + lo16(s0), rbf(n1 * hi16(c0)) + hi16(s0)), pack2(rbf(n2 * lo16(c1)) + lo16(s1), rbf(n3 * hi16(c1)) + hi16(s1))};
      *(v2u*)(hw + 2 * lc) = hh;
    }
    fence_c();
    float acc[8] = {0, 0, 0, 0, 0, 0, 0, 0};
#pragma unroll
    for (int i = 0; i < 5; ++i) {
      const v2u hv = *(const v2u*)(hw + 10 * rho + 2 * i);
      const unsigned h0 = hv[0], h1 = hv[1];
#pragma unroll
      for (int j = 0; j < 8; ++j) acc[j] = dot2(pr[16 * i + j], h0, acc[j]);
#pragma unroll
      for (int j = 0; j < 8; ++j) acc[j] = dot2(pr[16 * i + 8 + j], h1, acc[j]);
    }
    const float r = red8(acc, lane);
    float* red = (float*)(smem + L_RED);
    red[w * 64 + cq * 8 + red8col(lane)] = r;
    bar();
    if (w == 0 && lane < 16) {
      const f32x4 a0 = *(const f32x4*)(red + 4 * lane), a1 = *(const f32x4*)(red + 64 + 4 * lane), a2 = *(const f32x4*)(red + 128 + 4 * lane), a3 = *(const f32x4*)(red + 192 + 4 * lane);
      const f32x4 s = (a0 + a1) + (a2 + a3);
      stg(rw, OFF_Q + (16 * qi + lane) * 16, pack2(s[0], s[1]), pack2(s[2], s[3]), tag);
    }
    ST(3);
  }
  __builtin_amdgcn_sched_barrier(0);

  unsigned cvm[5];
  if (isW) {
#pragma unroll
    for (int i = 0; i < 5; ++i) cvm[i] = ((const unsigned*)c)[80 * w + 5 * (lane >> 2) + i];
  }
  __builtin_amdgcn_sched_barrier(0);
  if (BULK_DELAY > 0 && !isM1 && !isQ) __builtin_amdgcn_s_sleep(BULK_DELAY);
  if (PREQ > 0 && !isQ) {
#pragma unroll
    for (int i = 0; i < PREQ; ++i) a[i] = __builtin_amdgcn_raw_buffer_load_b128(rk, voff, i * 4096, AUXKV);
  }
  {
    const int c0 = cnt(0);
#pragma unroll
    for (int i = PREQ; i < WIN; ++i) {
      if (i >= ZMIN) a[i] = v4u{0, 0, 0, 0};
      if (i < ZMIN || i < c0) a[i] = __builtin_amdgcn_raw_buffer_load_b128(rk, voff, i * 4096, AUXKV);
    }
  }
  __builtin_amdgcn_sched_barrier(0);

  if (isW) {
#pragma unroll
    for (int i = 0; i < 5; ++i) sw[i] = pack2(silu(lo16(cvm[i])), silu(hi16(cvm[i])));
  }
  tag = __builtin_amdgcn_readfirstlane(ew) + 1;
  const int r4 = lane >> 4, cc = lane & 15;
  v4u qp;
  {
    v4u g0, g1;
    const int go = OFF_Q + head * 512 + cc * 32;
    bool got = false;
    if (isW || isF) {
      rsrc_t r0, r1, r2; unsigned v0, v1, v2, s1, s2, st0, st1, st2, l0, l1, l2;
      if (isW) {
        const int C = slot - 20, m2 = 10 * xc + C;
        r0 = mkrsrc(wmod, D * 6 * D * 2); r1 = r2 = mkrsrc(wo, 4096 * D * 2);
        v0 = (160 * w + 10 * (lane >> 2)) * 7680 + (2 * D + 32 * m2 + 8 * (lane & 3)) * 2;
        v1 = v2 = ((512 * xc + 128 * w + 16 * (lane >> 3)) * D + 64 * C + 8 * (lane & 7)) * 2;
        st0 = 7680; st1 = st2 = 1280; s1 = 0; s2 = 8 * 1280;
        l0 = ldsb + L_W + 65536 + w * 10240; l1 = ldsb + L_W + w * 16384; l2 = l1 + 8192;
      } else {
        const int fj = 7 * xc + slot - 13;
        r0 = r1 = mkrsrc(w < 2 ? wg : wu, D * F * 2); r2 = mkrsrc(wd, F * D * 2);
        v0 = v1 = ((320 * (w & 1) + 20 * (lane >> 2)) * F + 32 * fj + 8 * (lane & 3)) * 2;
        v2 = (32 * fj * D) * 2 + w * 10240 + lane * 16;
        st0 = st1 = F * 2; st2 = 1024; s1 = 10 * F * 2; s2 = 0;
        l0 = ldsb + L_W + w * 20480; l1 = l0 + 10240; l2 = ldsb + L_W + 81920 + w * 10240;
      }
      l0 = __builtin_amdgcn_readfirstlane(l0); l1 = __builtin_amdgcn_readfirstlane(l1); l2 = __builtin_amdgcn_readfirstlane(l2);
      if (NBPOLL > 0) { poll_dma<10, NTHALF>(g0, g1, go, rw, r0, v0, 0, st0, l0); got = __all(okg(g0, tag) && okg(g1, tag)); }
      else dma_batch<10, NTHALF>(r0, v0, 0, st0, l0);
      if (isW) {
        if (NBPOLL > 1 && !got) { poll_dma<8, NTFULL>(g0, g1, go, rw, r1, v1, s1, st1, l1); got = __all(okg(g0, tag) && okg(g1, tag)); }
        else dma_batch<8, NTFULL>(r1, v1, s1, st1, l1);
        if (NBPOLL > 2 && !got) { poll_dma<8, NTFULL>(g0, g1, go, rw, r2, v2, s2, st2, l2); got = __all(okg(g0, tag) && okg(g1, tag)); }
        else dma_batch<8, NTFULL>(r2, v2, s2, st2, l2);
      } else {
        if (NBPOLL > 1 && !got) { poll_dma<10, NTHALF>(g0, g1, go, rw, r1, v1, s1, st1, l1); got = __all(okg(g0, tag) && okg(g1, tag)); }
        else dma_batch<10, NTHALF>(r1, v1, s1, st1, l1);
        if (NBPOLL > 2 && !got) { poll_dma<10, NTFULL>(g0, g1, go, rw, r2, v2, s2, st2, l2); got = __all(okg(g0, tag) && okg(g1, tag)); }
        else dma_batch<10, NTFULL>(r2, v2, s2, st2, l2);
      }
    }
    if (!got) {
#pragma nounroll
      for (int spin = 0; spin < SPIN_CAP; ++spin) {
        fence_c();
        g0 = ldg(rw, go); g1 = ldg(rw, go + 16);
        if (__all(okg(g0, tag) && okg(g1, tag))) break;
        __builtin_amdgcn_s_sleep(1);
      }
    }
    qp[0] = g0[0]; qp[1] = g0[2]; qp[2] = g1[0]; qp[3] = g1[2];
  }
  ST(4);
  __builtin_amdgcn_sched_barrier(0);

  float* sp = (float*)(smem + L_SP) + (w * 4 + r4) * NLMAX;
  float mx = -INFINITY;
  int so = 0;
#pragma unroll 1
  for (int it = 0; it < NIT; ++it) {
    const bool lastk = it == NIT - 1;
    const int c = cnt(it), r = cnt(lastk ? 0 : it + 1);
    so = lastk ? 0 : so + c * 4096;
    const rsrc_t rr = lastk ? rv : rk;
#define TR_STEP(HALF, CTRL, BIT) _Pragma("unroll") for (int hh = 0; hh < (HALF); ++hh) { \
      const float lo_ = xs[hh], hi_ = xs[hh + (HALF)]; const float t1_ = lo_ + dpp<CTRL>(lo_), t2_ = hi_ + dpp<CTRL>(hi_); xs[hh] = (cc & (BIT)) ? t2_ : t1_; }
#define K_GROUP(J0, N) float xs[N]; \
      _Pragma("unroll") for (int t = 0; t < (N); ++t) { \
        const int j = (J0) + t; \
        float d = 0; \
        if (j < WIN) { \
          if (j < ZMIN || j < c) { _Pragma("unroll") for (int jj = 0; jj < 4; ++jj) d = dot2(a[j < WIN ? j : 0][jj], qp[jj], d); } \
          asm volatile("" : "+v"(d)); \
          __builtin_amdgcn_sched_barrier(0); \
          if (j < ZMIN || j < r) a[j < WIN ? j : 0] = __builtin_amdgcn_raw_buffer_load_b128(rr, voff, so + j * 4096, AUXKV); \
          __builtin_amdgcn_sched_barrier(0); \
        } \
        xs[t] = d; \
      }
#pragma unroll
    for (int g = 0; g < 2; ++g) {
      K_GROUP(g * 16, 16)
      TR_STEP(8, XOR8, 8) TR_STEP(4, XOR7, 4) TR_STEP(2, XOR2, 2) TR_STEP(1, XOR1, 1)
      const int j = g * 16 + cc;
      const float sc = j < c ? rbf(rbf(xs[0]) * 0.08838834764831845f) : -INFINITY;
      mx = fmaxf(mx, sc);
      sp[it * SPW + j] = sc;
    }
    if (SPW > 32) {
      K_GROUP(32, 4)
#pragma unroll
      for (int t = 0; t < 4; ++t) { xs[t] += dpp<XOR8>(xs[t]); xs[t] += dpp<XOR7>(xs[t]); }
      TR_STEP(2, XOR2, 2) TR_STEP(1, XOR1, 1)
      const int j = 32 + (cc & 3);
      const float sc = j < c ? rbf(rbf(xs[0]) * 0.08838834764831845f) : -INFINITY;
      mx = fmaxf(mx, sc);
      if (cc < 4) sp[it * SPW + j] = sc;
    }
#undef K_GROUP
#undef TR_STEP
  }
  ST(5);
  mx = wmax(mx);
  float l = 0;
#pragma unroll
  for (int g = 0; g < NLMAX / 16; ++g) { const float p = __expf(sp[g * 16 + cc] - mx); l += p; sp[g * 16 + cc] = p; }
  l = wsum(l);

  if (isW) {
    const int m2 = 10 * xc + slot - 20, cq = lane & 3;
    const v4u* wl = (const v4u*)(smem + L_W + 65536 + w * 10240) + lane;
    float acc[8] = {0, 0, 0, 0, 0, 0, 0, 0};
#pragma unroll
    for (int i = 0; i < 5; ++i) {
      const P8 p = mkpairs(wl[(2 * i) * 64], wl[(2 * i + 1) * 64]);
#pragma unroll
      for (int j = 0; j < 8; ++j) acc[j] = dot2(p.p[j], sw[i], acc[j]);
    }
    float r = red8(acc, lane);
    r += dpp<SHL4>(r);
    float* red = (float*)(smem + L_RED);
    if ((lane & 4) == 0) red[w * 32 + cq * 8 + red8col(lane)] = r;
    bar();
    if (w == 0) {
      const int g = lane & 7, rep = lane >> 3;
      const f32x4 a0 = *(const f32x4*)(red + 4 * g), a1 = *(const f32x4*)(red + 32 + 4 * g), a2 = *(const f32x4*)(red + 64 + 4 * g), a3 = *(const f32x4*)(red + 96 + 4 * g);
      const f32x4 s = (a0 + a1) + (a2 + a3);
      if (rep < RM) stg(rw, OFF_M + rep * 10240 + (8 * m2 + g) * 16, pack2(s[0], s[1]), pack2(s[2], s[3]), tag);
    }
    bar();
  }

  if (CODEPF2 < 32 && slot >= CODEPF2) code_pf();
  float acc[8] = {0, 0, 0, 0, 0, 0, 0, 0};
#define V_SLOTS(LOAD) _Pragma("unroll") for (int g = 0; g < (WIN + 3) / 4; ++g) { \
      const f32x4 p4 = *(const f32x4*)&sp[it * SPW + g * 4]; \
      _Pragma("unroll") for (int t = 0; t < 4; ++t) { \
        const int j = g * 4 + t; \
        if (j < WIN) { \
          const float pp = p4[t]; \
          if (j < ZMIN || j < c) { \
            _Pragma("unroll") for (int jj = 0; jj < 4; ++jj) { const unsigned wv = a[j < WIN ? j : 0][jj]; acc[2 * jj] += pp * lo16(wv); acc[2 * jj + 1] += pp * hi16(wv); } \
          } \
          asm volatile("" : "+v"(acc[0]), "+v"(acc[1]), "+v"(acc[2]), "+v"(acc[3]), "+v"(acc[4]), "+v"(acc[5]), "+v"(acc[6]), "+v"(acc[7])); \
          __builtin_amdgcn_sched_barrier(0); \
          if (LOAD && (j < ZMIN || j < r)) a[j < WIN ? j : 0] = __builtin_amdgcn_raw_buffer_load_b128(rv, voff, so + j * 4096, AUXKV); \
          __builtin_amdgcn_sched_barrier(0); \
        } \
      } \
    }
  so = 0;
#pragma unroll 1
  for (int it = 0; it < NIT - 1; ++it) {
    const int c = cnt(it), r = cnt(it + 1);
    so += c * 4096;
    V_SLOTS(true)
  }
  {
    const int it = NIT - 1, c = cnt(it), r = 0;
    V_SLOTS(false)
  }
#undef V_SLOTS
  ST(6);
  {
    float* sm = (float*)(smem + L_RED);
#pragma unroll
    for (int j = 0; j < 8; ++j) { acc[j] = tr16(acc[j], acc[j]); acc[j] = tr32(acc[j], acc[j]); }
    if (r4 == 0) { *(f32x4*)(sm + w * 132 + 4 + 8 * cc) = f32x4{acc[0], acc[1], acc[2], acc[3]}; *(f32x4*)(sm + w * 132 + 8 + 8 * cc) = f32x4{acc[4], acc[5], acc[6], acc[7]}; }
    if (lane == 0) { sm[w * 132] = mx; sm[w * 132 + 1] = l; }
    bar();
    if (w < 2) {
      const float m0 = sm[0], m1 = sm[132], m2 = sm[264], m3 = sm[396];
      const float M = fmaxf(fmaxf(m0, m1), fmaxf(m2, m3));
      const float e0 = __expf(m0 - M), e1 = __expf(m1 - M), e2 = __expf(m2 - M), e3 = __expf(m3 - M);
      const int po = OFF_P + (head * 8 + xc) * PSTR;
      if (w == 0) {
        const float* s0 = sm + 4 + 2 * lane;
        const float o0 = e0 * s0[0] + e1 * s0[132] + e2 * s0[264] + e3 * s0[396], o1 = e0 * s0[1] + e1 * s0[133] + e2 * s0[265] + e3 * s0[397];
        stg(rw, po + lane * 16, asu(o0), asu(o1), tag);
      } else if (lane == 0) {
        stg(rw, po + 64 * 16, asu(M), asu(e0 * sm[1] + e1 * sm[133] + e2 * sm[265] + e3 * sm[397]), tag);
      }
    }
    bar();
  }
  ST(7);

  if (isW) {
    const int C = slot - 20, hh = 4 * xc + w, cq = lane & 7;
    const v4u* wl = (const v4u*)(smem + L_W + w * 16384) + lane;
    unsigned pr[64];
#pragma unroll
    for (int i = 0; i < 8; ++i) {
      const P8 p = mkpairs(wl[(2 * i) * 64], wl[(2 * i + 1) * 64]);
#pragma unroll
      for (int j = 0; j < 8; ++j) pr[8 * i + j] = p.p[j];
    }
    const int po = OFF_P + hh * 8 * PSTR;
    v4u g[8], ml;
    bool have[9] = {false, false, false, false, false, false, false, false, false};
#pragma nounroll
    for (int spin = 0; spin < SPIN_CAP; ++spin) {
      fence_c();
#pragma unroll
      for (int s = 0; s < 8; ++s) if (!have[s]) g[s] = ldg(rw, po + s * PSTR + lane * 16);
      if (!have[8]) ml = ldg(rw, po + (lane & 7) * PSTR + 64 * 16);
      bool all = true;
#pragma unroll
      for (int s = 0; s < 8; ++s) { have[s] = __all(okg(g[s], tag)); all &= have[s]; }
      have[8] = __all(okg(ml, tag)); all &= have[8];
      if (all) break;
      __builtin_amdgcn_s_sleep(1);
    }
    ST(8);
    const unsigned mu = ml[0], lu = ml[2];
    const float ms = asf(mu);
    float M = fmaxf(ms, dpp<XOR1>(ms)); M = fmaxf(M, dpp<XOR2>(M)); M = fmaxf(M, dpp<XOR7>(M));
    const float e = __expf(ms - M);
    float L = e * asf(lu); L += dpp<XOR1>(L); L += dpp<XOR2>(L); L += dpp<XOR7>(L);
    const float f = e * __builtin_amdgcn_rcpf(L);
    float o0 = 0, o1 = 0;
#pragma unroll
    for (int s = 0; s < 8; ++s) {
      const float fs = asf(__builtin_amdgcn_readlane(asu(f), s));
      const unsigned u0 = g[s][0], u1 = g[s][2];
      o0 += fs * asf(u0); o1 += fs * asf(u1);
    }
    unsigned* hw = (unsigned*)(smem + L_HW) + w * 192;
    hw[lane] = pack2(o0, o1);
    fence_c();
    const v4u h0 = *(const v4u*)(hw + 8 * (lane >> 3)), h1 = *(const v4u*)(hw + 8 * (lane >> 3) + 4);
    float y[8] = {0, 0, 0, 0, 0, 0, 0, 0};
#pragma unroll
    for (int i = 0; i < 8; ++i) {
      const unsigned hv = i < 4 ? h0[i & 3] : h1[i & 3];
#pragma unroll
      for (int j = 0; j < 8; ++j) y[j] = dot2(pr[8 * i + j], hv, y[j]);
    }
    const float r = red8(y, lane);
    float* red = (float*)(smem + L_RED);
    red[w * 64 + cq * 8 + red8col(lane)] = r;
    bar();
    if (w == 0 && lane < 32) {
      const float* rp = red + 2 * lane;
      const float y0 = (rp[0] + rp[64]) + (rp[128] + rp[192]), y1 = (rp[1] + rp[65]) + (rp[129] + rp[193]);
      stg(rw, OFF_Y + (xc * 320 + 32 * C + lane) * 16, asu(y0), asu(y1), tag);
    }
    ST(9);
  } else if (isF) {
    const int fj = 7 * xc + slot - 13, wh = w & 1, rho = lane >> 2, cq = lane & 3;
    unsigned pr[80], pd[64];
    {
      const v4u* wl = (const v4u*)(smem + L_W + w * 20480) + lane;
#pragma unroll
      for (int i = 0; i < 10; ++i) {
        const P8 p = mkpairs(wl[(2 * i) * 64], wl[(2 * i + 1) * 64]);
#pragma unroll
        for (int j = 0; j < 8; ++j) pr[8 * i + j] = p.p[j];
      }
      const int tc = tid < 160 ? tid : 0;
      const v2u* dl = (const v2u*)(smem + L_W + 81920) + tc;
#pragma unroll
      for (int i = 0; i < 16; ++i) {
        const v2u r0 = dl[(2 * i) * 160], r1 = dl[(2 * i + 1) * 160];
        const unsigned a0 = r0[0], a1 = r0[1], b0 = r1[0], b1 = r1[1];
        pd[4 * i] = plo(a0, b0); pd[4 * i + 1] = phi(a0, b0); pd[4 * i + 2] = plo(a1, b1); pd[4 * i + 3] = phi(a1, b1);
      }
    }
    float* x1s = (float*)(smem + L_X1); float* cbs = (float*)(smem + L_CB); float* sbs = (float*)(smem + L_SB);
    if (tid < 160) {
      const int mo = OFF_M + (xc % RM) * 10240 + tid * 16;
      v4u ga, sb, cb;
#pragma nounroll
      for (int spin = 0; spin < SPIN_CAP; ++spin) {
        fence_c();
        ga = ldg(rw, mo); sb = ldg(rw, mo + 2560); cb = ldg(rw, mo + 5120);
        if (__all(okg(ga, tag) && okg(sb, tag) && okg(cb, tag))) break;
        __builtin_amdgcn_s_sleep(1);
      }
      const v2u xw = *(const v2u*)(x + 4 * tid);
      { const unsigned c0 = cb[0], c1 = cb[2], s0 = sb[0], s1 = sb[2];
        *(f32x4*)(cbs + 4 * tid) = f32x4{rbf(1.f + lo16(c0)), rbf(1.f + hi16(c0)), rbf(1.f + lo16(c1)), rbf(1.f + hi16(c1))};
        *(f32x4*)(sbs + 4 * tid) = f32x4{lo16(s0), hi16(s0), lo16(s1), hi16(s1)}; }
      v4u g[16];
      bool have[8] = {false, false, false, false, false, false, false, false};
#pragma nounroll
      for (int spin = 0; spin < SPIN_CAP; ++spin) {
        fence_c();
#pragma unroll
        for (int G = 0; G < 8; ++G) if (!have[G]) { g[2 * G] = ldg(rw, OFF_Y + (G * 320 + 2 * tid) * 16); g[2 * G + 1] = ldg(rw, OFF_Y + (G * 320 + 2 * tid + 1) * 16); }
        bool all = true;
#pragma unroll
        for (int G = 0; G < 8; ++G) { have[G] = __all(okg(g[2 * G], tag) && okg(g[2 * G + 1], tag)); all &= have[G]; }
        if (all) break;
        __builtin_amdgcn_s_sleep(1);
      }
      float y[4] = {0, 0, 0, 0};
#pragma unroll
      for (int G = 0; G < 8; ++G) { const unsigned u0 = g[2 * G][0], u1 = g[2 * G][2], u2 = g[2 * G + 1][0], u3 = g[2 * G + 1][2]; y[0] += asf(u0); y[1] += asf(u1); y[2] += asf(u2); y[3] += asf(u3); }
      const unsigned g0 = ga[0], g1 = ga[2], x0 = xw[0], x1w = xw[1];
      *(f32x4*)(x1s + 4 * tid) = f32x4{rbf(lo16(x0) + rbf(lo16(g0) * rbf(y[0]))), rbf(hi16(x0) + rbf(hi16(g0) * rbf(y[1]))),
                                       rbf(lo16(x1w) + rbf(lo16(g1) * rbf(y[2]))), rbf(hi16(x1w) + rbf(hi16(g1) * rbf(y[3])))};
    }
    ST(8);
    bar();
    ST(11);
    float xa[5], xb[5];
#pragma unroll
    for (int i = 0; i < 5; ++i) { xa[i] = x1s[5 * lane + i]; xb[i] = x1s[320 + 5 * lane + i]; }
    float s = 0;
#pragma unroll
    for (int i = 0; i < 5; ++i) s += xa[i] + xb[i];
    const float mean = wsum(s) * (1.f / D);
    float v = 0;
#pragma unroll
    for (int i = 0; i < 5; ++i) { const float d0 = xa[i] - mean, d1 = xb[i] - mean; v += d0 * d0 + d1 * d1; }
    const float var = wsum(v) * (1.f / D);
    const float rs = __builtin_amdgcn_rcpf(rbf(__builtin_sqrtf(rbf(rbf(var) + 1e-5f)))), mb = rbf(mean);
    STD(12, rs);
    u16* hs = (u16*)((unsigned*)(smem + L_HW) + w * 192);
#pragma unroll
    for (int i = 0; i < 5; ++i) {
      const int col = 320 * wh + 5 * lane + i;
      const float xx = wh ? xb[i] : xa[i];
      hs[5 * lane + i] = (u16)pack2(rbf(rbf(rbf(xx - mb) * rs) * cbs[col]) + sbs[col], 0.f);
    }
    fence_c();
    const v2u* hp = (const v2u*)((const unsigned*)hs + 10 * rho);
    float ga8[8] = {0, 0, 0, 0, 0, 0, 0, 0};
#pragma unroll
    for (int i = 0; i < 5; ++i) {
      const v2u hv = hp[i];
      const unsigned h0 = hv[0], h1 = hv[1];
#pragma unroll
      for (int j = 0; j < 8; ++j) ga8[j] = dot2(pr[16 * i + j], h0, ga8[j]);
#pragma unroll
      for (int j = 0; j < 8; ++j) ga8[j] = dot2(pr[16 * i + 8 + j], h1, ga8[j]);
    }
    STD(13, ga8[7]);
    float r = red8(ga8, lane);
    r += dpp<SHL4>(r);
    float* red = (float*)(smem + L_RED);
    if ((lane & 4) == 0) red[w * 32 + cq * 8 + red8col(lane)] = r;
    bar();
    ST(14);
    const int col = lane & 31;
    const float gg = rbf(red[col] + red[32 + col]), uu = rbf(red[64 + col] + red[96 + col]);
    const float av = rbf(rbf(silu(gg)) * uu);
    const unsigned aw = (asu(av) >> 16) | (asu(dpp<XOR1>(av)) & 0xffff0000u);
    float z[4] = {0, 0, 0, 0};
#pragma unroll
    for (int i = 0; i < 16; ++i) {
      const unsigned as = __builtin_amdgcn_readlane(aw, 2 * i);
#pragma unroll
      for (int j = 0; j < 4; ++j) z[j] = dot2(pd[4 * i + j], as, z[j]);
    }
    STD(15, z[3]);
    if (tid < 160) {
      stg(rw, OFF_Z + (fj * 320 + 2 * tid) * 16, asu(z[0]), asu(z[1]), tag);
      stg(rw, OFF_Z + (fj * 320 + 2 * tid + 1) * 16, asu(z[2]), asu(z[3]), tag);
    }
    ST(9);
  } else if (isM1) {
    const int fi = 5 * xc + slot, g8 = lane & 7;
    float x1a = 0, x1b = 0, gb0 = 0, gb1 = 0;
    if (w == 0) {
      const int mo = OFF_M + (xc % RM) * 10240 + (4 * fi + (g8 >> 1)) * 16;
      v4u ga, gb, yy;
#pragma nounroll
      for (int spin = 0; spin < SPIN_CAP; ++spin) {
        fence_c();
        ga = ldg(rw, mo); gb = ldg(rw, mo + 7680); yy = ldg(rw, OFF_Y + ((lane >> 3) * 320 + 8 * fi + g8) * 16);
        if (__all(okg(ga, tag) && okg(gb, tag) && okg(yy, tag))) break;
        __builtin_amdgcn_s_sleep(1);
      }
      const unsigned xw = ((const unsigned*)x)[8 * fi + g8];
      const unsigned gaw = (g8 & 1) ? ga[2] : ga[0], gbw = (g8 & 1) ? gb[2] : gb[0], u0 = yy[0], u1 = yy[2];
      float y0 = 0, y1 = 0;
#pragma unroll
      for (int G = 0; G < 8; ++G) { y0 += __shfl(asf(u0), G * 8 + g8); y1 += __shfl(asf(u1), G * 8 + g8); }
      x1a = rbf(lo16(xw) + rbf(lo16(gaw) * rbf(y0))); x1b = rbf(hi16(xw) + rbf(hi16(gaw) * rbf(y1)));
      gb0 = lo16(gbw); gb1 = hi16(gbw);
    }
    ST(8);
    const int p0 = tid >> 3;
    v4u z0, z1 = {0, 0, 0, 0};
    bool h0 = false, h1 = false;
#pragma nounroll
    for (int spin = 0; spin < SPIN_CAP; ++spin) {
      fence_c();
      if (!h0) z0 = ldg(rw, OFF_Z + (p0 * 320 + 8 * fi + g8) * 16);
      if (!h1 && p0 < 22) z1 = ldg(rw, OFF_Z + ((p0 + 32) * 320 + 8 * fi + g8) * 16);
      h0 = __all(okg(z0, tag)); h1 = __all(p0 >= 22 || okg(z1, tag));
      if (h0 && h1) break;
      __builtin_amdgcn_s_sleep(1);
    }
    ST(9);
    const unsigned u0 = z0[0], u1 = z0[2], u2 = z1[0], u3 = z1[2];
    float s0 = asf(u0) + asf(u2), s1 = asf(u1) + asf(u3);
    s0 += dpp<XOR8>(s0); s1 += dpp<XOR8>(s1);
    s0 = tr16(s0, s0); s1 = tr16(s1, s1); s0 = tr32(s0, s0); s1 = tr32(s1, s1);
    float* red = (float*)(smem + L_RED);
    if (lane < 8) { red[w * 16 + 2 * lane] = s0; red[w * 16 + 2 * lane + 1] = s1; }
    bar();
    if (w == 0 && lane < 8) {
      const float* rp = red + 2 * lane;
      const float y0 = (rp[0] + rp[16]) + (rp[32] + rp[48]), y1 = (rp[1] + rp[17]) + (rp[33] + rp[49]);
      ((unsigned*)out)[8 * fi + lane] = pack2(x1a + rbf(gb0 * rbf(y0)), x1b + rbf(gb1 * rbf(y1)));
      if (fi == 0 && lane == 0) { __builtin_amdgcn_raw_buffer_store_b32(tag, rw, 0, 0, 16); *(unsigned long long*)((char*)ws + (8 << 20)) = tstart; }
    }
    ST(10);
  }
  asm volatile(".Lmega_end:");
}

__global__ void __launch_bounds__(256, 1) post(const unsigned long long* p) { const unsigned long long tstart = *(const volatile unsigned long long*)p; for (int i_ = 0; i_ < 3000 && __builtin_amdgcn_s_memrealtime() < tstart + TFIX; ++i_) __builtin_amdgcn_s_sleep(1); }
extern "C" void kernel_launch(void* const* d_in, const int* in_sizes, int n_in, void* d_out, int out_size, void* d_ws, size_t ws_size,
                              hipStream_t stream) {
  hipFuncSetAttribute((const void*)mega, hipFuncAttributeMaxDynamicSharedMemorySize, LDS_BYTES);
  mega<<<256, 256, LDS_BYTES, stream>>>((const u16*)d_in[0], (const u16*)d_in[1], (const u16*)d_in[2], (const u16*)d_in[3], (const u16*)d_in[4],
                                        (const u16*)d_in[6], (const u16*)d_in[7], (const u16*)d_in[8], (const u16*)d_in[9], (const u16*)d_in[10],
                                        (u16*)d_out, (unsigned*)d_ws);
  post<<<1, 64, 0, stream>>>((const unsigned long long*)((char*)d_ws + (8 << 20)));
}
```

```cpp
#include <hip/hip_runtime.h>
#include <stdint.h>

typedef unsigned short u16;
typedef unsigned v4u __attribute__((ext_vector_type(4)));
typedef unsigned v2u __attribute__((ext_vector_type(2)));
typedef float f32x4 __attribute__((ext_vector_type(4)));
typedef __bf16 bf16x2 __attribute__((ext_vector_type(2)));
typedef __amdgpu_buffer_rsrc_t rsrc_t;
#define LAS __attribute__((address_space(3)))
#define DI __device__ __forceinline__

#define AUXKV 2
#define BULK_DELAY 16
#define PREQ 0
#define NBPOLL 3
#define AUXQ 2
#define NTFULL 1
#define NTHALF 0
#define CODEPF 32
#define CODEPF2 32
#define ARGS1 1
#define RH 4
#define RM 4
#define DELTAS 5, 6, 5, 5, 6, -4, -5, -5, -5, -5, -5, -5, -6, -2, -3, -3, -4, -3, -4, -4, 1, 3, 1, 1, 2, 3, 1, 1, 1, 2, 10, 10
#define PARITY 0
#define PWAIT 0
#define SPIN_CAP (1 << 16)

constexpr int D = 640, SK = 16384, F = 1728;
constexpr int OFF_H = 256;
constexpr int OFF_Q = OFF_H + RH * 5120;
constexpr int OFF_M = OFF_Q + 32 * 512;
constexpr int PSTR = 72 * 16;
constexpr int OFF_P = OFF_M + RM * 10240;
constexpr int OFF_Y = OFF_P + 256 * PSTR;
constexpr int OFF_Z = OFF_Y + 8 * 5120;
constexpr int WS_BYTES = OFF_Z + 54 * 5120;
constexpr int OFF_STAMP = 4 << 20;
constexpr int KDELTA[32] = {DELTAS};
constexpr unsigned long long kcount(int c) { unsigned long long r = 0; for (int k = 0; k < 8; ++k) r |= (unsigned long long)(64 + KDELTA[c + 4 * k]) << (8 * k); return r; }
constexpr unsigned long long kstart(int c) { unsigned long long r = 0; int s = 0; for (int k = 0; k < 8; ++k) { r |= (unsigned long long)(64 + s) << (8 * k); s += KDELTA[c + 4 * k]; } return r; }
constexpr bool kok() { for (int c = 0; c < 4; ++c) { int s = 0; for (int k = 0; k < 8; ++k) { s += KDELTA[c + 4 * k]; if (s < -60 || s > 60) return false; } if (s) return false; } return true; }
constexpr int kmin() { int m = 0; for (int i = 0; i < 32; ++i) m = KDELTA[i] < m ? KDELTA[i] : m; return 128 + m - PARITY; }
constexpr int kmax() { int m = 0; for (int i = 0; i < 32; ++i) m = KDELTA[i] > m ? KDELTA[i] : m; return 128 + m + PARITY; }
static_assert(kok(), "key split");
constexpr int NIT = 4, ZMIN = kmin() / NIT, WIN = (kmax() + NIT - 1) / NIT, SPW = WIN <= 32 ? 32 : 36, NLMAX = NIT * SPW;
static_assert(WIN <= 36 && ZMIN >= 20, "ring");
constexpr int L_SP = 0;
constexpr int L_RED = L_SP + 16 * NLMAX * 4;
constexpr int L_ML = L_RED + 4096;
constexpr int L_X1 = L_ML + 64;
constexpr int L_CB = L_X1 + 2560;
constexpr int L_SB = L_CB + 2560;
constexpr int L_HW = L_SB + 2560;
constexpr int L_W = L_HW + 4 * 768;
constexpr int L_DUMMY = L_W + 120 * 1024;
constexpr int LDS_BYTES = L_DUMMY + 1024;
static_assert(L_W % 16 == 0 && LDS_BYTES <= 160 * 1024, "lds");

DI float asf(unsigned u) { return __builtin_bit_cast(float, u); }
DI unsigned asu(float f) { return __builtin_bit_cast(unsigned, f); }
DI float lo16(unsigned w) { return asf(w << 16); }
DI float hi16(unsigned w) { return asf(w & 0xffff0000u); }
typedef float f32x2 __attribute__((ext_vector_type(2)));
DI unsigned pack2(float a, float b) { const f32x2 v = {a, b}; return __builtin_bit_cast(unsigned, __builtin_convertvector(v, bf16x2)); }
DI float rbf(float f) { return asf(pack2(0.f, f)); }
DI float dot2(unsigned k, unsigned q, float acc) {
  return __builtin_amdgcn_fdot2_f32_bf16(__builtin_bit_cast(bf16x2, k), __builtin_bit_cast(bf16x2, q), acc, false);
}
template <int CTRL> DI float dpp(float x) { return asf(__builtin_amdgcn_mov_dpp(asu(x), CTRL, 0xf, 0xf, true)); }
constexpr int XOR1 = 0xB1, XOR2 = 0x4E, XOR7 = 0x141, XOR8 = 0x128, ROR4 = 0x124, SHL4 = 0x104;
DI float tr32(float a, float b) { v2u r = __builtin_amdgcn_permlane32_swap(asu(a), asu(b), false, false); unsigned p = r[0], q = r[1]; return asf(p) + asf(q); }
DI float tr16(float a, float b) { v2u r = __builtin_amdgcn_permlane16_swap(asu(a), asu(b), false, false); unsigned p = r[0], q = r[1]; return asf(p) + asf(q); }
DI float tr8(float a, float b, bool hi) { const float t1 = a + dpp<XOR8>(a), t2 = b + dpp<XOR8>(b); return hi ? t2 : t1; }
DI float wsum(float v) {
  v += dpp<XOR1>(v); v += dpp<XOR2>(v); v += dpp<XOR7>(v); v += dpp<XOR8>(v); v = tr16(v, v); return tr32(v, v);
}
DI void wsum2(float& a, float& b) {
  a += dpp<XOR1>(a); b += dpp<XOR1>(b); a += dpp<XOR2>(a); b += dpp<XOR2>(b); a += dpp<XOR7>(a); b += dpp<XOR7>(b); a += dpp<XOR8>(a); b += dpp<XOR8>(b);
  a = tr16(a, a); b = tr16(b, b); a = tr32(a, a); b = tr32(b, b);
}
DI float mx16(float a) { v2u r = __builtin_amdgcn_permlane16_swap(asu(a), asu(a), false, false); unsigned p = r[0], q = r[1]; return fmaxf(asf(p), asf(q)); }
DI float mx32(float a) { v2u r = __builtin_amdgcn_permlane32_swap(asu(a), asu(a), false, false); unsigned p = r[0], q = r[1]; return fmaxf(asf(p), asf(q)); }
DI float wmax(float v) {
  v = fmaxf(v, dpp<XOR1>(v)); v = fmaxf(v, dpp<XOR2>(v)); v = fmaxf(v, dpp<XOR7>(v)); v = fmaxf(v, dpp<XOR8>(v)); return mx32(mx16(v));
}
DI float silu(float v) { return v * __builtin_amdgcn_rcpf(1.f + __expf(-v)); }
DI unsigned plo(unsigned a, unsigned b) { return __builtin_amdgcn_perm(b, a, 0x05040100u); }
DI unsigned phi(unsigned a, unsigned b) { return __builtin_amdgcn_perm(b, a, 0x07060302u); }
struct P8 { unsigned p[8]; };
DI P8 mkpairs(v4u a, v4u b) {
  P8 r;
#pragma unroll
  for (int i = 0; i < 4; ++i) { const unsigned x = a[i], y = b[i]; r.p[2 * i] = plo(x, y); r.p[2 * i + 1] = phi(x, y); }
  return r;
}
DI rsrc_t mkrsrc(const void* p, unsigned bytes) { return __builtin_amdgcn_make_buffer_rsrc((void*)p, 0, bytes, 0x00020000); }
DI v4u ldg(rsrc_t r, int off) { return __builtin_amdgcn_raw_buffer_load_b128(r, off, 0, 16); }
DI void stg(rsrc_t r, int off, unsigned a, unsigned b, unsigned tag) { v4u g = {a, tag, b, tag}; __builtin_amdgcn_raw_buffer_store_b128(g, r, off, 0, 16); }
DI bool okg(v4u g, unsigned tag) { const unsigned a = g[1], b = g[3]; return a == tag && b == tag; }
DI void fence_c() { asm volatile("" ::: "memory"); }
DI unsigned gbad(v4u g, unsigned tag) { const unsigned a = g[1], b = g[3]; return __builtin_amdgcn_bitop3_b32(a, b, tag, 0x7E); }
typedef unsigned long long u64;
DI u64 lanes(bool c) { return __builtin_amdgcn_ballot_w64(c); }
DI void pin(unsigned& v) { asm volatile("" : "+v"(v)); }
DI void pin(float& v) { asm volatile("" : "+v"(v)); }
DI void pins(float& v) { asm volatile("" : "+s"(v)); }
#define GROW(g, m, vo, so, imm) "s_mov_b64 exec, %[" #m "]\n\tbuffer_load_dwordx4 %[" #g "], %[" #vo "], %[rw], " so " offen offset:" #imm " sc1\n\t"
#define GHEAD "s_mov_b64 %[sv], exec\n\t"
#define GTAIL "s_mov_b64 exec, %[sv]\n\ts_waitcnt vmcnt(0)"
DI void grow1(v4u& g0, u64 m0, int vo, rsrc_t rw) {
  u64 sv;
  asm volatile(GHEAD GROW(g0, m0, vo, "0", 0) GTAIL : [g0] "+v"(g0), [sv] "=&s"(sv) : [m0] "s"(m0), [vo] "v"(vo), [rw] "s"(rw) : "memory");
}
DI void grow2(v4u& g0, v4u& g1, u64 m0, u64 m1, int v0, int v1, rsrc_t rw) {
  u64 sv;
  asm volatile(GHEAD GROW(g0, m0, v0, "0", 0) GROW(g1, m1, v1, "0", 0) GTAIL
               : [g0] "+v"(g0), [g1] "+v"(g1), [sv] "=&s"(sv) : [m0] "s"(m0), [m1] "s"(m1), [v0] "v"(v0), [v1] "v"(v1), [rw] "s"(rw) : "memory");
}
DI void grow3(v4u& g0, v4u& g1, v4u& g2, u64 m0, u64 m1, u64 m2, int v0, int v1, int v2, rsrc_t rw) {
  u64 sv;
  asm volatile(GHEAD GROW(g0, m0, v0, "0", 0) GROW(g1, m1, v1, "0", 0) GROW(g2, m2, v2, "0", 0) GTAIL
               : [g0] "+v"(g0), [g1] "+v"(g1), [g2] "+v"(g2), [sv] "=&s"(sv)
               : [m0] "s"(m0), [m1] "s"(m1), [m2] "s"(m2), [v0] "v"(v0), [v1] "v"(v1), [v2] "v"(v2), [rw] "s"(rw) : "memory");
}
DI void grow8(v4u (&g)[8], const u64 (&m)[8], int v0, int v1, rsrc_t rw) {
  u64 sv;
  asm volatile(GHEAD GROW(g0, m0, v0, "0", 0) GROW(g1, m1, v0, "0", 1152) GROW(g2, m2, v0, "0", 2304) GROW(g3, m3, v0, "0", 3456)
               GROW(g4, m4, v1, "0", 0) GROW(g5, m5, v1, "0", 1152) GROW(g6, m6, v1, "0", 2304) GROW(g7, m7, v1, "0", 3456) GTAIL
               : [g0] "+v"(g[0]), [g1] "+v"(g[1]), [g2] "+v"(g[2]), [g3] "+v"(g[3]), [g4] "+v"(g[4]), [g5] "+v"(g[5]), [g6] "+v"(g[6]), [g7] "+v"(g[7]), [sv] "=&s"(sv)
               : [m0] "s"(m[0]), [m1] "s"(m[1]), [m2] "s"(m[2]), [m3] "s"(m[3]), [m4] "s"(m[4]), [m5] "s"(m[5]), [m6] "s"(m[6]), [m7] "s"(m[7]),
                 [v0] "v"(v0), [v1] "v"(v1), [rw] "s"(rw) : "memory");
}
#define GPAIR(a, b, m, so) "s_mov_b64 exec, %[" #m "]\n\tbuffer_load_dwordx4 %[" #a "], %[vo], %[rw], " so " offen sc1\n\tbuffer_load_dwordx4 %[" #b "], %[vo], %[rw], " so " offen offset:2560 sc1\n\t"
DI void grow16(v4u (&g)[16], const u64 (&m)[8], int vo, rsrc_t rw) {
  u64 sv;
  const unsigned s1 = 5120, s2 = 10240, s3 = 15360, s4 = 20480, s5 = 25600, s6 = 30720, s7 = 35840;
  asm volatile(GHEAD GPAIR(g0, g1, m0, "0") GPAIR(g2, g3, m1, "%[s1]") GPAIR(g4, g5, m2, "%[s2]") GPAIR(g6, g7, m3, "%[s3]")
               GPAIR(g8, g9, m4, "%[s4]") GPAIR(g10, g11, m5, "%[s5]") GPAIR(g12, g13, m6, "%[s6]") GPAIR(g14, g15, m7, "%[s7]") GTAIL
               : [g0] "+v"(g[0]), [g1] "+v"(g[1]), [g2] "+v"(g[2]), [g3] "+v"(g[3]), [g4] "+v"(g[4]), [g5] "+v"(g[5]), [g6] "+v"(g[6]), [g7] "+v"(g[7]),
                 [g8] "+v"(g[8]), [g9] "+v"(g[9]), [g10] "+v"(g[10]), [g11] "+v"(g[11]), [g12] "+v"(g[12]), [g13] "+v"(g[13]), [g14] "+v"(g[14]), [g15] "+v"(g[15]), [sv] "=&s"(sv)
               : [m0] "s"(m[0]), [m1] "s"(m[1]), [m2] "s"(m[2]), [m3] "s"(m[3]), [m4] "s"(m[4]), [m5] "s"(m[5]), [m6] "s"(m[6]), [m7] "s"(m[7]),
                 [s1] "s"(s1), [s2] "s"(s2), [s3] "s"(s3), [s4] "s"(s4), [s5] "s"(s5), [s6] "s"(s6), [s7] "s"(s7), [vo] "v"(vo), [rw] "s"(rw) : "memory");
}
DI int plane(int j) { return ((j & 1) * 160 + (j >> 1)) * 16; }
DI void dma16(rsrc_t r, unsigned voff, unsigned soff, unsigned lds) {
  unsigned keep;
  asm volatile("s_mov_b32 %0, m0\n\ts_mov_b32 m0, %1\n\ts_nop 0\n\tbuffer_load_dwordx4 %2, %3, %4 offen lds\n\ts_mov_b32 m0, %0"
               : "=&s"(keep) : "s"(lds), "v"(voff), "s"(r), "s"(soff) : "memory");
}
#define DMA_BODY(NT) ".rept %c[n]\n\ts_nop 0\n\tbuffer_load_dwordx4 %[vo], %[r], %[so] offen" NT " lds\n\ts_add_u32 m0, m0, 0x400\n\ts_add_u32 %[so], %[so], %[st]\n\t.endr\n\t"
template <int N, bool NT> DI void dma_batch(rsrc_t r, unsigned voff, unsigned soff, unsigned stride, unsigned lds) {
  unsigned keep;
  if (NT) asm volatile("s_mov_b32 %[k], m0\n\ts_mov_b32 m0, %[l]\n\t" DMA_BODY(" nt") "s_mov_b32 m0, %[k]"
               : [k] "=&s"(keep), [so] "+s"(soff) : [vo] "v"(voff), [r] "s"(r), [st] "s"(stride), [l] "s"(lds), [n] "n"(N) : "memory", "scc");
  else asm volatile("s_mov_b32 %[k], m0\n\ts_mov_b32 m0, %[l]\n\t" DMA_BODY("") "s_mov_b32 m0, %[k]"
               : [k] "=&s"(keep), [so] "+s"(soff) : [vo] "v"(voff), [r] "s"(r), [st] "s"(stride), [l] "s"(lds), [n] "n"(N) : "memory", "scc");
}
#define POLL2 "buffer_load_dwordx4 %[g0], %[go], %[rw], 0 offen sc1\n\tbuffer_load_dwordx4 %[g1], %[go], %[rw], 0 offen offset:16 sc1\n\t"
template <int N, bool NT> DI void poll_dma(v4u& g0, v4u& g1, int goff, rsrc_t rw, rsrc_t r, unsigned voff, unsigned soff, unsigned stride, unsigned lds) {
  unsigned keep;
  if (NT) asm volatile(POLL2 "s_mov_b32 %[k], m0\n\ts_mov_b32 m0, %[l]\n\t" DMA_BODY(" nt") "s_mov_b32 m0, %[k]\n\ts_waitcnt vmcnt(%c[n])"
               : [g0] "=&v"(g0), [g1] "=&v"(g1), [k] "=&s"(keep), [so] "+s"(soff)
               : [go] "v"(goff), [rw] "s"(rw), [vo] "v"(voff), [r] "s"(r), [st] "s"(stride), [l] "s"(lds), [n] "n"(N) : "memory", "scc");
  else asm volatile(POLL2 "s_mov_b32 %[k], m0\n\ts_mov_b32 m0, %[l]\n\t" DMA_BODY("") "s_mov_b32 m0, %[k]\n\ts_waitcnt vmcnt(%c[n])"
               : [g0] "=&v"(g0), [g1] "=&v"(g1), [k] "=&s"(keep), [so] "+s"(soff)
               : [go] "v"(goff), [rw] "s"(rw), [vo] "v"(voff), [r] "s"(r), [st] "s"(stride), [l] "s"(lds), [n] "n"(N) : "memory", "scc");
}
DI void bar() { asm volatile("s_waitcnt lgkmcnt(0)\n\ts_barrier" ::: "memory"); }
DI float red8(const float (&a)[8], int lane) {
  const float b0 = tr32(a[0], a[4]), b1 = tr32(a[1], a[5]), b2 = tr32(a[2], a[6]), b3 = tr32(a[3], a[7]);
  const float c0 = tr16(b0, b2), c1 = tr16(b1, b3);
  return tr8(c0, c1, lane & 8);
}
DI int red8col(int lane) { return ((lane >> 5) & 1) * 4 + ((lane >> 4) & 1) * 2 + ((lane >> 3) & 1); }

#define STAMPMASK 0xffff
#define ST(i) do { } while (0)
#define STD(i, x) do { } while (0)

__global__ void __launch_bounds__(256, 1)
mega(const u16* __restrict__ x, const u16* __restrict__ K, const u16* __restrict__ V, const u16* __restrict__ wq, const u16* __restrict__ wo,
     const u16* __restrict__ wg, const u16* __restrict__ wu, const u16* __restrict__ wd, const u16* __restrict__ c, const u16* __restrict__ wmod,
     u16* __restrict__ out, unsigned* ws) {
  extern __shared__ __attribute__((aligned(16))) char smem[];
  const int tid = threadIdx.x, lane = tid & 63, w = __builtin_amdgcn_readfirstlane(tid >> 6), b = blockIdx.x;
  const int xc = b & 7, slot = b >> 3, head = (slot - 4 * xc) & 31;
  ST(0);
  const unsigned ldsb = __builtin_amdgcn_readfirstlane((unsigned)(size_t)(LAS char*)smem);
  rsrc_t rc;
  {
    unsigned long long pc; unsigned len;
    asm volatile("s_getpc_b64 %0\n.Lmega_pf0:\n\ts_mov_b32 %1, .Lmega_end-.Lmega_pf0" : "=s"(pc), "=s"(len));
    rc = mkrsrc((const void*)pc, len);
  }
  auto code_pf = [&]() {
    unsigned keep;
    asm volatile("s_mov_b32 %0, m0\n\ts_mov_b32 m0, %1\n\ts_nop 0\n\tbuffer_load_dword %2, %3, 0 offen lds\n\ts_mov_b32 m0, %0"
                 : "=&s"(keep) : "s"(ldsb + L_DUMMY), "v"(tid * 128), "s"(rc) : "memory");
  };
  if (b < CODEPF) code_pf();
  if (ARGS1) asm volatile("" :: "s"(x), "s"(K), "s"(V), "s"(wq), "s"(wo), "s"(wg), "s"(wu), "s"(wd), "s"(c), "s"(wmod), "s"(out), "s"(ws));
  const rsrc_t rw = mkrsrc(ws, WS_BYTES);
  const unsigned ew = __builtin_amdgcn_raw_buffer_load_b32(rw, 0, 0, 16);
  const bool isM1 = slot < 5, isQ = slot >= 5 && slot < 13, isF = slot >= 13 && slot < 20 && 7 * xc + slot - 13 < 54, isW = slot >= 20 && slot < 30;
  unsigned tag = 0;
  int nch, ch0;
  {
    const int cl = slot & 3, k = slot >> 2, e = (xc & 1) ? PARITY : -PARITY;
    const unsigned long long tc = cl == 0 ? kcount(0) : cl == 1 ? kcount(1) : cl == 2 ? kcount(2) : kcount(3);
    const unsigned long long ts = cl == 0 ? kstart(0) : cl == 1 ? kstart(1) : cl == 2 ? kstart(2) : kstart(3);
    nch = 64 + (int)((tc >> (8 * k)) & 255) + e;
    ch0 = 128 * k - 64 + (int)((ts >> (8 * k)) & 255) - ((k & 1) ? e : 0);
  }
  const int nkeys = 16 * nch, key0 = 16 * ch0;
  auto cnt = [&](int it) { return (nch + NIT - 1 - it) / NIT; };
  const rsrc_t rk = mkrsrc(K + ((size_t)head * SK + key0) * 128, nkeys * 256);
  const rsrc_t rv = mkrsrc(V + ((size_t)head * SK + key0) * 128, nkeys * 256);
  const int voff = lane * 16 + w * 1024;
  v4u a[WIN];
  unsigned sw[5];

  if (isM1) {
    const int mi = 5 * xc + slot, t = mi >= 20, g32 = mi - 20 * t, rho = lane >> 2, cq = lane & 3;
    const rsrc_t rm = mkrsrc(wmod, D * 6 * D * 2);
    const unsigned* cw = (const unsigned*)c + 80 * w + 5 * rho;
    unsigned cv[5];
#pragma unroll
    for (int i = 0; i < 5; ++i) cv[i] = cw[i];
    const int vo = (160 * w + 10 * rho) * 7680 + (t * D + 32 * g32 + 8 * cq) * 2;
    v4u wv[10];
#pragma unroll
    for (int i = 0; i < 10; ++i) wv[i] = __builtin_amdgcn_raw_buffer_load_b128(rm, vo, i * 7680, 0);
    __builtin_amdgcn_sched_barrier(0);
    ST(11);
#pragma unroll
    for (int i = 0; i < 5; ++i) sw[i] = pack2(silu(lo16(cv[i])), silu(hi16(cv[i])));
    STD(12, sw[4]);
    float acc[8] = {0, 0, 0, 0, 0, 0, 0, 0};
#pragma unroll
    for (int i = 0; i < 5; ++i) {
      const P8 p = mkpairs(wv[2 * i], wv[2 * i + 1]);
#pragma unroll
      for (int j = 0; j < 8; ++j) acc[j] = dot2(p.p[j], sw[i], acc[j]);
    }
    STD(14, acc[7]);
    float r = red8(acc, lane);
    r += dpp<SHL4>(r);
    float* red = (float*)(smem + L_RED);
    if ((lane & 4) == 0) red[w * 32 + cq * 8 + red8col(lane)] = r;
    bar();
    if (w == 0) {
      const int g = lane & 7, rep = lane >> 3;
      const f32x4 a0 = *(const f32x4*)(red + 4 * g), a1 = *(const f32x4*)(red + 32 + 4 * g), a2 = *(const f32x4*)(red + 64 + 4 * g), a3 = *(const f32x4*)(red + 96 + 4 * g);
      f32x4 s = (a0 + a1) + (a2 + a3);
      if (t) { s[0] = 1.f + rbf(s[0]); s[1] = 1.f + rbf(s[1]); s[2] = 1.f + rbf(s[2]); s[3] = 1.f + rbf(s[3]); }
      tag = __builtin_amdgcn_readfirstlane(ew) + 1;
      if (rep < RH) stg(rw, OFF_H + rep * 5120 + (t * 160 + 8 * g32 + g) * 16, pack2(s[0], s[1]), pack2(s[2], s[3]), tag);
    }
    ST(1);
  } else if (isQ) {
    const int qi = 8 * xc + slot - 5, rho = lane >> 3, cq = lane & 7, lc = lane < 40 ? lane : 39;
    const rsrc_t rq = mkrsrc(wq, D * 4096 * 2);
    unsigned xv[5];
#pragma unroll
    for (int i = 0; i < 5; ++i) xv[i] = ((const unsigned*)x)[5 * lane + i];
    const v2u xo = *(const v2u*)(x + 160 * w + 4 * lc);
    const int vo = (160 * w + 20 * rho) * 8192 + (64 * qi + 8 * cq) * 2;
    v4u wv[20];
#pragma unroll
    for (int i = 0; i < 20; ++i) wv[i] = __builtin_amdgcn_raw_buffer_load_b128(rq, vo, i * 8192, AUXQ);
    __builtin_amdgcn_sched_barrier(0);
#pragma unroll
    for (int i = 0; i < PREQ; ++i) a[i] = __builtin_amdgcn_raw_buffer_load_b128(rk, voff, i * 4096, AUXKV);
    __builtin_amdgcn_sched_barrier(0);
    float s = 0;
#pragma unroll
    for (int i = 0; i < 5; ++i) s += lo16(xv[i]) + hi16(xv[i]);
    const float mean = wsum(s) * (1.f / D);
    float v = 0;
#pragma unroll
    for (int i = 0; i < 5; ++i) { const float d0 = lo16(xv[i]) - mean, d1 = hi16(xv[i]) - mean; v += d0 * d0 + d1 * d1; }
    const float var = wsum(v) * (1.f / D);
    const float rs = __builtin_amdgcn_rcpf(rbf(__builtin_amdgcn_sqrtf(rbf(rbf(var) + 1e-5f)))), mb = rbf(mean);
    const unsigned xo0 = xo[0], xo1 = xo[1];
    float n0 = rbf(rbf(lo16(xo0) - mb) * rs), n1 = rbf(rbf(hi16(xo0) - mb) * rs), n2 = rbf(rbf(lo16(xo1) - mb) * rs), n3 = rbf(rbf(hi16(xo1) - mb) * rs);
    pin(n0); pin(n1); pin(n2); pin(n3);
    unsigned pr[80];
#pragma unroll
    for (int i = 0; i < 10; ++i) {
      const P8 p = mkpairs(wv[2 * i], wv[2 * i + 1]);
#pragma unroll
      for (int j = 0; j < 8; ++j) pr[8 * i + j] = p.p[j];
    }
#pragma unroll
    for (int j = 0; j < 80; ++j) pin(pr[j]);
    tag = __builtin_amdgcn_readfirstlane(ew) + 1;
    ST(1);
    v4u gs = {0, 0, 0, 0}, gc = {0, 0, 0, 0};
    const int go = OFF_H + (xc % RH) * 5120 + (40 * w + lc) * 16;
    {
      u64 m0 = lanes(true), m1 = m0;
#pragma nounroll
      for (int spin = 0; spin < SPIN_CAP; ++spin) {
        grow2(gs, gc, m0, m1, go, go + 2560, rw);
        const unsigned b0 = gbad(gs, tag), b1 = gbad(gc, tag);
        if (lanes((b0 | b1) != 0) == 0) break;
        m0 = lanes(b0 != 0); m1 = lanes(b1 != 0);
      }
    }
    ST(2);
    unsigned* hw = (unsigned*)(smem + L_HW) + w * 192;
    {
      const unsigned s0 = gs[0], s1 = gs[2], c0 = gc[0], c1 = gc[2];
      const v2u hh = {pack2(rbf(n0 * lo16(c0)) + lo16(s0), rbf(n1 * hi16(c0)) + hi16(s0)), pack2(rbf(n2 * lo16(c1)) + lo16(s1), rbf(n3 * hi16(c1)) + hi16(s1))};
      *(v2u*)(hw + 2 * lc) = hh;
    }
    fence_c();
    float acc[8] = {0, 0, 0, 0, 0, 0, 0, 0};
#pragma unroll
    for (int i = 0; i < 5; ++i) {
      const v2u hv = *(const v2u*)(hw + 10 * rho + 2 * i);
      const unsigned h0 = hv[0], h1 = hv[1];
#pragma unroll
      for (int j = 0; j < 8; ++j) acc[j] = dot2(pr[16 * i + j], h0, acc[j]);
#pragma unroll
      for (int j = 0; j < 8; ++j) acc[j] = dot2(pr[16 * i + 8 + j], h1, acc[j]);
    }
    const float r = red8(acc, lane);
    float* red = (float*)(smem + L_RED);
    red[w * 64 + cq * 8 + red8col(lane)] = r;
    bar();
    if (w == 0 && lane < 16) {
      const f32x4 a0 = *(const f32x4*)(red + 4 * lane), a1 = *(const f32x4*)(red + 64 + 4 * lane), a2 = *(const f32x4*)(red + 128 + 4 * lane), a3 = *(const f32x4*)(red + 192 + 4 * lane);
      const f32x4 s = (a0 + a1) + (a2 + a3);
      stg(rw, OFF_Q + (16 * qi + lane) * 16, pack2(s[0], s[1]), pack2(s[2], s[3]), tag);
    }
    ST(3);
  }
  __builtin_amdgcn_sched_barrier(0);

  unsigned cvm[5];
  if (isW) {
#pragma unroll
    for (int i = 0; i < 5; ++i) cvm[i] = ((const unsigned*)c)[80 * w + 5 * (lane >> 2) + i];
  }
  __builtin_amdgcn_sched_barrier(0);
  if (BULK_DELAY > 0 && !isM1 && !isQ) __builtin_amdgcn_s_sleep(BULK_DELAY);
  if (PREQ > 0 && !isQ) {
#pragma unroll
    for (int i = 0; i < PREQ; ++i) a[i] = __builtin_amdgcn_raw_buffer_load_b128(rk, voff, i * 4096, AUXKV);
  }
  {
    const int c0 = cnt(0);
#pragma unroll
    for (int i = PREQ; i < WIN; ++i) {
      if (i >= ZMIN) a[i] = v4u{0, 0, 0, 0};
      if (i < ZMIN || i < c0) a[i] = __builtin_amdgcn_raw_buffer_load_b128(rk, voff, i * 4096, AUXKV);
    }
  }
  __builtin_amdgcn_sched_barrier(0);

  if (isW) {
#pragma unroll
    for (int i = 0; i < 5; ++i) sw[i] = pack2(silu(lo16(cvm[i])), silu(hi16(cvm[i])));
  }
  tag = __builtin_amdgcn_readfirstlane(ew) + 1;
  const int r4 = lane >> 4, cc = lane & 15;
  v4u qp;
  {
    v4u g0, g1;
    const int go = OFF_Q + head * 512 + cc * 32;
    bool got = false;
    if (isW || isF) {
      rsrc_t r0, r1, r2; unsigned v0, v1, v2, s1, s2, st0, st1, st2, l0, l1, l2;
      if (isW) {
        const int C = slot - 20, m2 = 10 * xc + C;
        r0 = mkrsrc(wmod, D * 6 * D * 2); r1 = r2 = mkrsrc(wo, 4096 * D * 2);
        v0 = (160 * w + 10 * (lane >> 2)) * 7680 + (2 * D + 32 * m2 + 8 * (lane & 3)) * 2;
        v1 = v2 = ((512 * xc + 128 * w + 16 * (lane >> 3)) * D + 64 * C + 8 * (lane & 7)) * 2;
        st0 = 7680; st1 = st2 = 1280; s1 = 0; s2 = 8 * 1280;
        l0 = ldsb + L_W + 65536 + w * 10240; l1 = ldsb + L_W + w * 16384; l2 = l1 + 8192;
      } else {
        const int fj = 7 * xc + slot - 13;
        r0 = r1 = mkrsrc(w < 2 ? wg : wu, D * F * 2); r2 = mkrsrc(wd, F * D * 2);
        v0 = v1 = ((320 * (w & 1) + 20 * (lane >> 2)) * F + 32 * fj + 8 * (lane & 3)) * 2;
        v2 = (32 * fj * D) * 2 + w * 10240 + lane * 16;
        st0 = st1 = F * 2; st2 = 1024; s1 = 10 * F * 2; s2 = 0;
        l0 = ldsb + L_W + w * 20480; l1 = l0 + 10240; l2 = ldsb + L_W + 81920 + w * 10240;
      }
      l0 = __builtin_amdgcn_readfirstlane(l0); l1 = __builtin_amdgcn_readfirstlane(l1); l2 = __builtin_amdgcn_readfirstlane(l2);
      if (NBPOLL > 0) { poll_dma<10, NTHALF>(g0, g1, go, rw, r0, v0, 0, st0, l0); got = __all(okg(g0, tag) && okg(g1, tag)); }
      else dma_batch<10, NTHALF>(r0, v0, 0, st0, l0);
      if (isW) {
        if (NBPOLL > 1 && !got) { poll_dma<8, NTFULL>(g0, g1, go, rw, r1, v1, s1, st1, l1); got = __all(okg(g0, tag) && okg(g1, tag)); }
        else dma_batch<8, NTFULL>(r1, v1, s1, st1, l1);
        if (NBPOLL > 2 && !got) { poll_dma<8, NTFULL>(g0, g1, go, rw, r2, v2, s2, st2, l2); got = __all(okg(g0, tag) && okg(g1, tag)); }
        else dma_batch<8, NTFULL>(r2, v2, s2, st2, l2);
      } else {
        if (NBPOLL > 1 && !got) { poll_dma<10, NTHALF>(g0, g1, go, rw, r1, v1, s1, st1, l1); got = __all(okg(g0, tag) && okg(g1, tag)); }
        else dma_batch<10, NTHALF>(r1, v1, s1, st1, l1);
        if (NBPOLL > 2 && !got) { poll_dma<10, NTFULL>(g0, g1, go, rw, r2, v2, s2, st2, l2); got = __all(okg(g0, tag) && okg(g1, tag)); }
        else dma_batch<10, NTFULL>(r2, v2, s2, st2, l2);
      }
    }
    if (!got) {
#pragma nounroll
      for (int spin = 0; spin < SPIN_CAP; ++spin) {
        fence_c();
        g0 = ldg(rw, go); g1 = ldg(rw, go + 16);
        if (__all(okg(g0, tag) && okg(g1, tag))) break;
        __builtin_amdgcn_s_sleep(1);
      }
    }
    qp[0] = g0[0]; qp[1] = g0[2]; qp[2] = g1[0]; qp[3] = g1[2];
  }
  ST(4);
  __builtin_amdgcn_sched_barrier(0);

  float* sp = (float*)(smem + L_SP) + (w * 4 + r4) * NLMAX;
  float mx = -INFINITY;
  int so = 0;
#pragma unroll 1
  for (int it = 0; it < NIT; ++it) {
    const bool lastk = it == NIT - 1;
    const int c = cnt(it), r = cnt(lastk ? 0 : it + 1);
    so = lastk ? 0 : so + c * 4096;
    const rsrc_t rr = lastk ? rv : rk;
#define TR_STEP(HALF, CTRL, BIT) _Pragma("unroll") for (int hh = 0; hh < (HALF); ++hh) { \
      const float lo_ = xs[hh], hi_ = xs[hh + (HALF)]; const float t1_ = lo_ + dpp<CTRL>(lo_), t2_ = hi_ + dpp<CTRL>(hi_); xs[hh] = (cc & (BIT)) ? t2_ : t1_; }
#define K_GROUP(J0, N) float xs[N]; \
      _Pragma("unroll") for (int t = 0; t < (N); ++t) { \
        const int j = (J0) + t; \
        float d = 0; \
        if (j < WIN) { \
          if (j < ZMIN || j < c) { _Pragma("unroll") for (int jj = 0; jj < 4; ++jj) d = dot2(a[j < WIN ? j : 0][jj], qp[jj], d); } \
          asm volatile("" : "+v"(d)); \
          __builtin_amdgcn_sched_barrier(0); \
          if (j < ZMIN || j < r) a[j < WIN ? j : 0] = __builtin_amdgcn_raw_buffer_load_b128(rr, voff, so + j * 4096, AUXKV); \
          __builtin_amdgcn_sched_barrier(0); \
        } \
        xs[t] = d; \
      }
#pragma unroll
    for (int g = 0; g < 2; ++g) {
      K_GROUP(g * 16, 16)
      TR_STEP(8, XOR8, 8) TR_STEP(4, XOR7, 4) TR_STEP(2, XOR2, 2) TR_STEP(1, XOR1, 1)
      const int j = g * 16 + cc;
      const float sc = j < c ? rbf(rbf(xs[0]) * 0.08838834764831845f) : -INFINITY;
      mx = fmaxf(mx, sc);
      sp[it * SPW + j] = sc;
    }
    if (SPW > 32) {
      K_GROUP(32, 4)
#pragma unroll
      for (int t = 0; t < 4; ++t) { xs[t] += dpp<XOR8>(xs[t]); xs[t] += dpp<XOR7>(xs[t]); }
      TR_STEP(2, XOR2, 2) TR_STEP(1, XOR1, 1)
      const int j = 32 + (cc & 3);
      const float sc = j < c ? rbf(rbf(xs[0]) * 0.08838834764831845f) : -INFINITY;
      mx = fmaxf(mx, sc);
      if (cc < 4) sp[it * SPW + j] = sc;
    }
#undef K_GROUP
#undef TR_STEP
  }
  ST(5);
  const int po = OFF_P + (head * 8 + xc) * PSTR;
  {
    float* sml = (float*)(smem + L_ML);
    mx = wmax(mx);
    if (lane == 0) sml[w] = mx;
    bar();
    const f32x4 m4 = *(const f32x4*)sml;
    mx = fmaxf(fmaxf(m4[0], m4[1]), fmaxf(m4[2], m4[3]));
    float l = 0;
#pragma unroll
    for (int g = 0; g < NLMAX / 16; ++g) { const float p = __expf(sp[g * 16 + cc] - mx); l += p; sp[g * 16 + cc] = p; }
    l = wsum(l);
    if (lane == 0) sml[4 + w] = l;
    bar();
    if (tid == 0) {
      const f32x4 l4 = *(const f32x4*)(sml + 4);
      stg(rw, po + 64 * 16, asu(mx), asu((l4[0] + l4[1]) + (l4[2] + l4[3])), tag);
    }
  }

  if (isW) {
    const int m2 = 10 * xc + slot - 20, cq = lane & 3;
    const v4u* wl = (const v4u*)(smem + L_W + 65536 + w * 10240) + lane;
    float acc[8] = {0, 0, 0, 0, 0, 0, 0, 0};
#pragma unroll
    for (int i = 0; i < 5; ++i) {
      const P8 p = mkpairs(wl[(2 * i) * 64], wl[(2 * i + 1) * 64]);
#pragma unroll
      for (int j = 0; j < 8; ++j) acc[j] = dot2(p.p[j], sw[i], acc[j]);
    }
    float r = red8(acc, lane);
    r += dpp<SHL4>(r);
    float* red = (float*)(smem + L_RED);
    if ((lane & 4) == 0) red[w * 32 + cq * 8 + red8col(lane)] = r;
    bar();
    if (w == 0) {
      const int g = lane & 7, rep = lane >> 3;
      const f32x4 a0 = *(const f32x4*)(red + 4 * g), a1 = *(const f32x4*)(red + 32 + 4 * g), a2 = *(const f32x4*)(red + 64 + 4 * g), a3 = *(const f32x4*)(red + 96 + 4 * g);
      const f32x4 s = (a0 + a1) + (a2 + a3);
      if (rep < RM) stg(rw, OFF_M + rep * 10240 + (8 * m2 + g) * 16, pack2(s[0], s[1]), pack2(s[2], s[3]), tag);
    }
    bar();
  }

  if (CODEPF2 < 32 && slot >= CODEPF2) code_pf();
  float acc[8] = {0, 0, 0, 0, 0, 0, 0, 0};
#define V_SLOTS(LOAD) _Pragma("unroll") for (int g = 0; g < (WIN + 3) / 4; ++g) { \
      const f32x4 p4 = *(const f32x4*)&sp[it * SPW + g * 4]; \
      _Pragma("unroll") for (int t = 0; t < 4; ++t) { \
        const int j = g * 4 + t; \
        if (j < WIN) { \
          const float pp = p4[t]; \
          if (j < ZMIN || j < c) { \
            _Pragma("unroll") for (int jj = 0; jj < 4; ++jj) { const unsigned wv = a[j < WIN ? j : 0][jj]; acc[2 * jj] += pp * lo16(wv); acc[2 * jj + 1] += pp * hi16(wv); } \
          } \
          asm volatile("" : "+v"(acc[0]), "+v"(acc[1]), "+v"(acc[2]), "+v"(acc[3]), "+v"(acc[4]), "+v"(acc[5]), "+v"(acc[6]), "+v"(acc[7])); \
          __builtin_amdgcn_sched_barrier(0); \
          if (LOAD && (j < ZMIN || j < r)) a[j < WIN ? j : 0] = __builtin_amdgcn_raw_buffer_load_b128(rv, voff, so + j * 4096, AUXKV); \
          __builtin_amdgcn_sched_barrier(0); \
        } \
      } \
    }
  so = 0;
#pragma unroll 1
  for (int it = 0; it < NIT - 1; ++it) {
    const int c = cnt(it), r = cnt(it + 1);
    so += c * 4096;
    V_SLOTS(true)
  }
  v4u ml = {0, 0, 0, 0};
  const int hpo = OFF_P + (4 * xc + w) * 8 * PSTR, mlo = hpo + (lane & 7) * PSTR + 64 * 16;
  if (isW) ml = ldg(rw, mlo);
  {
    const int it = NIT - 1, c = cnt(it), r = 0;
    V_SLOTS(false)
  }
#undef V_SLOTS
  ST(6);
  {
    const float b0 = tr32(acc[0], acc[4]), b1 = tr32(acc[1], acc[5]), b2 = tr32(acc[2], acc[6]), b3 = tr32(acc[3], acc[7]);
    const f32x2 o = {tr16(b0, b2), tr16(b1, b3)};
    constexpr int SPWAVE = 4 * NLMAX;
    f32x2* pp = (f32x2*)(smem + L_SP) + lane;
    pp[w * (SPWAVE / 2)] = o;
    bar();
    if (w == 0) {
      const f32x2 t = (pp[0] + pp[SPWAVE / 2]) + (pp[SPWAVE] + pp[3 * SPWAVE / 2]);
      stg(rw, po + (4 * cc + r4) * 16, asu(t[0]), asu(t[1]), tag);
    }
  }
  ST(7);

  if (isW) {
    const int C = slot - 20, cq = lane & 7;
    const v4u* wl = (const v4u*)(smem + L_W + w * 16384) + lane;
    unsigned pr[64];
#pragma unroll
    for (int i = 0; i < 8; ++i) {
      const P8 p = mkpairs(wl[(2 * i) * 64], wl[(2 * i + 1) * 64]);
#pragma unroll
      for (int j = 0; j < 8; ++j) pr[8 * i + j] = p.p[j];
    }
#pragma unroll
    for (int j = 0; j < 64; ++j) pin(pr[j]);
    v4u g[8];
    u64 m[8];
#pragma unroll
    for (int s = 0; s < 8; ++s) { g[s] = v4u{0, 0, 0, 0}; m[s] = lanes(true); }
#pragma nounroll
    for (int spin = 0; spin < SPIN_CAP; ++spin) {
      const u64 mb = lanes(gbad(ml, tag) != 0);
      if (mb == 0) break;
      grow1(ml, mb, mlo, rw);
    }
    float fs[8];
    {
      const unsigned mu = ml[0], lu = ml[2];
      const float ms = asf(mu);
      float M = fmaxf(ms, dpp<XOR1>(ms)); M = fmaxf(M, dpp<XOR2>(M)); M = fmaxf(M, dpp<XOR7>(M));
      const float e = __expf(ms - M);
      float L = e * asf(lu); L += dpp<XOR1>(L); L += dpp<XOR2>(L); L += dpp<XOR7>(L);
      const float f = e * __builtin_amdgcn_rcpf(L);
#pragma unroll
      for (int s = 0; s < 8; ++s) { fs[s] = asf(__builtin_amdgcn_readlane(asu(f), s)); pins(fs[s]); }
    }
    const int v0 = hpo + lane * 16, v1 = v0 + 4 * PSTR;
    unsigned* hw = (unsigned*)(smem + L_HW) + w * 192;
    float* red = (float*)(smem + L_RED);
#pragma nounroll
    for (int spin = 0; spin < SPIN_CAP; ++spin) {
      grow8(g, m, v0, v1, rw);
      unsigned bd[8];
#pragma unroll
      for (int s = 0; s < 8; ++s) bd[s] = gbad(g[s], tag);
      if (lanes(((bd[0] | bd[1] | bd[2]) | (bd[3] | bd[4] | bd[5]) | (bd[6] | bd[7])) != 0) == 0) break;
#pragma unroll
      for (int s = 0; s < 8; ++s) m[s] = lanes(bd[s] != 0);
    }
    ST(8);
    float o0, o1;
    { const unsigned u0 = g[0][0], u1 = g[0][2]; o0 = fs[0] * asf(u0); o1 = fs[0] * asf(u1); }
#pragma unroll
    for (int s = 1; s < 8; ++s) { const unsigned u0 = g[s][0], u1 = g[s][2]; o0 += fs[s] * asf(u0); o1 += fs[s] * asf(u1); }
    hw[lane] = pack2(o0, o1);
    fence_c();
    const v4u h0 = *(const v4u*)(hw + 8 * (lane >> 3)), h1 = *(const v4u*)(hw + 8 * (lane >> 3) + 4);
    float y[8] = {0, 0, 0, 0, 0, 0, 0, 0};
#pragma unroll
    for (int i = 0; i < 8; ++i) {
      const unsigned hv = i < 4 ? h0[i & 3] : h1[i & 3];
#pragma unroll
      for (int j = 0; j < 8; ++j) y[j] = dot2(pr[8 * i + j], hv, y[j]);
    }
    const float r = red8(y, lane);
    red[w * 64 + cq * 8 + red8col(lane)] = r;
    bar();
    if (w == 0 && lane < 32) {
      const float* rp = red + 2 * lane;
      const float y0 = (rp[0] + rp[64]) + (rp[128] + rp[192]), y1 = (rp[1] + rp[65]) + (rp[129] + rp[193]);
      stg(rw, OFF_Y + xc * 5120 + plane(32 * C + lane), asu(y0), asu(y1), tag);
    }
    ST(9);
  } else if (isF) {
    const int fj = 7 * xc + slot - 13, wh = w & 1, rho = lane >> 2, cq = lane & 3;
    unsigned pr[80], pd[64];
    {
      const v4u* wl = (const v4u*)(smem + L_W + w * 20480) + lane;
#pragma unroll
      for (int i = 0; i < 10; ++i) {
        const P8 p = mkpairs(wl[(2 * i) * 64], wl[(2 * i + 1) * 64]);
#pragma unroll
        for (int j = 0; j < 8; ++j) pr[8 * i + j] = p.p[j];
      }
      const int tc = tid < 160 ? tid : 0;
      const v2u* dl = (const v2u*)(smem + L_W + 81920) + tc;
#pragma unroll
      for (int i = 0; i < 16; ++i) {
        const v2u r0 = dl[(2 * i) * 160], r1 = dl[(2 * i + 1) * 160];
        const unsigned a0 = r0[0], a1 = r0[1], b0 = r1[0], b1 = r1[1];
        pd[4 * i] = plo(a0, b0); pd[4 * i + 1] = phi(a0, b0); pd[4 * i + 2] = plo(a1, b1); pd[4 * i + 3] = phi(a1, b1);
      }
#pragma unroll
      for (int j = 0; j < 80; ++j) pin(pr[j]);
#pragma unroll
      for (int j = 0; j < 64; ++j) pin(pd[j]);
    }
    float* x1s = (float*)(smem + L_X1); float* cbs = (float*)(smem + L_CB); float* sbs = (float*)(smem + L_SB);
    float gaf[4], xf[4];
    if (tid < 160) {
      const int mo = OFF_M + (xc % RM) * 10240 + tid * 16;
      v4u ga = {0, 0, 0, 0}, sb = ga, cb = ga;
      u64 m0 = lanes(true), m1 = m0, m2 = m0;
      const v2u xw = *(const v2u*)(x + 4 * tid);
#pragma nounroll
      for (int spin = 0; spin < SPIN_CAP; ++spin) {
        grow3(ga, sb, cb, m0, m1, m2, mo, mo + 2560, mo + 5120, rw);
        const unsigned b0 = gbad(ga, tag), b1 = gbad(sb, tag), b2 = gbad(cb, tag);
        if (lanes((b0 | b1 | b2) != 0) == 0) break;
        m0 = lanes(b0 != 0); m1 = lanes(b1 != 0); m2 = lanes(b2 != 0);
      }
      const unsigned c0 = cb[0], c1 = cb[2], s0 = sb[0], s1 = sb[2], g0 = ga[0], g1 = ga[2], x0 = xw[0], x1w = xw[1];
      *(f32x4*)(cbs + 4 * tid) = f32x4{rbf(1.f + lo16(c0)), rbf(1.f + hi16(c0)), rbf(1.f + lo16(c1)), rbf(1.f + hi16(c1))};
      *(f32x4*)(sbs + 4 * tid) = f32x4{lo16(s0), hi16(s0), lo16(s1), hi16(s1)};
      gaf[0] = lo16(g0); gaf[1] = hi16(g0); gaf[2] = lo16(g1); gaf[3] = hi16(g1);
      xf[0] = lo16(x0); xf[1] = hi16(x0); xf[2] = lo16(x1w); xf[3] = hi16(x1w);
#pragma unroll
      for (int j = 0; j < 4; ++j) { pin(gaf[j]); pin(xf[j]); }
    }
    if (PWAIT) {
      v4u pg = {0, 0, 0, 0};
      u64 m0 = lanes(true);
#pragma nounroll
      for (int spin = 0; spin < SPIN_CAP; ++spin) {
        grow1(pg, m0, OFF_P + tid * PSTR, rw);
        m0 = lanes(gbad(pg, tag) != 0);
        if (m0 == 0) break;
      }
      bar();
    }
    if (tid < 160) {
      v4u g[16];
      u64 m[8];
#pragma unroll
      for (int G = 0; G < 8; ++G) { g[2 * G] = v4u{0, 0, 0, 0}; g[2 * G + 1] = v4u{0, 0, 0, 0}; m[G] = lanes(true); }
#pragma nounroll
      for (int spin = 0; spin < SPIN_CAP; ++spin) {
        grow16(g, m, OFF_Y + tid * 16, rw);
        unsigned bd[8];
#pragma unroll
        for (int G = 0; G < 8; ++G) bd[G] = gbad(g[2 * G], tag) | gbad(g[2 * G + 1], tag);
        if (lanes(((bd[0] | bd[1] | bd[2]) | (bd[3] | bd[4] | bd[5]) | (bd[6] | bd[7])) != 0) == 0) break;
#pragma unroll
        for (int G = 0; G < 8; ++G) m[G] = lanes(bd[G] != 0);
      }
      float y[4];
      { const unsigned u0 = g[0][0], u1 = g[0][2], u2 = g[1][0], u3 = g[1][2]; y[0] = asf(u0); y[1] = asf(u1); y[2] = asf(u2); y[3] = asf(u3); }
#pragma unroll
      for (int G = 1; G < 8; ++G) { const unsigned u0 = g[2 * G][0], u1 = g[2 * G][2], u2 = g[2 * G + 1][0], u3 = g[2 * G + 1][2]; y[0] += asf(u0); y[1] += asf(u1); y[2] += asf(u2); y[3] += asf(u3); }
      *(f32x4*)(x1s + 4 * tid) = f32x4{rbf(xf[0] + rbf(gaf[0] * rbf(y[0]))), rbf(xf[1] + rbf(gaf[1] * rbf(y[1]))),
                                       rbf(xf[2] + rbf(gaf[2] * rbf(y[2]))), rbf(xf[3] + rbf(gaf[3] * rbf(y[3])))};
    }
    ST(8);
    bar();
    ST(11);
    float xa[5], xb[5], cbv[5], sbv[5];
#pragma unroll
    for (int i = 0; i < 5; ++i) { xa[i] = x1s[5 * lane + i]; xb[i] = x1s[320 + 5 * lane + i]; }
    const float sh = x1s[0];
#pragma unroll
    for (int i = 0; i < 5; ++i) { cbv[i] = cbs[320 * wh + 5 * lane + i]; sbv[i] = sbs[320 * wh + 5 * lane + i]; }
    float s1 = 0, s2 = 0;
#pragma unroll
    for (int i = 0; i < 5; ++i) { const float d0 = xa[i] - sh, d1 = xb[i] - sh; s1 += d0 + d1; s2 += d0 * d0; s2 += d1 * d1; }
    wsum2(s1, s2);
    const float md = s1 * (1.f / D), mean = sh + md, var = fmaxf(s2 * (1.f / D) - md * md, 0.f);
    const float rs = __builtin_amdgcn_rcpf(rbf(__builtin_amdgcn_sqrtf(rbf(rbf(var) + 1e-5f)))), mb = rbf(mean);
    STD(12, rs);
    u16* hs = (u16*)((unsigned*)(smem + L_HW) + w * 192);
#pragma unroll
    for (int i = 0; i < 5; ++i) {
      const float xx = wh ? xb[i] : xa[i];
      hs[5 * lane + i] = (u16)pack2(rbf(rbf(rbf(xx - mb) * rs) * cbv[i]) + sbv[i], 0.f);
    }
    fence_c();
    const v2u* hp = (const v2u*)((const unsigned*)hs + 10 * rho);
    float ga8[8] = {0, 0, 0, 0, 0, 0, 0, 0};
#pragma unroll
    for (int i = 0; i < 5; ++i) {
      const v2u hv = hp[i];
      const unsigned h0 = hv[0], h1 = hv[1];
#pragma unroll
      for (int j = 0; j < 8; ++j) ga8[j] = dot2(pr[16 * i + j], h0, ga8[j]);
#pragma unroll
      for (int j = 0; j < 8; ++j) ga8[j] = dot2(pr[16 * i + 8 + j], h1, ga8[j]);
    }
    STD(13, ga8[7]);
    float r = red8(ga8, lane);
    r += dpp<SHL4>(r);
    float* red = (float*)(smem + L_RED);
    if ((lane & 4) == 0) red[w * 32 + cq * 8 + red8col(lane)] = r;
    bar();
    ST(14);
    const int col = lane & 31;
    const float gg = rbf(red[col] + red[32 + col]), uu = rbf(red[64 + col] + red[96 + col]);
    const float av = rbf(rbf(silu(gg)) * uu);
    const unsigned aw = (asu(av) >> 16) | (asu(dpp<XOR1>(av)) & 0xffff0000u);
    float z[4] = {0, 0, 0, 0};
#pragma unroll
    for (int i = 0; i < 16; ++i) {
      const unsigned as = __builtin_amdgcn_readlane(aw, 2 * i);
#pragma unroll
      for (int j = 0; j < 4; ++j) z[j] = dot2(pd[4 * i + j], as, z[j]);
    }
    STD(15, z[3]);
    if (tid < 160) {
      stg(rw, OFF_Z + fj * 5120 + tid * 16, asu(z[0]), asu(z[1]), tag);
      stg(rw, OFF_Z + fj * 5120 + 2560 + tid * 16, asu(z[2]), asu(z[3]), tag);
    }
    ST(9);
  } else if (isM1) {
    const int fi = 5 * xc + slot, g8 = lane & 7, pj = plane(8 * fi + g8);
    float x1a = 0, x1b = 0, gb0 = 0, gb1 = 0;
    if (w == 0) {
      const int mo = OFF_M + (xc % RM) * 10240 + (4 * fi + (g8 >> 1)) * 16;
      v4u ga = {0, 0, 0, 0}, gb = ga, yy = ga;
      u64 m0 = lanes(true), m1 = m0, m2 = m0;
#pragma nounroll
      for (int spin = 0; spin < SPIN_CAP; ++spin) {
        grow3(ga, gb, yy, m0, m1, m2, mo, mo + 7680, OFF_Y + (lane >> 3) * 5120 + pj, rw);
        const unsigned b0 = gbad(ga, tag), b1 = gbad(gb, tag), b2 = gbad(yy, tag);
        if (lanes((b0 | b1 | b2) != 0) == 0) break;
        m0 = lanes(b0 != 0); m1 = lanes(b1 != 0); m2 = lanes(b2 != 0);
        __builtin_amdgcn_s_sleep(1);
      }
      const unsigned xw = ((const unsigned*)x)[8 * fi + g8];
      const unsigned gaw = (g8 & 1) ? ga[2] : ga[0], gbw = (g8 & 1) ? gb[2] : gb[0], u0 = yy[0], u1 = yy[2];
      float y0 = __shfl(asf(u0), g8), y1 = __shfl(asf(u1), g8);
#pragma unroll
      for (int G = 1; G < 8; ++G) { y0 += __shfl(asf(u0), G * 8 + g8); y1 += __shfl(asf(u1), G * 8 + g8); }
      x1a = rbf(lo16(xw) + rbf(lo16(gaw) * rbf(y0))); x1b = rbf(hi16(xw) + rbf(hi16(gaw) * rbf(y1)));
      gb0 = lo16(gbw); gb1 = hi16(gbw);
      pin(x1a); pin(x1b); pin(gb0); pin(gb1);
    }
    ST(8);
    const int p0 = tid >> 3;
    v4u z0 = {0, 0, 0, 0}, z1 = {0, tag, 0, tag};
    {
      const int zo = OFF_Z + p0 * 5120 + pj;
      u64 m0 = lanes(true), m1 = lanes(p0 < 22);
#pragma nounroll
      for (int spin = 0; spin < SPIN_CAP; ++spin) {
        grow2(z0, z1, m0, m1, zo, zo + 32 * 5120, rw);
        const unsigned b0 = gbad(z0, tag), b1 = gbad(z1, tag);
        if (lanes((b0 | b1) != 0) == 0) break;
        m0 = lanes(b0 != 0); m1 = lanes(b1 != 0);
      }
    }
    ST(9);
    const unsigned u0 = z0[0], u1 = z0[2], u2 = z1[0], u3 = z1[2];
    float sz = tr32(asf(u0) + asf(u2), asf(u1) + asf(u3));
    sz = tr16(sz, sz);
    sz += dpp<XOR8>(sz);
    float* red = (float*)(smem + L_RED);
    if ((lane & 24) == 0) red[((lane >> 5) * 8 + g8) * 4 + w] = sz;
    bar();
    if (w == 0 && lane < 8) {
      const f32x4 e = *(const f32x4*)(red + 4 * lane), o = *(const f32x4*)(red + 32 + 4 * lane);
      const float y0 = (e[0] + e[1]) + (e[2] + e[3]), y1 = (o[0] + o[1]) + (o[2] + o[3]);
      ((unsigned*)out)[8 * fi + lane] = pack2(x1a + rbf(gb0 * rbf(y0)), x1b + rbf(gb1 * rbf(y1)));
      if (fi == 0 && lane == 0) __builtin_amdgcn_raw_buffer_store_b32(tag, rw, 0, 0, 16);
    }
    ST(10);
  }
  asm volatile(".Lmega_end:");
}

extern "C" void kernel_launch(void* const* d_in, const int* in_sizes, int n_in, void* d_out, int out_size, void* d_ws, size_t ws_size,
                              hipStream_t stream) {
  hipFuncSetAttribute((const void*)mega, hipFuncAttributeMaxDynamicSharedMemorySize, LDS_BYTES);
  mega<<<256, 256, LDS_BYTES, stream>>>((const u16*)d_in[0], (const u16*)d_in[1], (const u16*)d_in[2], (const u16*)d_in[3], (const u16*)d_in[4],
                                        (const u16*)d_in[6], (const u16*)d_in[7], (const u16*)d_in[8], (const u16*)d_in[9], (const u16*)d_in[10],
                                        (u16*)d_out, (unsigned*)d_ws);
}
```

```cpp
#include <hip/hip_runtime.h>
#include <stdint.h>

typedef unsigned short u16;
typedef unsigned v4u __attribute__((ext_vector_type(4)));
typedef unsigned v2u __attribute__((ext_vector_type(2)));
typedef float f32x4 __attribute__((ext_vector_type(4)));
typedef __bf16 bf16x2 __attribute__((ext_vector_type(2)));
typedef __amdgpu_buffer_rsrc_t rsrc_t;
#define LAS __attribute__((address_space(3)))
#define DI __device__ __forceinline__

constexpr int AUX_NT = 2, AUX_DEV = 16;
constexpr int BULK_DELAY = 8;
constexpr int CODEPFL = 16;
constexpr int RH = 4, RM = 4;
constexpr int PACE0 = 3800, PACE1 = 80;
constexpr int PGATE = 220;
constexpr int PG = 4;
constexpr int SPIN_CAP = 1 << 16;
#define DELTAS 5, 6, 5, 5, 6, -4, -5, -5, -5, -5, -5, -5, -6, -2, -3, -3, -4, -3, -4, -4, 1, 3, 1, 1, 2, 3, 1, 1, 1, 2, 10, 10

constexpr int D = 640, SK = 16384, F = 1728;
constexpr int OFF_H = 256;
constexpr int OFF_Q = OFF_H + RH * 5120;
constexpr int OFF_M = OFF_Q + 32 * 512;
constexpr int PSTR = 72 * 16;
constexpr int OFF_P = OFF_M + RM * 10240;
constexpr int OFF_Y = OFF_P + 256 * PSTR;
constexpr int OFF_Z = OFF_Y + 8 * 5120;
constexpr int OFF_T = OFF_Z + 54 * 5120;
constexpr int WS_BYTES = OFF_T + 8 * 256;
constexpr int KDELTA[32] = {DELTAS};
constexpr unsigned long long kcount(int c) { unsigned long long r = 0; for (int k = 0; k < 8; ++k) r |= (unsigned long long)(64 + KDELTA[c + 4 * k]) << (8 * k); return r; }
constexpr unsigned long long kstart(int c) { unsigned long long r = 0; int s = 0; for (int k = 0; k < 8; ++k) { r |= (unsigned long long)(64 + s) << (8 * k); s += KDELTA[c + 4 * k]; } return r; }
constexpr bool kok() { for (int c = 0; c < 4; ++c) { int s = 0; for (int k = 0; k < 8; ++k) { s += KDELTA[c + 4 * k]; if (s < -60 || s > 60) return false; } if (s) return false; } return true; }
constexpr int XDELTA[8] = {0, 1, 1, 1, 0, -1, -1, -1};
constexpr unsigned xpack() { unsigned r = 0; for (int x = 0; x < 8; ++x) r |= (unsigned)(XDELTA[x] + 8) << (4 * x); return r; }
constexpr bool xok() { int s = 0; for (int x = 0; x < 8; ++x) { s += XDELTA[x]; if (XDELTA[x] < -4 || XDELTA[x] > 4) return false; } return s == 0; }
constexpr int kmin() { int m = 0, e = 0; for (int i = 0; i < 32; ++i) m = KDELTA[i] < m ? KDELTA[i] : m; for (int x = 0; x < 8; ++x) e = XDELTA[x] < e ? XDELTA[x] : e; return 128 + m + e; }
constexpr int kmax() { int m = 0, e = 0; for (int i = 0; i < 32; ++i) m = KDELTA[i] > m ? KDELTA[i] : m; for (int x = 0; x < 8; ++x) e = XDELTA[x] > e ? XDELTA[x] : e; return 128 + m + e; }
static_assert(kok() && xok(), "key split");
constexpr int NIT = 4, ZMIN = kmin() / NIT, WIN = (kmax() + NIT - 1) / NIT, SPW = WIN <= 32 ? 32 : 36, NLMAX = NIT * SPW;
static_assert(WIN <= 36 && ZMIN >= 20, "ring");
constexpr int L_SP = 0;
constexpr int L_RED = L_SP + 16 * NLMAX * 4;
constexpr int L_ML = L_RED + 4096;
constexpr int L_X1 = L_ML + 64;
constexpr int L_CB = L_X1 + 2560;
constexpr int L_SB = L_CB + 2560;
constexpr int L_HW = L_SB + 2560;
constexpr int L_W = L_HW + 4 * 768;
constexpr int L_DUMMY = L_W + 120 * 1024;
constexpr int LDS_BYTES = L_DUMMY + 1024;
static_assert(L_W % 16 == 0 && LDS_BYTES <= 160 * 1024, "lds");

DI float asf(unsigned u) { return __builtin_bit_cast(float, u); }
DI unsigned asu(float f) { return __builtin_bit_cast(unsigned, f); }
DI float lo16(unsigned w) { return asf(w << 16); }
DI float hi16(unsigned w) { return asf(w & 0xffff0000u); }
typedef float f32x2 __attribute__((ext_vector_type(2)));
DI unsigned pack2(float a, float b) { const f32x2 v = {a, b}; return __builtin_bit_cast(unsigned, __builtin_convertvector(v, bf16x2)); }
DI float rbf(float f) { return asf(pack2(0.f, f)); }
DI float dot2(unsigned k, unsigned q, float acc) {
  return __builtin_amdgcn_fdot2_f32_bf16(__builtin_bit_cast(bf16x2, k), __builtin_bit_cast(bf16x2, q), acc, false);
}
template <int CTRL> DI float dpp(float x) { return asf(__builtin_amdgcn_mov_dpp(asu(x), CTRL, 0xf, 0xf, true)); }
constexpr int XOR1 = 0xB1, XOR2 = 0x4E, XOR7 = 0x141, XOR8 = 0x128, ROR4 = 0x124, SHL4 = 0x104;
DI float tr32(float a, float b) { v2u r = __builtin_amdgcn_permlane32_swap(asu(a), asu(b), false, false); unsigned p = r[0], q = r[1]; return asf(p) + asf(q); }
DI float tr16(float a, float b) { v2u r = __builtin_amdgcn_permlane16_swap(asu(a), asu(b), false, false); unsigned p = r[0], q = r[1]; return asf(p) + asf(q); }
DI float tr8(float a, float b, bool hi) { const float t1 = a + dpp<XOR8>(a), t2 = b + dpp<XOR8>(b); return hi ? t2 : t1; }
DI float wsum(float v) {
  v += dpp<XOR1>(v); v += dpp<XOR2>(v); v += dpp<XOR7>(v); v += dpp<XOR8>(v); v = tr16(v, v); return tr32(v, v);
}
DI void wsum2(float& a, float& b) {
  a += dpp<XOR1>(a); b += dpp<XOR1>(b); a += dpp<XOR2>(a); b += dpp<XOR2>(b); a += dpp<XOR7>(a); b += dpp<XOR7>(b); a += dpp<XOR8>(a); b += dpp<XOR8>(b);
  a = tr16(a, a); b = tr16(b, b); a = tr32(a, a); b = tr32(b, b);
}
DI float mx16(float a) { v2u r = __builtin_amdgcn_permlane16_swap(asu(a), asu(a), false, false); unsigned p = r[0], q = r[1]; return fmaxf(asf(p), asf(q)); }
DI float mx32(float a) { v2u r = __builtin_amdgcn_permlane32_swap(asu(a), asu(a), false, false); unsigned p = r[0], q = r[1]; return fmaxf(asf(p), asf(q)); }
DI float wmax(float v) {
  v = fmaxf(v, dpp<XOR1>(v)); v = fmaxf(v, dpp<XOR2>(v)); v = fmaxf(v, dpp<XOR7>(v)); v = fmaxf(v, dpp<XOR8>(v)); return mx32(mx16(v));
}
DI float silu(float v) { return v * __builtin_amdgcn_rcpf(1.f + __expf(-v)); }
DI unsigned plo(unsigned a, unsigned b) { return __builtin_amdgcn_perm(b, a, 0x05040100u); }
DI unsigned phi(unsigned a, unsigned b) { return __builtin_amdgcn_perm(b, a, 0x07060302u); }
struct P8 { unsigned p[8]; };
DI P8 mkpairs(v4u a, v4u b) {
  P8 r;
#pragma unroll
  for (int i = 0; i < 4; ++i) { const unsigned x = a[i], y = b[i]; r.p[2 * i] = plo(x, y); r.p[2 * i + 1] = phi(x, y); }
  return r;
}
DI rsrc_t mkrsrc(const void* p, unsigned bytes) { return __builtin_amdgcn_make_buffer_rsrc((void*)p, 0, bytes, 0x00020000); }
DI v4u ldg(rsrc_t r, int off) { return __builtin_amdgcn_raw_buffer_load_b128(r, off, 0, AUX_DEV); }
DI void stg(rsrc_t r, int off, unsigned a, unsigned b, unsigned tag) { v4u g = {a, tag, b, tag}; __builtin_amdgcn_raw_buffer_store_b128(g, r, off, 0, AUX_DEV); }
DI bool okg(v4u g, unsigned tag) { const unsigned a = g[1], b = g[3]; return a == tag && b == tag; }
DI void fence_c() { asm volatile("" ::: "memory"); }
DI unsigned gbad(v4u g, unsigned tag) { const unsigned a = g[1], b = g[3]; return __builtin_amdgcn_bitop3_b32(a, b, tag, 0x7E); }
typedef unsigned long long u64;
DI u64 lanes(bool c) { return __builtin_amdgcn_ballot_w64(c); }
DI void pin(unsigned& v) { asm volatile("" : "+v"(v)); }
DI void pin(float& v) { asm volatile("" : "+v"(v)); }
DI void pin(v4u& v) { asm volatile("" : "+v"(v)); }
DI void pins(float& v) { asm volatile("" : "+s"(v)); }
#define GROW(g, m, vo, so, imm) "s_mov_b64 exec, %[" #m "]\n\tbuffer_load_dwordx4 %[" #g "], %[" #vo "], %[rw], " so " offen offset:" #imm " sc1\n\t"
#define GHEAD "s_mov_b64 %[sv], exec\n\t"
#define GTAIL "s_mov_b64 exec, %[sv]\n\ts_waitcnt vmcnt(0)"
DI void grow1(v4u& g0, u64 m0, int vo, rsrc_t rw) {
  u64 sv;
  asm volatile(GHEAD GROW(g0, m0, vo, "0", 0) GTAIL : [g0] "+v"(g0), [sv] "=&s"(sv) : [m0] "s"(m0), [vo] "v"(vo), [rw] "s"(rw) : "memory");
}
DI void grow2(v4u& g0, v4u& g1, u64 m0, u64 m1, int v0, int v1, rsrc_t rw) {
  u64 sv;
  asm volatile(GHEAD GROW(g0, m0, v0, "0", 0) GROW(g1, m1, v1, "0", 0) GTAIL
               : [g0] "+v"(g0), [g1] "+v"(g1), [sv] "=&s"(sv) : [m0] "s"(m0), [m1] "s"(m1), [v0] "v"(v0), [v1] "v"(v1), [rw] "s"(rw) : "memory");
}
DI void grow3(v4u& g0, v4u& g1, v4u& g2, u64 m0, u64 m1, u64 m2, int v0, int v1, int v2, rsrc_t rw) {
  u64 sv;
  asm volatile(GHEAD GROW(g0, m0, v0, "0", 0) GROW(g1, m1, v1, "0", 0) GROW(g2, m2, v2, "0", 0) GTAIL
               : [g0] "+v"(g0), [g1] "+v"(g1), [g2] "+v"(g2), [sv] "=&s"(sv)
               : [m0] "s"(m0), [m1] "s"(m1), [m2] "s"(m2), [v0] "v"(v0), [v1] "v"(v1), [v2] "v"(v2), [rw] "s"(rw) : "memory");
}
DI void grow8(v4u (&g)[8], const u64 (&m)[8], int v0, int v1, rsrc_t rw) {
  u64 sv;
  asm volatile(GHEAD GROW(g0, m0, v0, "0", 0) GROW(g1, m1, v0, "0", 1152) GROW(g2, m2, v0, "0", 2304) GROW(g3, m3, v0, "0", 3456)
               GROW(g4, m4, v1, "0", 0) GROW(g5, m5, v1, "0", 1152) GROW(g6, m6, v1, "0", 2304) GROW(g7, m7, v1, "0", 3456) GTAIL
               : [g0] "+v"(g[0]), [g1] "+v"(g[1]), [g2] "+v"(g[2]), [g3] "+v"(g[3]), [g4] "+v"(g[4]), [g5] "+v"(g[5]), [g6] "+v"(g[6]), [g7] "+v"(g[7]), [sv] "=&s"(sv)
               : [m0] "s"(m[0]), [m1] "s"(m[1]), [m2] "s"(m[2]), [m3] "s"(m[3]), [m4] "s"(m[4]), [m5] "s"(m[5]), [m6] "s"(m[6]), [m7] "s"(m[7]),
                 [v0] "v"(v0), [v1] "v"(v1), [rw] "s"(rw) : "memory");
}
#define GPAIR(a, b, m, so) "s_mov_b64 exec, %[" #m "]\n\tbuffer_load_dwordx4 %[" #a "], %[vo], %[rw], " so " offen sc1\n\tbuffer_load_dwordx4 %[" #b "], %[vo], %[rw], " so " offen offset:2560 sc1\n\t"
DI void grow16(v4u (&g)[16], const u64 (&m)[8], int vo, rsrc_t rw) {
  u64 sv;
  const unsigned s1 = 5120, s2 = 10240, s3 = 15360, s4 = 20480, s5 = 25600, s6 = 30720, s7 = 35840;
  asm volatile(GHEAD GPAIR(g0, g1, m0, "0") GPAIR(g2, g3, m1, "%[s1]") GPAIR(g4, g5, m2, "%[s2]") GPAIR(g6, g7, m3, "%[s3]")
               GPAIR(g8, g9, m4, "%[s4]") GPAIR(g10, g11, m5, "%[s5]") GPAIR(g12, g13, m6, "%[s6]") GPAIR(g14, g15, m7, "%[s7]") GTAIL
               : [g0] "+v"(g[0]), [g1] "+v"(g[1]), [g2] "+v"(g[2]), [g3] "+v"(g[3]), [g4] "+v"(g[4]), [g5] "+v"(g[5]), [g6] "+v"(g[6]), [g7] "+v"(g[7]),
                 [g8] "+v"(g[8]), [g9] "+v"(g[9]), [g10] "+v"(g[10]), [g11] "+v"(g[11]), [g12] "+v"(g[12]), [g13] "+v"(g[13]), [g14] "+v"(g[14]), [g15] "+v"(g[15]), [sv] "=&s"(sv)
               : [m0] "s"(m[0]), [m1] "s"(m[1]), [m2] "s"(m[2]), [m3] "s"(m[3]), [m4] "s"(m[4]), [m5] "s"(m[5]), [m6] "s"(m[6]), [m7] "s"(m[7]),
                 [s1] "s"(s1), [s2] "s"(s2), [s3] "s"(s3), [s4] "s"(s4), [s5] "s"(s5), [s6] "s"(s6), [s7] "s"(s7), [vo] "v"(vo), [rw] "s"(rw) : "memory");
}
DI int plane(int j) { return ((j & 1) * 160 + (j >> 1)) * 16; }
#define DMA_BODY(NT) ".rept %c[n]\n\ts_nop 0\n\tbuffer_load_dwordx4 %[vo], %[r], %[so] offen" NT " lds\n\ts_add_u32 m0, m0, 0x400\n\ts_add_u32 %[so], %[so], %[st]\n\t.endr\n\t"
template <int N, bool NT> DI void dma_batch(rsrc_t r, unsigned voff, unsigned soff, unsigned stride, unsigned lds) {
  unsigned keep;
  if (NT) asm volatile("s_mov_b32 %[k], m0\n\ts_mov_b32 m0, %[l]\n\t" DMA_BODY(" nt") "s_mov_b32 m0, %[k]"
               : [k] "=&s"(keep), [so] "+s"(soff) : [vo] "v"(voff), [r] "s"(r), [st] "s"(stride), [l] "s"(lds), [n] "n"(N) : "memory", "scc");
  else asm volatile("s_mov_b32 %[k], m0\n\ts_mov_b32 m0, %[l]\n\t" DMA_BODY("") "s_mov_b32 m0, %[k]"
               : [k] "=&s"(keep), [so] "+s"(soff) : [vo] "v"(voff), [r] "s"(r), [st] "s"(stride), [l] "s"(lds), [n] "n"(N) : "memory", "scc");
}
#define POLL2 "buffer_load_dwordx4 %[g0], %[go], %[rw], 0 offen sc1\n\tbuffer_load_dwordx4 %[g1], %[go], %[rw], 0 offen offset:16 sc1\n\t"
template <int N, bool NT> DI void poll_dma(v4u& g0, v4u& g1, int goff, rsrc_t rw, rsrc_t r, unsigned voff, unsigned soff, unsigned stride, unsigned lds) {
  unsigned keep;
  if (NT) asm volatile(POLL2 "s_mov_b32 %[k], m0\n\ts_mov_b32 m0, %[l]\n\t" DMA_BODY(" nt") "s_mov_b32 m0, %[k]\n\ts_waitcnt vmcnt(%c[n])"
               : [g0] "=&v"(g0), [g1] "=&v"(g1), [k] "=&s"(keep), [so] "+s"(soff)
               : [go] "v"(goff), [rw] "s"(rw), [vo] "v"(voff), [r] "s"(r), [st] "s"(stride), [l] "s"(lds), [n] "n"(N) : "memory", "scc");
  else asm volatile(POLL2 "s_mov_b32 %[k], m0\n\ts_mov_b32 m0, %[l]\n\t" DMA_BODY("") "s_mov_b32 m0, %[k]\n\ts_waitcnt vmcnt(%c[n])"
               : [g0] "=&v"(g0), [g1] "=&v"(g1), [k] "=&s"(keep), [so] "+s"(soff)
               : [go] "v"(goff), [rw] "s"(rw), [vo] "v"(voff), [r] "s"(r), [st] "s"(stride), [l] "s"(lds), [n] "n"(N) : "memory", "scc");
}
DI void bar() { asm volatile("s_waitcnt lgkmcnt(0)\n\ts_barrier" ::: "memory"); }
DI float red8(const float (&a)[8], int lane) {
  const float b0 = tr32(a[0], a[4]), b1 = tr32(a[1], a[5]), b2 = tr32(a[2], a[6]), b3 = tr32(a[3], a[7]);
  const float c0 = tr16(b0, b2), c1 = tr16(b1, b3);
  return tr8(c0, c1, lane & 8);
}
DI int red8col(int lane) { return ((lane >> 5) & 1) * 4 + ((lane >> 4) & 1) * 2 + ((lane >> 3) & 1); }

__global__ void __launch_bounds__(256, 1)
mega(const u16* __restrict__ x, const u16* __restrict__ K, const u16* __restrict__ V, const u16* __restrict__ wq, const u16* __restrict__ wo,
     const u16* __restrict__ wg, const u16* __restrict__ wu, const u16* __restrict__ wd, const u16* __restrict__ c, const u16* __restrict__ wmod,
     u16* __restrict__ out, unsigned* ws) {
  extern __shared__ __attribute__((aligned(16))) char smem[];
  const int tid = threadIdx.x, lane = tid & 63, w = __builtin_amdgcn_readfirstlane(tid >> 6), b = blockIdx.x;
  const int xc = b & 7, slot = b >> 3, head = (slot - 4 * xc) & 31;
  const unsigned pt0 = (unsigned)__builtin_amdgcn_s_memrealtime();
  const unsigned ldsb = __builtin_amdgcn_readfirstlane((unsigned)(size_t)(LAS char*)smem);
  rsrc_t rc;
  {
    unsigned long long pc; unsigned len;
    asm volatile("s_getpc_b64 %0\n.Lmega_pf0:\n\ts_mov_b32 %1, .Lmega_end-.Lmega_pf0" : "=s"(pc), "=s"(len));
    rc = mkrsrc((const void*)pc, len);
  }
  auto code_pf = [&]() {
    unsigned keep;
    asm volatile("s_mov_b32 %0, m0\n\ts_mov_b32 m0, %1\n\ts_nop 0\n\tbuffer_load_dword %2, %3, 0 offen lds\n\ts_mov_b32 m0, %0"
                 : "=&s"(keep) : "s"(ldsb + L_DUMMY), "v"(tid * 128), "s"(rc) : "memory");
  };
  if (b >= 256 - CODEPFL) code_pf();
  asm volatile("" :: "s"(x), "s"(K), "s"(V), "s"(wq), "s"(wo), "s"(wg), "s"(wu), "s"(wd), "s"(c), "s"(wmod), "s"(out), "s"(ws));
  const rsrc_t rw = mkrsrc(ws, WS_BYTES);
  const unsigned ew = __builtin_amdgcn_raw_buffer_load_b32(rw, 0, 0, AUX_DEV);
  const bool isM1 = slot < 5, isQ = slot >= 5 && slot < 13, isF = slot >= 13 && slot < 20 && 7 * xc + slot - 13 < 54, isW = slot >= 20 && slot < 30;
  unsigned tag = 0;
  int nch, ch0;
  {
    const int cl = slot & 3, k = slot >> 2;
    int e = (int)((xpack() >> (4 * xc)) & 15) - 8, pre = 0;
#pragma unroll
    for (int k2 = 0; k2 < 7; ++k2) pre += k2 < k ? (int)((xpack() >> (4 * ((k2 - (head >> 2)) & 7))) & 15) - 8 : 0;
    const unsigned long long tc = cl == 0 ? kcount(0) : cl == 1 ? kcount(1) : cl == 2 ? kcount(2) : kcount(3);
    const unsigned long long ts = cl == 0 ? kstart(0) : cl == 1 ? kstart(1) : cl == 2 ? kstart(2) : kstart(3);
    nch = 64 + (int)((tc >> (8 * k)) & 255) + e;
    ch0 = 128 * k - 64 + (int)((ts >> (8 * k)) & 255) + pre;
  }
  unsigned porg = pt0;
  const int pend = PACE0 - (isW ? PACE1 : 0);
  int prem = 2 * nch - nch / NIT, ptb = 0, ptau = 0;
  unsigned pnow = pt0;
  auto plan = [&](int c, bool common) {
    if (common) {
      const v2u tr = *(const v2u*)(smem + L_DUMMY + 256 + w * 64 + (lane & 7) * 8);
      const unsigned tt = tr[0], tg = tr[1];
      const bool okt = tg == tag && (unsigned)(tt - pt0 + 300) < 600u;
      int d = okt ? (int)(tt - pt0) : 1000;
      d = min(d, __builtin_amdgcn_mov_dpp(d, XOR1, 0xf, 0xf, true)); d = min(d, __builtin_amdgcn_mov_dpp(d, XOR2, 0xf, 0xf, true));
      d = min(d, __builtin_amdgcn_mov_dpp(d, XOR7, 0xf, 0xf, true));
      d = __builtin_amdgcn_readfirstlane(d);
      if (d < 1000) porg = pt0 + d;
    }
    pnow = (unsigned)__builtin_amdgcn_s_memrealtime();
    const int t = (int)(pnow - porg), left = pend - t;
    ptb = t * 64;
    ptau = left > 0 ? __builtin_amdgcn_readfirstlane((int)((float)(left * 64) * __builtin_amdgcn_rcpf((float)prem))) : 0;
    prem -= c;
  };
  auto pace = [&](int j) {
    const int tg = (ptb + j * ptau) >> 6;
    if ((int)(pnow - porg) < tg) {
#pragma nounroll
      for (int spin = 0; spin < 4096; ++spin) { if ((int)((unsigned)__builtin_amdgcn_s_memrealtime() - porg) >= tg) break; __builtin_amdgcn_s_sleep(1); }
    }
  };
  const int nkeys = 16 * nch, key0 = 16 * ch0;
  auto cnt = [&](int it) { return (nch + NIT - 1 - it) / NIT; };
  const rsrc_t rk = mkrsrc(K + ((size_t)head * SK + key0) * 128, nkeys * 256);
  const rsrc_t rv = mkrsrc(V + ((size_t)head * SK + key0) * 128, nkeys * 256);
  const int voff = lane * 16 + w * 1024;
  v4u a[WIN];
  unsigned sw[5];

  if (isM1) {
    const int mi = 5 * xc + slot, t = mi >= 20, g32 = mi - 20 * t, rho = lane >> 2, cq = lane & 3;
    const rsrc_t rm = mkrsrc(wmod, D * 6 * D * 2);
    const unsigned* cw = (const unsigned*)c + 80 * w + 5 * rho;
    unsigned cv[5];
#pragma unroll
    for (int i = 0; i < 5; ++i) cv[i] = cw[i];
    const int vo = (160 * w + 10 * rho) * 7680 + (t * D + 32 * g32 + 8 * cq) * 2;
    v4u wv[10];
#pragma unroll
    for (int i = 0; i < 10; ++i) wv[i] = __builtin_amdgcn_raw_buffer_load_b128(rm, vo, i * 7680, 0);
    __builtin_amdgcn_sched_barrier(0);
#pragma unroll
    for (int i = 0; i < 5; ++i) sw[i] = pack2(silu(lo16(cv[i])), silu(hi16(cv[i])));
    float acc[8] = {0, 0, 0, 0, 0, 0, 0, 0};
#pragma unroll
    for (int i = 0; i < 5; ++i) {
      const P8 p = mkpairs(wv[2 * i], wv[2 * i + 1]);
#pragma unroll
      for (int j = 0; j < 8; ++j) acc[j] = dot2(p.p[j], sw[i], acc[j]);
    }
    float r = red8(acc, lane);
    r += dpp<SHL4>(r);
    float* red = (float*)(smem + L_RED);
    if ((lane & 4) == 0) red[w * 32 + cq * 8 + red8col(lane)] = r;
    bar();
    if (w == 0) {
      const int g = lane & 7, rep = lane >> 3;
      const f32x4 a0 = *(const f32x4*)(red + 4 * g), a1 = *(const f32x4*)(red + 32 + 4 * g), a2 = *(const f32x4*)(red + 64 + 4 * g), a3 = *(const f32x4*)(red + 96 + 4 * g);
      f32x4 s = (a0 + a1) + (a2 + a3);
      if (t) { s[0] = 1.f + rbf(s[0]); s[1] = 1.f + rbf(s[1]); s[2] = 1.f + rbf(s[2]); s[3] = 1.f + rbf(s[3]); }
      tag = __builtin_amdgcn_readfirstlane(ew) + 1;
      if (rep < RH) stg(rw, OFF_H + rep * 5120 + (t * 160 + 8 * g32 + g) * 16, pack2(s[0], s[1]), pack2(s[2], s[3]), tag);
      if (slot == 0 && lane < 8) __builtin_amdgcn_raw_buffer_store_b64(v2u{pt0, tag}, rw, OFF_T + lane * 256 + xc * 8, 0, AUX_DEV);
    }
  } else if (isQ) {
    const int qi = 8 * xc + slot - 5, rho = lane >> 3, cq = lane & 7, lc = lane < 40 ? lane : 39;
    const rsrc_t rq = mkrsrc(wq, D * 4096 * 2);
    unsigned xv[5];
#pragma unroll
    for (int i = 0; i < 5; ++i) xv[i] = ((const unsigned*)x)[5 * lane + i];
    const v2u xo = *(const v2u*)(x + 160 * w + 4 * lc);
    const int vo = (160 * w + 20 * rho) * 8192 + (64 * qi + 8 * cq) * 2;
    v4u wv[20];
#pragma unroll
    for (int i = 0; i < 20; ++i) wv[i] = __builtin_amdgcn_raw_buffer_load_b128(rq, vo, i * 8192, AUX_NT);
    __builtin_amdgcn_sched_barrier(0);
    float s = 0;
#pragma unroll
    for (int i = 0; i < 5; ++i) s += lo16(xv[i]) + hi16(xv[i]);
    const float mean = wsum(s) * (1.f / D);
    float v = 0;
#pragma unroll
    for (int i = 0; i < 5; ++i) { const float d0 = lo16(xv[i]) - mean, d1 = hi16(xv[i]) - mean; v += d0 * d0 + d1 * d1; }
    const float var = wsum(v) * (1.f / D);
    const float rs = __builtin_amdgcn_rcpf(rbf(__builtin_amdgcn_sqrtf(rbf(rbf(var) + 1e-5f)))), mb = rbf(mean);
    const unsigned xo0 = xo[0], xo1 = xo[1];
    float n0 = rbf(rbf(lo16(xo0) - mb) * rs), n1 = rbf(rbf(hi16(xo0) - mb) * rs), n2 = rbf(rbf(lo16(xo1) - mb) * rs), n3 = rbf(rbf(hi16(xo1) - mb) * rs);
    pin(n0); pin(n1); pin(n2); pin(n3);
    unsigned pr[80];
#pragma unroll
    for (int i = 0; i < 10; ++i) {
      const P8 p = mkpairs(wv[2 * i], wv[2 * i + 1]);
#pragma unroll
      for (int j = 0; j < 8; ++j) pr[8 * i + j] = p.p[j];
    }
#pragma unroll
    for (int j = 0; j < 80; ++j) pin(pr[j]);
    tag = __builtin_amdgcn_readfirstlane(ew) + 1;
    v4u gs = {0, 0, 0, 0}, gc = {0, 0, 0, 0};
    const int go = OFF_H + (xc % RH) * 5120 + (40 * w + lc) * 16;
    {
      u64 m0 = lanes(true), m1 = m0;
#pragma nounroll
      for (int spin = 0; spin < SPIN_CAP; ++spin) {
        grow2(gs, gc, m0, m1, go, go + 2560, rw);
        const unsigned b0 = gbad(gs, tag), b1 = gbad(gc, tag);
        if (lanes((b0 | b1) != 0) == 0) break;
        m0 = lanes(b0 != 0); m1 = lanes(b1 != 0);
      }
    }
    unsigned* hw = (unsigned*)(smem + L_HW) + w * 192;
    {
      const unsigned s0 = gs[0], s1 = gs[2], c0 = gc[0], c1 = gc[2];
      const v2u hh = {pack2(rbf(n0 * lo16(c0)) + lo16(s0), rbf(n1 * hi16(c0)) + hi16(s0)), pack2(rbf(n2 * lo16(c1)) + lo16(s1), rbf(n3 * hi16(c1)) + hi16(s1))};
      *(v2u*)(hw + 2 * lc) = hh;
    }
    fence_c();
    float acc[8] = {0, 0, 0, 0, 0, 0, 0, 0};
#pragma unroll
    for (int i = 0; i < 5; ++i) {
      const v2u hv = *(const v2u*)(hw + 10 * rho + 2 * i);
      const unsigned h0 = hv[0], h1 = hv[1];
#pragma unroll
      for (int j = 0; j < 8; ++j) acc[j] = dot2(pr[16 * i + j], h0, acc[j]);
#pragma unroll
      for (int j = 0; j < 8; ++j) acc[j] = dot2(pr[16 * i + 8 + j], h1, acc[j]);
    }
    const float r = red8(acc, lane);
    float* red = (float*)(smem + L_RED);
    red[w * 64 + cq * 8 + red8col(lane)] = r;
    bar();
    if (w == 0 && lane < 16) {
      const f32x4 a0 = *(const f32x4*)(red + 4 * lane), a1 = *(const f32x4*)(red + 64 + 4 * lane), a2 = *(const f32x4*)(red + 128 + 4 * lane), a3 = *(const f32x4*)(red + 192 + 4 * lane);
      const f32x4 s = (a0 + a1) + (a2 + a3);
      stg(rw, OFF_Q + (16 * qi + lane) * 16, pack2(s[0], s[1]), pack2(s[2], s[3]), tag);
    }
  }
  __builtin_amdgcn_sched_barrier(0);

  unsigned cvm[5];
  if (isW) {
#pragma unroll
    for (int i = 0; i < 5; ++i) cvm[i] = ((const unsigned*)c)[80 * w + 5 * (lane >> 2) + i];
  }
  __builtin_amdgcn_sched_barrier(0);
  if (!isM1 && !isQ) __builtin_amdgcn_s_sleep(BULK_DELAY);
  {
    const int c0 = cnt(0);
#pragma unroll
    for (int i = 0; i < WIN; ++i) {
      if (i >= ZMIN) a[i] = v4u{0, 0, 0, 0};
      if (i < ZMIN || i < c0) a[i] = __builtin_amdgcn_raw_buffer_load_b128(rk, voff, i * 4096, AUX_NT);
      if (i % 4 == 3) __builtin_amdgcn_sched_barrier(0);
    }
  }
  __builtin_amdgcn_sched_barrier(0);

  if (isW) {
#pragma unroll
    for (int i = 0; i < 5; ++i) sw[i] = pack2(silu(lo16(cvm[i])), silu(hi16(cvm[i])));
  }
  tag = __builtin_amdgcn_readfirstlane(ew) + 1;
  const int r4 = lane >> 4, cc = lane & 15;
  v4u qp;
  {
    v4u g0, g1;
    const int go = OFF_Q + head * 512 + cc * 32;
    bool got = false;
    if (isW || isF) {
      rsrc_t r0, r1, r2; unsigned v0, v1, v2, s1, s2, st0, st1, st2, l0, l1, l2;
      if (isW) {
        const int C = slot - 20, m2 = 10 * xc + C;
        r0 = mkrsrc(wmod, D * 6 * D * 2); r1 = r2 = mkrsrc(wo, 4096 * D * 2);
        v0 = (160 * w + 10 * (lane >> 2)) * 7680 + (2 * D + 32 * m2 + 8 * (lane & 3)) * 2;
        v1 = v2 = ((512 * xc + 128 * w + 16 * (lane >> 3)) * D + 64 * C + 8 * (lane & 7)) * 2;
        st0 = 7680; st1 = st2 = 1280; s1 = 0; s2 = 8 * 1280;
        l0 = ldsb + L_W + 65536 + w * 10240; l1 = ldsb + L_W + w * 16384; l2 = l1 + 8192;
      } else {
        const int fj = 7 * xc + slot - 13;
        r0 = r1 = mkrsrc(w < 2 ? wg : wu, D * F * 2); r2 = mkrsrc(wd, F * D * 2);
        v0 = v1 = ((320 * (w & 1) + 20 * (lane >> 2)) * F + 32 * fj + 8 * (lane & 3)) * 2;
        v2 = (32 * fj * D) * 2 + w * 10240 + lane * 16;
        st0 = st1 = F * 2; st2 = 1024; s1 = 10 * F * 2; s2 = 0;
        l0 = ldsb + L_W + w * 20480; l1 = l0 + 10240; l2 = ldsb + L_W + 81920 + w * 10240;
      }
      l0 = __builtin_amdgcn_readfirstlane(l0); l1 = __builtin_amdgcn_readfirstlane(l1); l2 = __builtin_amdgcn_readfirstlane(l2);
      if (isW) { poll_dma<10, false>(g0, g1, go, rw, r0, v0, 0, st0, l0); got = __all(okg(g0, tag) && okg(g1, tag)); }
      if (isW) {
        if (!got) { poll_dma<8, true>(g0, g1, go, rw, r1, v1, s1, st1, l1); got = __all(okg(g0, tag) && okg(g1, tag)); }
        else dma_batch<8, true>(r1, v1, s1, st1, l1);
        if (!got) { poll_dma<8, true>(g0, g1, go, rw, r2, v2, s2, st2, l2); got = __all(okg(g0, tag) && okg(g1, tag)); }
        else dma_batch<8, true>(r2, v2, s2, st2, l2);
      } else {
        if (!got) { poll_dma<10, true>(g0, g1, go, rw, r2, v2, s2, st2, l2); got = __all(okg(g0, tag) && okg(g1, tag)); }
        else dma_batch<10, true>(r2, v2, s2, st2, l2);
      }
    }
    if (!got) {
#pragma nounroll
      for (int spin = 0; spin < SPIN_CAP; ++spin) {
        fence_c();
        g0 = ldg(rw, go); g1 = ldg(rw, go + 16);
        const bool okq = __all(okg(g0, tag) && okg(g1, tag));
        if (okq) break;
        __builtin_amdgcn_s_sleep(1);
      }
    }
    qp[0] = g0[0]; qp[1] = g0[2]; qp[2] = g1[0]; qp[3] = g1[2];
  }
  if (lane < 16) {
    unsigned keep;
    asm volatile("s_mov_b32 %0, m0\n\ts_mov_b32 m0, %1\n\ts_nop 0\n\tbuffer_load_dword %2, %3, 0 offen sc1 lds\n\ts_mov_b32 m0, %0"
                 : "=&s"(keep) : "s"(__builtin_amdgcn_readfirstlane(ldsb + L_DUMMY + 256 + w * 64)), "v"(OFF_T + xc * 256 + lane * 4), "s"(rw) : "memory");
  }
  __builtin_amdgcn_sched_barrier(0);

  float* sp = (float*)(smem + L_SP) + (w * 4 + r4) * NLMAX;
  float mx = -INFINITY;
  int so = 0;
#pragma unroll 1
  for (int it = 0; it < NIT; ++it) {
    const bool lastk = it == NIT - 1;
    const int c = cnt(it), r = cnt(lastk ? 0 : it + 1);
    so = lastk ? 0 : so + c * 4096;
    const rsrc_t rr = lastk ? rv : rk;
    plan(c, it >= 1);
#define TR_STEP(HALF, CTRL, BIT) _Pragma("unroll") for (int hh = 0; hh < (HALF); ++hh) { \
      const float lo_ = xs[hh], hi_ = xs[hh + (HALF)]; const float t1_ = lo_ + dpp<CTRL>(lo_), t2_ = hi_ + dpp<CTRL>(hi_); xs[hh] = (cc & (BIT)) ? t2_ : t1_; }
#define K_GROUP(J0, N) float xs[N]; \
      _Pragma("unroll") for (int t = 0; t < (N); ++t) { \
        const int j = (J0) + t; \
        float d = 0; \
        if (j < WIN) { \
          if (j % PG == 0 && (j < ZMIN || j < c)) pace(j); \
          if (j < ZMIN || j < c) { _Pragma("unroll") for (int jj = 0; jj < 4; ++jj) d = dot2(a[j < WIN ? j : 0][jj], qp[jj], d); } \
          asm volatile("" : "+v"(d)); \
          __builtin_amdgcn_sched_barrier(0); \
          if (j < ZMIN || j < r) a[j < WIN ? j : 0] = __builtin_amdgcn_raw_buffer_load_b128(rr, voff, so + j * 4096, AUX_NT); \
          if (j % PG == 1 % PG) pnow = (unsigned)__builtin_amdgcn_s_memrealtime(); \
          __builtin_amdgcn_sched_barrier(0); \
        } \
        xs[t] = d; \
      }
#pragma unroll
    for (int g = 0; g < 2; ++g) {
      K_GROUP(g * 16, 16)
      TR_STEP(8, XOR8, 8) TR_STEP(4, XOR7, 4) TR_STEP(2, XOR2, 2) TR_STEP(1, XOR1, 1)
      const int j = g * 16 + cc;
      const float sc = j < c ? rbf(rbf(xs[0]) * 0.08838834764831845f) : -INFINITY;
      mx = fmaxf(mx, sc);
      sp[it * SPW + j] = sc;
    }
    if (SPW > 32) {
      K_GROUP(32, 4)
#pragma unroll
      for (int t = 0; t < 4; ++t) { xs[t] += dpp<XOR8>(xs[t]); xs[t] += dpp<XOR7>(xs[t]); }
      TR_STEP(2, XOR2, 2) TR_STEP(1, XOR1, 1)
      const int j = 32 + (cc & 3);
      const float sc = j < c ? rbf(rbf(xs[0]) * 0.08838834764831845f) : -INFINITY;
      mx = fmaxf(mx, sc);
      if (cc < 4) sp[it * SPW + j] = sc;
    }
#undef K_GROUP
#undef TR_STEP

  }
  const int po = OFF_P + (head * 8 + xc) * PSTR;
  {
    float* sml = (float*)(smem + L_ML);
    mx = wmax(mx);
    if (lane == 0) sml[w] = mx;
    bar();
    const f32x4 m4 = *(const f32x4*)sml;
    mx = fmaxf(fmaxf(m4[0], m4[1]), fmaxf(m4[2], m4[3]));
    float l = 0;
#pragma unroll
    for (int g = 0; g < NLMAX / 16; ++g) { const float p = __expf(sp[g * 16 + cc] - mx); l += p; sp[g * 16 + cc] = p; }
    l = wsum(l);
    if (lane == 0) sml[4 + w] = l;
    bar();
    if (tid == 0) {
      const f32x4 l4 = *(const f32x4*)(sml + 4);
      stg(rw, po + 64 * 16, asu(mx), asu((l4[0] + l4[1]) + (l4[2] + l4[3])), tag);
    }
  }

  if (isW) {
    const int m2 = 10 * xc + slot - 20, cq = lane & 3;
    const v4u* wl = (const v4u*)(smem + L_W + 65536 + w * 10240) + lane;
    float acc[8] = {0, 0, 0, 0, 0, 0, 0, 0};
#pragma unroll
    for (int i = 0; i < 5; ++i) {
      const P8 p = mkpairs(wl[(2 * i) * 64], wl[(2 * i + 1) * 64]);
#pragma unroll
      for (int j = 0; j < 8; ++j) acc[j] = dot2(p.p[j], sw[i], acc[j]);
    }
    float r = red8(acc, lane);
    r += dpp<SHL4>(r);
    float* red = (float*)(smem + L_RED);
    if ((lane & 4) == 0) red[w * 32 + cq * 8 + red8col(lane)] = r;
    bar();
    if (w == 0) {
      const int g = lane & 7, rep = lane >> 3;
      const f32x4 a0 = *(const f32x4*)(red + 4 * g), a1 = *(const f32x4*)(red + 32 + 4 * g), a2 = *(const f32x4*)(red + 64 + 4 * g), a3 = *(const f32x4*)(red + 96 + 4 * g);
      const f32x4 s = (a0 + a1) + (a2 + a3);
      if (rep < RM) stg(rw, OFF_M + rep * 10240 + (8 * m2 + g) * 16, pack2(s[0], s[1]), pack2(s[2], s[3]), tag);
    }
    bar();
  }

  float acc[8] = {0, 0, 0, 0, 0, 0, 0, 0};
#define V_SLOTS(LOAD) _Pragma("unroll") for (int g = 0; g < (WIN + 3) / 4; ++g) { \
      const f32x4 p4 = *(const f32x4*)&sp[it * SPW + g * 4]; \
      _Pragma("unroll") for (int t = 0; t < 4; ++t) { \
        const int j = g * 4 + t; \
        if (j < WIN) { \
          const float pp = p4[t]; \
          if (LOAD && j % PG == 0 && (j < ZMIN || j < c)) pace(j); \
          if (j < ZMIN || j < c) { \
            _Pragma("unroll") for (int jj = 0; jj < 4; ++jj) { const unsigned wv = a[j < WIN ? j : 0][jj]; acc[2 * jj] += pp * lo16(wv); acc[2 * jj + 1] += pp * hi16(wv); } \
          } \
          asm volatile("" : "+v"(acc[0]), "+v"(acc[1]), "+v"(acc[2]), "+v"(acc[3]), "+v"(acc[4]), "+v"(acc[5]), "+v"(acc[6]), "+v"(acc[7])); \
          __builtin_amdgcn_sched_barrier(0); \
          if (LOAD && (j < ZMIN || j < r)) a[j < WIN ? j : 0] = __builtin_amdgcn_raw_buffer_load_b128(rv, voff, so + j * 4096, AUX_NT); \
          if (LOAD && j % PG == 1 % PG) pnow = (unsigned)__builtin_amdgcn_s_memrealtime(); \
          __builtin_amdgcn_sched_barrier(0); \
        } \
      } \
    }
  so = 0;
#pragma unroll 1
  for (int it = 0; it < NIT - 1; ++it) {
    const int c = cnt(it), r = cnt(it + 1);
    so += c * 4096;
    plan(c, false);
    V_SLOTS(true)
  }
  v4u ml = {0, 0, 0, 0};
  const int hpo = OFF_P + (4 * xc + w) * 8 * PSTR, mlo = hpo + (lane & 7) * PSTR + 64 * 16;
  if (isW) ml = ldg(rw, mlo);
  {
    const int it = NIT - 1, c = cnt(it), r = 0;
    V_SLOTS(false)
  }
#undef V_SLOTS
  {
    const float b0 = tr32(acc[0], acc[4]), b1 = tr32(acc[1], acc[5]), b2 = tr32(acc[2], acc[6]), b3 = tr32(acc[3], acc[7]);
    const f32x2 o = {tr16(b0, b2), tr16(b1, b3)};
    constexpr int SPWAVE = 4 * NLMAX;
    f32x2* pp = (f32x2*)(smem + L_SP) + lane;
    pp[w * (SPWAVE / 2)] = o;
    bar();
    if (w == 0) {
      const f32x2 t = (pp[0] + pp[SPWAVE / 2]) + (pp[SPWAVE] + pp[3 * SPWAVE / 2]);
      stg(rw, po + (4 * cc + r4) * 16, asu(t[0]), asu(t[1]), tag);
    }
  }

  if (isW) {
    const int C = slot - 20, cq = lane & 7;
    const int v0 = hpo + lane * 16, v1 = v0 + 4 * PSTR;
    v4u g[8];
    const v4u* wl = (const v4u*)(smem + L_W + w * 16384) + lane;
    unsigned pr[64];
#pragma unroll
    for (int i = 0; i < 8; ++i) {
      const P8 p = mkpairs(wl[(2 * i) * 64], wl[(2 * i + 1) * 64]);
#pragma unroll
      for (int j = 0; j < 8; ++j) { pr[8 * i + j] = p.p[j]; pin(pr[8 * i + j]); }
    }
#pragma nounroll
    for (int spin = 0; spin < SPIN_CAP; ++spin) {
      const u64 mb = lanes(gbad(ml, tag) != 0);
      if (mb == 0) break;
      grow1(ml, mb, mlo, rw);
    }
    float fs[8];
    {
      const unsigned mu = ml[0], lu = ml[2];
      const float ms = asf(mu);
      float M = fmaxf(ms, dpp<XOR1>(ms)); M = fmaxf(M, dpp<XOR2>(M)); M = fmaxf(M, dpp<XOR7>(M));
      const float e = __expf(ms - M);
      float L = e * asf(lu); L += dpp<XOR1>(L); L += dpp<XOR2>(L); L += dpp<XOR7>(L);
      const float f = e * __builtin_amdgcn_rcpf(L);
#pragma unroll
      for (int s = 0; s < 8; ++s) { fs[s] = asf(__builtin_amdgcn_readlane(asu(f), s)); pins(fs[s]); }
    }
    unsigned* hw = (unsigned*)(smem + L_HW) + w * 192;
    float* red = (float*)(smem + L_RED);
#pragma nounroll
    for (int spin = 0; spin < 4096; ++spin) { if ((int)((unsigned)__builtin_amdgcn_s_memrealtime() - porg) >= PACE0 + PGATE) break; __builtin_amdgcn_s_sleep(1); }
#pragma unroll
    for (int s = 0; s < 8; ++s) g[s] = ldg(rw, (s < 4 ? v0 : v1) + (s & 3) * PSTR);
#pragma nounroll
    for (int spin = 0; spin < SPIN_CAP; ++spin) {
      unsigned bd[8];
      u64 m[8];
#pragma unroll
      for (int s = 0; s < 8; ++s) bd[s] = gbad(g[s], tag);
      if (lanes(((bd[0] | bd[1] | bd[2]) | (bd[3] | bd[4] | bd[5]) | (bd[6] | bd[7])) != 0) == 0) break;
#pragma unroll
      for (int s = 0; s < 8; ++s) m[s] = lanes(bd[s] != 0);
      grow8(g, m, v0, v1, rw);
    }
    float o0, o1;
    { const unsigned u0 = g[0][0], u1 = g[0][2]; o0 = fs[0] * asf(u0); o1 = fs[0] * asf(u1); }
#pragma unroll
    for (int s = 1; s < 8; ++s) { const unsigned u0 = g[s][0], u1 = g[s][2]; o0 += fs[s] * asf(u0); o1 += fs[s] * asf(u1); }
    hw[lane] = pack2(o0, o1);
    fence_c();
    const v4u h0 = *(const v4u*)(hw + 8 * (lane >> 3)), h1 = *(const v4u*)(hw + 8 * (lane >> 3) + 4);
    float y[8] = {0, 0, 0, 0, 0, 0, 0, 0};
#pragma unroll
    for (int i = 0; i < 8; ++i) {
      const unsigned hv = i < 4 ? h0[i & 3] : h1[i & 3];
#pragma unroll
      for (int j = 0; j < 8; ++j) y[j] = dot2(pr[8 * i + j], hv, y[j]);
    }
    const float r = red8(y, lane);
    red[w * 64 + cq * 8 + red8col(lane)] = r;
    bar();
    if (w == 0 && lane < 32) {
      const float* rp = red + 2 * lane;
      const float y0 = (rp[0] + rp[64]) + (rp[128] + rp[192]), y1 = (rp[1] + rp[65]) + (rp[129] + rp[193]);
      stg(rw, OFF_Y + xc * 5120 + plane(32 * C + lane), asu(y0), asu(y1), tag);
    }
  }
  int roleF = __builtin_amdgcn_readfirstlane(7 * xc + slot - 13 < 54 ? slot - 13 : 7), roleE = __builtin_amdgcn_readfirstlane(slot);
  asm volatile("" : "+s"(roleF), "+s"(roleE));
  if ((unsigned)roleF < 7u) {
    const int fj = 7 * xc + slot - 13, wh = w & 1, rho = lane >> 2, cq = lane & 3;
    const int tcl = tid < 160 ? tid : 0, mo = OFF_M + (xc % RM) * 10240 + tcl * 16;
    v4u ga = {0, 0, 0, 0}, sb = ga, cb = ga;
    const v2u xw = *(const v2u*)(x + 4 * tcl);
    {
      ga = ldg(rw, mo); sb = ldg(rw, mo + 2560); cb = ldg(rw, mo + 5120);
      __builtin_amdgcn_sched_barrier(0);
      const rsrc_t rgu = mkrsrc(w < 2 ? wg : wu, D * F * 2);
      const int vgu = ((320 * wh + 20 * rho) * F + 32 * fj + 8 * cq) * 2;
#pragma unroll
      for (int j = 0; j < 20; ++j) a[j] = __builtin_amdgcn_raw_buffer_load_b128(rgu, vgu, j * F * 2, 0);
      __builtin_amdgcn_sched_barrier(0);
    }
    float* x1s = (float*)(smem + L_X1); float* cbs = (float*)(smem + L_CB); float* sbs = (float*)(smem + L_SB);
    float gaf[4], xf[4];
    auto mods = [&]() {
      u64 m0 = lanes(true), m1 = m0, m2 = m0;
      bool first = true;
#pragma nounroll
      for (int spin = 0; spin < SPIN_CAP; ++spin) {
        if (!first) grow3(ga, sb, cb, m0, m1, m2, mo, mo + 2560, mo + 5120, rw);
        first = false;
        const unsigned b0 = gbad(ga, tag), b1 = gbad(sb, tag), b2 = gbad(cb, tag);
        if (lanes((b0 | b1 | b2) != 0) == 0) break;
        m0 = lanes(b0 != 0); m1 = lanes(b1 != 0); m2 = lanes(b2 != 0);
      }
      const unsigned c0 = cb[0], c1 = cb[2], s0 = sb[0], s1 = sb[2], g0 = ga[0], g1 = ga[2], x0 = xw[0], x1w = xw[1];
      if (tid < 160) {
        *(f32x4*)(cbs + 4 * tid) = f32x4{rbf(1.f + lo16(c0)), rbf(1.f + hi16(c0)), rbf(1.f + lo16(c1)), rbf(1.f + hi16(c1))};
        *(f32x4*)(sbs + 4 * tid) = f32x4{lo16(s0), hi16(s0), lo16(s1), hi16(s1)};
      }
      gaf[0] = lo16(g0); gaf[1] = hi16(g0); gaf[2] = lo16(g1); gaf[3] = hi16(g1);
      xf[0] = lo16(x0); xf[1] = hi16(x0); xf[2] = lo16(x1w); xf[3] = hi16(x1w);
#pragma unroll
      for (int j = 0; j < 4; ++j) { pin(gaf[j]); pin(xf[j]); }
    };
    mods();
    __builtin_amdgcn_sched_barrier(0);
    v4u pr[20], pd[16];
    {
      const int tc = tid < 160 ? tid : 0;
      const v2u* dl = (const v2u*)(smem + L_W + 81920) + tc;
#pragma unroll
      for (int i = 0; i < 16; ++i) {
        const v2u r0 = dl[(2 * i) * 160], r1 = dl[(2 * i + 1) * 160];
        const unsigned a0 = r0[0], a1 = r0[1], b0 = r1[0], b1 = r1[1];
        pd[i] = v4u{plo(a0, b0), phi(a0, b0), plo(a1, b1), phi(a1, b1)};
        pin(pd[i]);
      }
      __builtin_amdgcn_sched_barrier(0);
#pragma unroll
      for (int i = 0; i < 10; ++i) {
        const P8 p = mkpairs(a[2 * i], a[2 * i + 1]);
        pr[2 * i] = v4u{p.p[0], p.p[1], p.p[2], p.p[3]}; pr[2 * i + 1] = v4u{p.p[4], p.p[5], p.p[6], p.p[7]};
        pin(pr[2 * i]); pin(pr[2 * i + 1]);
      }
    }
    if (tid < 160) {
      v4u g[16];
      u64 m[8];
#pragma unroll
      for (int G = 0; G < 8; ++G) { g[2 * G] = v4u{0, 0, 0, 0}; g[2 * G + 1] = v4u{0, 0, 0, 0}; m[G] = lanes(true); }
#pragma nounroll
      for (int spin = 0; spin < SPIN_CAP; ++spin) {
        grow16(g, m, OFF_Y + tid * 16, rw);
        unsigned bd[8];
#pragma unroll
        for (int G = 0; G < 8; ++G) bd[G] = gbad(g[2 * G], tag) | gbad(g[2 * G + 1], tag);
        if (lanes(((bd[0] | bd[1] | bd[2]) | (bd[3] | bd[4] | bd[5]) | (bd[6] | bd[7])) != 0) == 0) break;
#pragma unroll
        for (int G = 0; G < 8; ++G) m[G] = lanes(bd[G] != 0);
      }
      float y[4];
      { const unsigned u0 = g[0][0], u1 = g[0][2], u2 = g[1][0], u3 = g[1][2]; y[0] = asf(u0); y[1] = asf(u1); y[2] = asf(u2); y[3] = asf(u3); }
#pragma unroll
      for (int G = 1; G < 8; ++G) { const unsigned u0 = g[2 * G][0], u1 = g[2 * G][2], u2 = g[2 * G + 1][0], u3 = g[2 * G + 1][2]; y[0] += asf(u0); y[1] += asf(u1); y[2] += asf(u2); y[3] += asf(u3); }
      *(f32x4*)(x1s + 4 * tid) = f32x4{rbf(xf[0] + rbf(gaf[0] * rbf(y[0]))), rbf(xf[1] + rbf(gaf[1] * rbf(y[1]))),
                                       rbf(xf[2] + rbf(gaf[2] * rbf(y[2]))), rbf(xf[3] + rbf(gaf[3] * rbf(y[3])))};
    }
    bar();
    float xa[5], xb[5], cbv[5], sbv[5];
#pragma unroll
    for (int i = 0; i < 5; ++i) { xa[i] = x1s[5 * lane + i]; xb[i] = x1s[320 + 5 * lane + i]; }
    const float sh = x1s[0];
#pragma unroll
    for (int i = 0; i < 5; ++i) { cbv[i] = cbs[320 * wh + 5 * lane + i]; sbv[i] = sbs[320 * wh + 5 * lane + i]; }
    float s1 = 0, s2 = 0;
#pragma unroll
    for (int i = 0; i < 5; ++i) { const float d0 = xa[i] - sh, d1 = xb[i] - sh; s1 += d0 + d1; s2 += d0 * d0; s2 += d1 * d1; }
    wsum2(s1, s2);
    const float md = s1 * (1.f / D), mean = sh + md, var = fmaxf(s2 * (1.f / D) - md * md, 0.f);
    const float rs = __builtin_amdgcn_rcpf(rbf(__builtin_amdgcn_sqrtf(rbf(rbf(var) + 1e-5f)))), mb = rbf(mean);
    u16* hs = (u16*)((unsigned*)(smem + L_HW) + w * 192);
#pragma unroll
    for (int i = 0; i < 5; ++i) {
      const float xx = wh ? xb[i] : xa[i];
      hs[5 * lane + i] = (u16)pack2(rbf(rbf(rbf(xx - mb) * rs) * cbv[i]) + sbv[i], 0.f);
    }
    fence_c();
    const v2u* hp = (const v2u*)((const unsigned*)hs + 10 * rho);
    float ga8[8] = {0, 0, 0, 0, 0, 0, 0, 0};
#pragma unroll
    for (int i = 0; i < 5; ++i) {
      const v2u hv = hp[i];
      const unsigned h0 = hv[0], h1 = hv[1];
#pragma unroll
      for (int j = 0; j < 8; ++j) { const unsigned pw = pr[4 * i + (j >> 2)][j & 3]; ga8[j] = dot2(pw, h0, ga8[j]); }
#pragma unroll
      for (int j = 0; j < 8; ++j) { const unsigned pw = pr[4 * i + 2 + (j >> 2)][j & 3]; ga8[j] = dot2(pw, h1, ga8[j]); }
    }
    float r = red8(ga8, lane);
    r += dpp<SHL4>(r);
    float* red = (float*)(smem + L_RED);
    if ((lane & 4) == 0) red[w * 32 + cq * 8 + red8col(lane)] = r;
    bar();
    const int col = lane & 31;
    const float gg = rbf(red[col] + red[32 + col]), uu = rbf(red[64 + col] + red[96 + col]);
    const float av = rbf(rbf(silu(gg)) * uu);
    const unsigned aw = (asu(av) >> 16) | (asu(dpp<XOR1>(av)) & 0xffff0000u);
    float z[4] = {0, 0, 0, 0};
#pragma unroll
    for (int i = 0; i < 16; ++i) {
      const unsigned as = __builtin_amdgcn_readlane(aw, 2 * i);
#pragma unroll
      for (int j = 0; j < 4; ++j) { const unsigned pw = pd[i][j]; z[j] = dot2(pw, as, z[j]); }
    }
    if (tid < 160) {
      stg(rw, OFF_Z + fj * 5120 + tid * 16, asu(z[0]), asu(z[1]), tag);
      stg(rw, OFF_Z + fj * 5120 + 2560 + tid * 16, asu(z[2]), asu(z[3]), tag);
    }
  }
  if (roleE < 5) {
    const int fi = 5 * xc + slot, g8 = lane & 7, pj = plane(8 * fi + g8);
    float x1a = 0, x1b = 0, gb0 = 0, gb1 = 0;
    if (w == 0) {
      const int mo = OFF_M + (xc % RM) * 10240 + (4 * fi + (g8 >> 1)) * 16;
      v4u ga = {0, 0, 0, 0}, gb = ga, yy = ga;
      u64 m0 = lanes(true), m1 = m0, m2 = m0;
#pragma nounroll
      for (int spin = 0; spin < SPIN_CAP; ++spin) {
        grow3(ga, gb, yy, m0, m1, m2, mo, mo + 7680, OFF_Y + (lane >> 3) * 5120 + pj, rw);
        const unsigned b0 = gbad(ga, tag), b1 = gbad(gb, tag), b2 = gbad(yy, tag);
        if (lanes((b0 | b1 | b2) != 0) == 0) break;
        m0 = lanes(b0 != 0); m1 = lanes(b1 != 0); m2 = lanes(b2 != 0);
        __builtin_amdgcn_s_sleep(1);
      }
      const unsigned xw = ((const unsigned*)x)[8 * fi + g8];
      const unsigned gaw = (g8 & 1) ? ga[2] : ga[0], gbw = (g8 & 1) ? gb[2] : gb[0], u0 = yy[0], u1 = yy[2];
      float y0 = __shfl(asf(u0), g8), y1 = __shfl(asf(u1), g8);
#pragma unroll
      for (int G = 1; G < 8; ++G) { y0 += __shfl(asf(u0), G * 8 + g8); y1 += __shfl(asf(u1), G * 8 + g8); }
      x1a = rbf(lo16(xw) + rbf(lo16(gaw) * rbf(y0))); x1b = rbf(hi16(xw) + rbf(hi16(gaw) * rbf(y1)));
      gb0 = lo16(gbw); gb1 = hi16(gbw);
      pin(x1a); pin(x1b); pin(gb0); pin(gb1);
    }
    bar();
    const int p0 = tid >> 3;
    v4u z0 = {0, 0, 0, 0}, z1 = {0, tag, 0, tag};
    {
      const int zo = OFF_Z + p0 * 5120 + pj;
      u64 m0 = lanes(true), m1 = lanes(p0 < 22);
#pragma nounroll
      for (int spin = 0; spin < SPIN_CAP; ++spin) {
        grow2(z0, z1, m0, m1, zo, zo + 32 * 5120, rw);
        const unsigned b0 = gbad(z0, tag), b1 = gbad(z1, tag);
        if (lanes((b0 | b1) != 0) == 0) break;
        m0 = lanes(b0 != 0); m1 = lanes(b1 != 0);
      }
    }
    const unsigned u0 = z0[0], u1 = z0[2], u2 = z1[0], u3 = z1[2];
    float sz = tr32(asf(u0) + asf(u2), asf(u1) + asf(u3));
    sz = tr16(sz, sz);
    sz += dpp<XOR8>(sz);
    float* red = (float*)(smem + L_RED);
    if ((lane & 24) == 0) red[((lane >> 5) * 8 + g8) * 4 + w] = sz;
    bar();
    if (w == 0 && lane < 8) {
      const f32x4 e = *(const f32x4*)(red + 4 * lane), o = *(const f32x4*)(red + 32 + 4 * lane);
      const float y0 = (e[0] + e[1]) + (e[2] + e[3]), y1 = (o[0] + o[1]) + (o[2] + o[3]);
      ((unsigned*)out)[8 * fi + lane] = pack2(x1a + rbf(gb0 * rbf(y0)), x1b + rbf(gb1 * rbf(y1)));
      if (fi == 0 && lane == 0) __builtin_amdgcn_raw_buffer_store_b32(tag, rw, 0, 0, AUX_DEV);
    }
  }
  asm volatile(".Lmega_end:");
}

extern "C" void kernel_launch(void* const* d_in, const int* in_sizes, int n_in, void* d_out, int out_size, void* d_ws, size_t ws_size,
                              hipStream_t stream) {
  hipFuncSetAttribute((const void*)mega, hipFuncAttributeMaxDynamicSharedMemorySize, LDS_BYTES);
  mega<<<256, 256, LDS_BYTES, stream>>>((const u16*)d_in[0], (const u16*)d_in[1], (const u16*)d_in[2], (const u16*)d_in[3], (const u16*)d_in[4],
                                        (const u16*)d_in[6], (const u16*)d_in[7], (const u16*)d_in[8], (const u16*)d_in[9], (const u16*)d_in[10],
                                        (u16*)d_out, (unsigned*)d_ws);
}
```
